```python
import torch

def _softmax(x, axis=-1):
    return torch.softmax(x, dim=axis)

def _gelu(x):
    return torch.nn.functional.gelu(x, approximate='tanh')

def _silu(x):
    return torch.nn.functional.silu(x)

def _sigmoid(x):
    return torch.sigmoid(x)

def _layernorm(x, axis=-1, eps=1e-05):
    m = x.mean(dim=axis, keepdim=True)
    v = x.var(dim=axis, keepdim=True, unbiased=False)
    return (x - m) / torch.sqrt(v + eps)

def _scatter_add(tgt, idx, src, axis=0):
    return tgt.clone().index_add_(axis, idx, src)

def _segment_sum(src, seg, n):
    out = torch.zeros((n,) + src.shape[1:], dtype=src.dtype)
    return out.index_add_(0, seg, src)

def _pad(x, pw):
    flat = tuple((v for pair in reversed(pw) for v in pair))
    return torch.nn.functional.pad(x, flat)
kernel_name = 'adaLN_cond__sliding_local_dilated__glu_gated_swiglu__prompt'

def setup_inputs():

    _w = lambda a: (lambda t: t.to(torch.bfloat16) if t.is_floating_point() else t)(torch.as_tensor(a))

    def _A_setup():
        B, S, D = (1, 2048, 1024)
        return {'x': _w((lambda *s: torch.randn(*s, dtype=torch.bfloat16) * 0.05)(B, S, D)), 'wqkv': _w((lambda *s: torch.randn(*s, dtype=torch.bfloat16) * 0.05)(D, 3 * D)), 'wo': _w((lambda *s: torch.randn(*s, dtype=torch.bfloat16) * 0.05)(D, D))}
    _A_in = _A_setup()

    def _B_setup():
        B, S, D, F = (1, 2048, 1024, 3584)
        return {'x': _w((lambda *s: torch.randn(*s, dtype=torch.bfloat16) * 0.05)(B, S, D)), 'w1': _w((lambda *s: torch.randn(*s, dtype=torch.bfloat16) * 0.8)(D, F) * (2.0 / D) ** 0.5), 'w3': _w((lambda *s: torch.randn(*s, dtype=torch.bfloat16) * 0.8)(D, F) * (2.0 / D) ** 0.5), 'w2': _w((lambda *s: torch.randn(*s, dtype=torch.bfloat16) * 0.8)(F, D) * (2.0 / F) ** 0.5)}
    _B_in = _B_setup()
    return {'A_x': _A_in['x'], 'A_wqkv': _A_in['wqkv'], 'A_wo': _A_in['wo'], 'B_x': _B_in['x'], 'B_w1': _B_in['w1'], 'B_w3': _B_in['w3'], 'B_w2': _B_in['w2'], 'c': _w((lambda *s: torch.randn(*s, dtype=torch.bfloat16) * 0.1)(1, 1024) * 8.0), 'w_mod': _w((lambda *s: torch.randn(*s, dtype=torch.bfloat16) * 0.1)(1024, 6 * 1024) * 1024 ** (-0.5) * 8.0)}

def reference(A_x, A_wqkv, A_wo, B_x, B_w1, B_w3, B_w2, c, w_mod):

    def _A_ref(x, wqkv, wo):
        B, S, D = x.shape
        H = 32
        Dh = D // H
        W, dil = (8, 2)
        qkv = (lambda x, *d: x.permute(*d))((x @ wqkv).reshape(B, S, 3, H, Dh), 2, 0, 3, 1, 4)
        q, k, v = (qkv[0], qkv[1], qkv[2])
        scale = Dh ** (-0.5)
        pos = torch.arange(S)
        off = torch.arange(W) * dil
        idx = pos[:, None] - off[None, :]
        mask = idx >= 0
        idx_c = torch.where(mask, idx, 0).reshape(-1)
        kg = k[:, :, idx_c, :].reshape(B, H, S, W, Dh)
        vg = v[:, :, idx_c, :].reshape(B, H, S, W, Dh)
        logits = torch.einsum('bhsd,bhswd->bhsw', q, kg) * scale
        logits = torch.where(mask[None, None, :, :], logits, -1000000000.0)
        attn = _softmax(logits, axis=-1)
        o = torch.einsum('bhsw,bhswd->bhsd', attn, vg)
        return (lambda x, *d: x.permute(*d))(o, 0, 2, 1, 3).reshape(B, S, D) @ wo

    def _B_ref(x, w1, w3, w2):
        gate = torch.einsum('bsd,df->bsf', x, w1)
        up = torch.einsum('bsd,df->bsf', x, w3)
        h = _silu(gate) * up
        out = torch.einsum('bsf,fd->bsd', h, w2)
        return out
    x = A_x
    mod = (_silu(c) @ w_mod).reshape(c.shape[0], 6, 1, -1)
    sa, ca, ga, sb, cb, gb = (mod[:, 0].clone(), mod[:, 1].clone(), mod[:, 2].clone(), mod[:, 3].clone(), mod[:, 4].clone(), mod[:, 5].clone())
    h = _layernorm(x, axis=-1) * (1 + ca) + sa
    x = x + ga * _A_ref(h, A_wqkv, A_wo)
    h = _layernorm(x, axis=-1) * (1 + cb) + sb
    x = x + gb * _B_ref(h, B_w1, B_w3, B_w2)
    return x
```

```cpp
#include <hip/hip_runtime.h>
#include <hip/hip_bf16.h>
#include <stdint.h>
#include <utility>

typedef unsigned short u16;
typedef short bf16x8 __attribute__((ext_vector_type(8)));
typedef short bf16x4 __attribute__((ext_vector_type(4)));
typedef float f32x4 __attribute__((ext_vector_type(4)));
typedef float f32x2 __attribute__((ext_vector_type(2)));
typedef unsigned int u32x4 __attribute__((ext_vector_type(4)));
typedef unsigned int u32x2 __attribute__((ext_vector_type(2)));
typedef __amdgpu_buffer_rsrc_t rsrc_t;
#define LDS_PTR(T) __attribute__((address_space(3))) T*
#define DI __device__ __forceinline__
#define GAP() __builtin_amdgcn_sched_barrier(0)
template <int... Is> using iseq = std::integer_sequence<int, Is...>;
template <int N> using make_iseq = std::make_integer_sequence<int, N>;

constexpr int S_ = 2048, D_ = 1024, F_ = 3584;
constexpr int KBS = (S_ / 16) * 1024;

DI float bf2f(u16 v) { return __uint_as_float(((unsigned)v) << 16); }
typedef __bf16 bf16x2_t __attribute__((ext_vector_type(2)));
DI unsigned pk2(float lo, float hi) { const f32x2 v = {lo, hi}; return __builtin_bit_cast(unsigned, __builtin_convertvector(v, bf16x2_t)); }
DI float rbf(float v) { return __uint_as_float(pk2(v, 0.f) << 16); }
DI float lo2f(unsigned u) { return __uint_as_float(u << 16); }
DI float hi2f(unsigned u) { return __uint_as_float(u & 0xffff0000u); }
template <int N> DI void wait_vm() { asm volatile("s_waitcnt vmcnt(%0)" ::"n"(N) : "memory"); }
DI void drain_vm() { wait_vm<0>(); __builtin_amdgcn_s_waitcnt(0x0F70); asm volatile("" ::: "memory"); }
template <int N> DI void wait_lgkm() { __builtin_amdgcn_s_waitcnt(0xC07F | (N << 8)); asm volatile("" ::: "memory"); }
DI void bar() { asm volatile("" ::: "memory"); __builtin_amdgcn_s_barrier(); asm volatile("" ::: "memory"); }
template <int IMM> DI void m0_add(unsigned base) { asm volatile("s_add_u32 m0, %0, %1" ::"s"(base), "n"(IMM) : "scc", "memory"); }
template <int N> DI void nops() { asm volatile("s_nop %0" ::"n"(N - 1)); }
template <class T> DI void pin(T& v) { asm volatile("" : "+v"(v)); }
template <class T> DI void pins(T& v) { asm volatile("" : "+s"(v)); }
DI rsrc_t mk_rsrc(const void* p, unsigned bytes) { return __builtin_amdgcn_make_buffer_rsrc((void*)p, 0, bytes, 0x00020000); }
template <int OFF> DI void bdma(unsigned voff, const rsrc_t& rs, unsigned soff) {
  asm volatile("buffer_load_dwordx4 %0, %1, %2 offen offset:%3 lds" ::"v"(voff), "s"(rs), "s"(soff), "n"(OFF) : "memory");
}
DI void touch_line(const void* p, unsigned lds_scratch) {
  asm volatile("s_mov_b32 m0, %1\n\ts_nop 0\n\tglobal_load_lds_dword %0, off" ::"v"(p), "s"(lds_scratch) : "memory");
}
#define ACLOB "a0","a1","a2","a3","a4","a5","a6","a7","a8","a9","a10","a11","a12","a13","a14","a15","a16","a17","a18","a19","a20","a21","a22","a23","a24","a25","a26","a27","a28","a29","a30","a31","a32","a33","a34","a35","a36","a37","a38","a39","a40","a41","a42","a43","a44","a45","a46","a47","a48","a49","a50","a51","a52","a53","a54","a55","a56","a57","a58","a59","a60","a61","a62","a63","a64","a65","a66","a67","a68","a69","a70","a71","a72","a73","a74","a75","a76","a77","a78","a79","a80","a81","a82","a83","a84","a85","a86","a87","a88","a89","a90","a91","a92","a93","a94","a95","a96","a97","a98","a99","a100","a101","a102","a103","a104","a105","a106","a107","a108","a109","a110","a111","a112","a113","a114","a115","a116","a117","a118","a119","a120","a121","a122","a123","a124","a125","a126","a127","a128","a129","a130","a131","a132","a133","a134","a135","a136","a137","a138","a139","a140","a141","a142","a143","a144","a145","a146","a147","a148","a149","a150","a151","a152","a153","a154","a155","a156","a157","a158","a159","a160","a161","a162","a163","a164","a165","a166","a167","a168","a169","a170","a171","a172","a173","a174","a175","a176","a177","a178","a179","a180","a181","a182","a183","a184","a185","a186","a187","a188","a189","a190","a191","a192","a193","a194","a195","a196","a197","a198","a199","a200","a201","a202","a203","a204","a205","a206","a207","a208","a209","a210","a211","a212","a213","a214","a215","a216","a217","a218","a219","a220","a221","a222","a223","a224","a225","a226","a227","a228","a229","a230","a231","a232","a233","a234","a235","a236","a237","a238","a239","a240","a241","a242","a243","a244","a245","a246","a247","a248","a249","a250","a251","a252","a253","a254","a255"
template <int R> DI void ld_act(unsigned voff, const rsrc_t& rs, unsigned soff) {
  asm volatile("buffer_load_dwordx4 a[%c3:%c4], %0, %1, %2 offen offset:%5 sc1" ::"v"(voff), "s"(rs), "s"(soff), "i"(4 * R), "i"(4 * R + 3), "n"((R & 1) * 1024) : "memory");
}
template <int R, bool SW, bool FIRST> DI void mfa(f32x4& c, bf16x8 w) {
  if constexpr (FIRST && SW) asm volatile("v_mfma_f32_16x16x32_bf16 %0, a[%c2:%c3], %1, 0" : "=&v"(c) : "v"(w), "i"(4 * R), "i"(4 * R + 3));
  else if constexpr (FIRST) asm volatile("v_mfma_f32_16x16x32_bf16 %0, %1, a[%c2:%c3], 0" : "=&v"(c) : "v"(w), "i"(4 * R), "i"(4 * R + 3));
  else if constexpr (SW) asm volatile("v_mfma_f32_16x16x32_bf16 %0, a[%c2:%c3], %1, %0" : "+v"(c) : "v"(w), "i"(4 * R), "i"(4 * R + 3));
  else asm volatile("v_mfma_f32_16x16x32_bf16 %0, %1, a[%c2:%c3], %0" : "+v"(c) : "v"(w), "i"(4 * R), "i"(4 * R + 3));
}
DI void mfv(f32x4& d, const u32x4& a, const u32x4& b) { asm volatile("v_mfma_f32_16x16x32_bf16 %0, %1, %2, 0" : "=&v"(d) : "v"(a), "v"(b)); }
DI void mfma_fence() { GAP(); asm volatile("s_nop 7\n\ts_nop 1" ::: "memory"); GAP(); }
DI bf16x4 ldsT(unsigned addr) { return __builtin_amdgcn_ds_read_tr16_b64_v4i16((LDS_PTR(bf16x4))(uintptr_t)addr); }
DI bf16x8 cat(bf16x4 lo, bf16x4 hi) { return __builtin_shufflevector(lo, hi, 0, 1, 2, 3, 4, 5, 6, 7); }
DI u32x4 pack8(const f32x4& a, const f32x4& b) { return (u32x4){pk2(a[0], a[1]), pk2(a[2], a[3]), pk2(b[0], b[1]), pk2(b[2], b[3])}; }
DI void v_mul(float& d, float a, float b) { asm volatile("v_mul_f32 %0, %1, %2" : "=v"(d) : "v"(a), "v"(b)); }
DI void v_mulc(float& d, float a) { asm volatile("v_mul_f32 %0, 0xbfb8aa3b, %1" : "=v"(d) : "v"(a)); }
DI void v_exp(float& d, float a) { asm volatile("v_exp_f32 %0, %1" : "=v"(d) : "v"(a)); }
DI void v_fmac(float& d) { asm volatile("v_fma_f32 %0, %0, %1, %1" : "+v"(d) : "s"(-1.4426950408889634f)); }
DI void v_rcp(float& d) { asm volatile("v_rcp_f32 %0, %0" : "+v"(d)); }
DI unsigned v_cvt(float a, float b) { unsigned d; asm volatile("v_cvt_pk_bf16_f32 %0, %1, %2" : "=v"(d) : "v"(a), "v"(b)); return d; }

DI float fmax1(float a, float b) { return __builtin_amdgcn_fmed3f(a, b, __builtin_inff()); }
DI float xadd16(float v) { auto r = __builtin_amdgcn_permlane16_swap(__float_as_uint(v), __float_as_uint(v), false, false); return __uint_as_float(r[0]) + __uint_as_float(r[1]); }
DI float xadd32(float v) { auto r = __builtin_amdgcn_permlane32_swap(__float_as_uint(v), __float_as_uint(v), false, false); return __uint_as_float(r[0]) + __uint_as_float(r[1]); }
DI float add2(float u, float v) {
  auto r = __builtin_amdgcn_permlane16_swap(__float_as_uint(u), __float_as_uint(v), false, false);
  return xadd32(__uint_as_float(r[0]) + __uint_as_float(r[1]));
}
DI f32x2 spread2(float t) {
  auto r = __builtin_amdgcn_permlane16_swap(__float_as_uint(t), __float_as_uint(t), false, false);
  return (f32x2){__uint_as_float(r[0]), __uint_as_float(r[1])};
}
template <int CTRL> DI float dpp_add(float v) { return v + __uint_as_float(__builtin_amdgcn_update_dpp(0, __float_as_uint(v), CTRL, 0xf, 0xf, true)); }
DI f32x2 red8(const f32x2 (&a)[4]) {
  f32x2 o;
#pragma unroll
  for (int e = 0; e < 2; ++e) {
    auto r0 = __builtin_amdgcn_permlane16_swap(__float_as_uint(a[0][e]), __float_as_uint(a[1][e]), false, false);
    auto r1 = __builtin_amdgcn_permlane16_swap(__float_as_uint(a[2][e]), __float_as_uint(a[3][e]), false, false);
    const float u = __uint_as_float(r0[0]) + __uint_as_float(r0[1]), v = __uint_as_float(r1[0]) + __uint_as_float(r1[1]);
    auto r2 = __builtin_amdgcn_permlane32_swap(__float_as_uint(u), __float_as_uint(v), false, false);
    o[e] = dpp_add<0x128>(__uint_as_float(r2[0]) + __uint_as_float(r2[1]));
  }
  return o;
}
DI float row16_sum(float v) {
  v = dpp_add<0xB1>(v);
  v = dpp_add<0x4E>(v);
  v = dpp_add<0x141>(v);
  v = dpp_add<0x140>(v);
  return v;
}
DI f32x2 unpk(unsigned u) { return (f32x2){lo2f(u), hi2f(u)}; }
DI f32x2 rbf2(f32x2 v) { return unpk(pk2(v[0], v[1])); }
DI float ln_rstd(float var) { return __builtin_amdgcn_rcpf(rbf(__builtin_amdgcn_sqrtf(rbf(rbf(var) + 1e-5f)))); }
DI float silu_bf(u16 c) { const float x = bf2f(c); return rbf(x * __builtin_amdgcn_rcpf(1.f + __builtin_amdgcn_exp2f(x * -1.4426950408889634f))); }
DI unsigned umax4(const u32x4& v) { return max(max(v[0], v[1]), max(v[2], v[3])); }
DI unsigned umax16(const u32x4 (&v)[4]) { return max(max(umax4(v[0]), umax4(v[1])), max(umax4(v[2]), umax4(v[3]))); }
template <class T> DI T lds_ld(unsigned a) { return *(const LDS_PTR(T))(uintptr_t)a; }
template <class T> DI void lds_st(unsigned a, const T& v) { *(LDS_PTR(T))(uintptr_t)a = v; }
DI u32x4 swz_rows(const u32x4& o) {
  auto r0 = __builtin_amdgcn_permlane16_swap(o[0], o[2], false, false);
  auto r1 = __builtin_amdgcn_permlane16_swap(o[1], o[3], false, false);
  return (u32x4){r0[0], r1[0], r0[1], r1[1]};
}

constexpr unsigned W_H1 = 1u << 16, W_O = W_H1 + (4u << 20), W_X1 = W_O + (4u << 20), W_H2 = W_X1 + (4u << 20), W_HH = W_H2 + (4u << 20);
constexpr unsigned W_PART = 32u << 20, W_FLAGS = W_PART + (128u << 10), W_STATS = W_FLAGS + (128u << 10), W_HALO = W_STATS + (256u << 10);
constexpr unsigned W_XCH = 34u << 20, W_N1 = 42u << 20, W_END = 46u << 20;
enum { PH_LN1 = 0, PH_G1, PH_G2, PH_G3, PH_G4 };
constexpr unsigned POISON = 0xffffffffu;
constexpr int AUX_SC1 = 16;
constexpr unsigned SPIN_CAP = 1u << 17;
constexpr int SLOT = 16384, L_KV = 8 * SLOT, L_WV = L_KV + 20480, L_RED = L_WV + 4096, L_TCH = L_RED + 1024, LDS_TOTAL = 163840;

#define STAMP()
#define STAMP0()
DI int opaque_tid() { int t = threadIdx.x; asm volatile("" : "+v"(t)); return t; }
DI int opaque_bid() { int b = blockIdx.x; asm volatile("" : "+s"(b)); return b; }

__global__ __launch_bounds__(256) void pre_kernel(const u16* __restrict__ c, const u16* __restrict__ wmod, char* __restrict__ ws,
                                                  const u16* __restrict__ x, const u16* __restrict__ wqkv, const u16* __restrict__ wo,
                                                  const u16* __restrict__ w1, const u16* __restrict__ w3) {
  __shared__ __attribute__((aligned(16))) char T[32 * 528];
  const int t = threadIdx.x;
#define K1END()
  auto side = [&]() {
    const int gl = blockIdx.x * 256 + t;
    if (gl < 81920) *(u32x4*)(ws + W_STATS + gl * 16) = (u32x4){POISON, POISON, POISON, POISON};
    else if (gl < 86016) *(u32x4*)(ws + W_PART + ((gl >> 10) & 3) * 24576 + 8192 + (gl & 1023) * 16) = (u32x4){POISON, POISON, POISON, POISON};
    else if (gl < 88064) *(u32x4*)(ws + W_FLAGS + ((gl - 86016) >> 1) * 128 + (gl & 1) * 16) = (u32x4){0u, 0u, 0u, 0u};
    const int bb = blockIdx.x;
    const unsigned wl = ((gl >> 4) & 1023) * (F_ * 2) + (gl & 15) * 128;
    const uintptr_t a = bb < 192 ? (uintptr_t)wqkv + gl * 128 : bb < 256 ? (uintptr_t)wo + (gl - 49152) * 128 : bb < 320 ? (uintptr_t)w1 + wl : (uintptr_t)w3 + wl;
    unsigned r = *(const __attribute__((address_space(1))) unsigned*)a;
    if (bb >= 344 && bb < 348) {
      unsigned long long pc; int d; unsigned n;
      asm volatile("s_getpc_b64 %0\n1:\n\ts_mov_b32 %1, .Lblk_begin-1b\n\ts_mov_b32 %2, .Lblk_end-.Lblk_begin" : "=s"(pc), "=s"(d), "=s"(n));
      r += __builtin_amdgcn_raw_buffer_load_b32(mk_rsrc((const void*)(pc + d), n), (gl - 344 * 256) * 128, 0, 0);
    }
    return r;
  };
  if (blockIdx.x >= 128) {
    const int rb = blockIdx.x - 128, r = t >> 5, l = t & 31;
    u32x4 v[4];
#pragma unroll
    for (int i = 0; i < 4; ++i) v[i] = *(const u32x4*)(x + (size_t)(rb * 8 + r) * D_ + (l + 32 * i) * 8);
    const unsigned pf = side();
    f32x2 f[16], s2 = {0.f, 0.f};
#pragma unroll
    for (int i = 0; i < 16; ++i) { f[i] = unpk(v[i >> 2][i & 3]); s2 += f[i]; }
    const float mean = xadd16(row16_sum(s2[0] + s2[1])) * (1.f / 1024.f);
    f32x2 q2 = {0.f, 0.f};
#pragma unroll
    for (int i = 0; i < 16; ++i) { const f32x2 d = f[i] - mean; q2 += d * d; }
    const float rstd = ln_rstd(xadd16(row16_sum(q2[0] + q2[1])) * (1.f / 1024.f)), mb = rbf(mean);
#pragma unroll
    for (int i = 0; i < 4; ++i) {
      const int cch = l + 32 * i, kb = cch >> 2, j = cch & 3;
      char* p = T + kb * 528 + (j & 1) * 256 + r * 16 + (j >> 1) * 8;
#pragma unroll
      for (int hf = 0; hf < 2; ++hf) {
        const f32x2 n0 = rbf2(f[4 * i + 2 * hf] - mb) * rstd, n1 = rbf2(f[4 * i + 2 * hf + 1] - mb) * rstd;
        *(u32x2*)(p + 128 * hf) = (u32x2){pk2(n0[0], n0[1]), pk2(n1[0], n1[1])};
      }
    }
    __syncthreads();
    char* hb = ws + W_N1 + (rb >> 1) * 1024 + (rb & 1) * 128;
#pragma unroll
    for (int i = 0; i < 4; ++i) {
      const int idx = t + 256 * i, kb = idx >> 5, g = (idx >> 3) & 3, fr = idx & 7;
      *(u32x4*)(hb + (size_t)kb * KBS + g * 256 + fr * 16) = *(const u32x4*)(T + kb * 528 + g * 128 + fr * 16);
    }
    asm volatile("" ::"v"(pf));
    K1END();
    return;
  }
  float (*red)[64] = (float (*)[64])T;
  const int tc = t & 7, tr = t >> 3, cg = blockIdx.x >> 2, kq = blockIdx.x & 3;
  const int n0 = cg * 64 + tc * 8, k0 = kq * 256 + tr;
  u32x4 w[8]; u16 cv[8];
#pragma unroll
  for (int i = 0; i < 8; ++i) w[i] = __builtin_nontemporal_load((const u32x4*)(wmod + (size_t)(k0 + 32 * i) * 6144 + n0));
#pragma unroll
  for (int i = 0; i < 8; ++i) cv[i] = c[k0 + 32 * i];
  const unsigned pf = side();
  f32x2 acc[4];
#pragma unroll
  for (int j = 0; j < 4; ++j) acc[j] = (f32x2){0.f, 0.f};
#pragma unroll
  for (int i = 0; i < 8; ++i) {
    const float sv = silu_bf(cv[i]);
#pragma unroll
    for (int j = 0; j < 4; ++j) acc[j] += unpk(w[i][j]) * sv;
  }
  *(f32x2*)&red[t >> 6][tc * 8 + ((t >> 4) & 3) * 2] = red8(acc);
  __syncthreads();
  if (t < 64) ((float*)(ws + W_PART))[kq * 6144 + cg * 64 + t] = (red[0][t] + red[1][t]) + (red[2][t] + red[3][t]);
  asm volatile("" ::"v"(pf));
  K1END();
}

DI void map_g1(int b, int& tm, int& tn) { const int xcd = b & 7, idx = b >> 3; tm = (xcd & 1) * 8 + (idx & 7); tn = (xcd >> 1) * 4 + (idx >> 3); }
DI void map_g2(int b, int& tm, int& tn) { const int xcd = b & 7, idx = b >> 3; tm = (xcd & 3) * 4 + (idx >> 3); tn = (xcd >> 2) * 8 + (idx & 7); }
DI void map_g3(int b, int& tm, int& tn) { const int xcd = b & 7, idx = b >> 3; tm = (xcd >> 2) * 8 + (idx >> 2); tn = (xcd & 3) * 4 + (idx & 3); }

struct Ctx {
  const u16 *x, *wqkv, *wo, *w1, *w3, *w2, *cc, *wmod; u16* out; char* ws;
  rsrc_t rs, rB, rN, rG, rC;
  f32x4 acc[4][2][2];
  f32x4 pv[2][2][2], sg[2][2][2];
  bf16x4 wl[8][2], wh[8][2];
  float t[8]; u32x4 o;
  unsigned bW[2][2], bM[2][2], bX[2][2], vA, vB[4], vN[4], vG[4];
  const u16* pT[2];
  unsigned lds0, sT, oT[3], sB, sA, mB, so, dG, sB4[2][2], sA4[2], fl3, fl4, xs4;
  int wave, it;
  u32x4 kf[2][2], vf[2][2], hl[4]; unsigned kvw, hso;
  u32x4 xr[2][2], lw[8], lc;
  u32x4 mp[4]; unsigned mo; f32x2 eg[2][4], er[2][2][4];
  unsigned pollv, pollo, flv, tch, vT, vC; u32x4 xl[8];
};
DI void st16(const Ctx& c, unsigned off, const u32x4& v) { __builtin_amdgcn_raw_buffer_store_b128(v, c.rs, off, 0, AUX_SC1); }
DI void ld16x4(const Ctx& c, u32x4 (&v)[4], unsigned off, unsigned step) {
  asm volatile("buffer_load_dwordx4 %0, %4, %5, 0 offen sc1\n\tbuffer_load_dwordx4 %1, %4, %5, %6 offen sc1\n\tbuffer_load_dwordx4 %2, %4, %5, %7 offen sc1\n\tbuffer_load_dwordx4 %3, %4, %5, %8 offen sc1\n\ts_waitcnt vmcnt(0)"
               : "=&v"(v[0]), "=&v"(v[1]), "=&v"(v[2]), "=&v"(v[3]) : "v"(off), "s"(c.rs), "s"(step), "s"(2 * step), "s"(3 * step) : "memory");
}
DI void retry(const Ctx& c, u32x4 (&v)[4], unsigned off, unsigned step) {
  if (__builtin_expect(__any(umax16(v) == POISON), 0)) {
#pragma unroll 1
    for (unsigned spins = 0; spins < SPIN_CAP; ++spins) { ld16x4(c, v, off, step); if (!__any(umax16(v) == POISON)) break; __builtin_amdgcn_s_sleep(1); }
  }
}
DI unsigned flag_off(int tm, int wave, int col, int ph) { return W_FLAGS + ((tm * 4 + wave) * 16 + col) * 128 + ph * 4; }
DI void set_flag(const Ctx& c, unsigned off, unsigned val) {
  asm volatile("s_mov_b64 exec, 1\n\tbuffer_store_dword %0, off, %1, %2 sc1\n\ts_mov_b64 exec, -1" ::"v"(val), "s"(c.rs), "s"(off) : "memory");
}
DI void spin(const Ctx& c, unsigned voff, unsigned need) {
#pragma unroll 1
  for (unsigned spins = 0; spins < SPIN_CAP; ++spins) {
    if (__all(__builtin_amdgcn_raw_buffer_load_b32(c.rs, voff, 0, AUX_SC1) >= need)) break;
    __builtin_amdgcn_s_sleep(1);
    asm volatile("" ::: "memory");
  }
  asm volatile("" ::: "memory");
}

#define L4 6
namespace en {
constexpr int RD = 4, PRE = 8, NHM = 672, H4 = 448, NB4 = 14 - L4;
constexpr int seg_nh(int s) { return s == 1 ? 192 : s == 2 ? 64 : NHM; }
constexpr int seg_nt(int s) { return s == 1 ? 32 : s == 2 ? 16 : 84; }
constexpr int g4j(int s) { return s < 2 * NB4 ? s / 2 : s < 2 * NB4 + L4 ? s - NB4 : s - NB4 - L4; }
constexpr int g4h(int s) { return s < 2 * NB4 ? s & 1 : s >= 2 * NB4 + L4; }
constexpr int g4first(int j) { return j < NB4 ? 2 * j : j + NB4; }
constexpr int g4last(int j) { return j < NB4 ? 2 * j + 1 : j + NB4 + L4; }
constexpr int g4kb(int j) { return j < 12 ? 32 * (j / 4) + 4 * (j % 4) : 96 + 4 * (j - 12); }
constexpr bool isbar(int s, int H) { return (H & 7) == 8 - RD && (s != 3 || H < NHM - 8); }
enum { K_A = 1, K_D, K_A4, K_P, K_XS, K_XL, K_M, K_T, K_C };
enum { A_FLAG = 1, A_CHK2, A_CHK3, A_CHK4, A_FLAG4, A_CHKX, A_MOD };
constexpr int reg0(int s, int H) { return s == 3 && H >= H4 ? H4 : H & ~63; }
constexpr bool hh_store(int s, int H, int f) { return s == 3 && f >= 0 && (H >= H4 ? f >= 80 && f < 112 && (f - 80) % 16 == 13 : H >= 128 && !((H >> 6) & 1) && f >= 32 && f < 96 && f % 16 == 13); }
#define HP1 4
#define HP2 12
#define XD1 14
#define XD2 4
#define XD3 10
struct Plan { short op[NHM + 1], f[4 * NHM + 4], act[NHM + 1], vw[NHM + 1], dend, slack; };
constexpr Plan mk(int s) {
  Plan p{};
  const int NH = seg_nh(s), NT = seg_nt(s), OF = 2 * PRE;
  int atA[64] = {}, atD[4 * 84] = {}, atA4[112] = {}, req[NHM + 1] = {}, nvm[NHM + 2 * PRE + 1] = {}, cum[NHM + 2 * PRE + 2] = {};
  for (int H = 0; H <= NH; ++H) req[H] = -1000;
  for (int n = 0; n < OF; ++n) { atA[n] = n - OF; nvm[n] = 1; }
  int na = OF, nd = 32, n4 = 0, nxs = 0, nxl = 0, lim = 7, hchk = 100000;
  const int chk[4] = {386, 386, H4 + 8 * HP1 + 16, H4 + 8 * HP2 + 16}, hx0 = H4 + 8 * (2 * NB4 + L4) + 2;
  if (s == 3) {
    p.op[244] = p.op[372] = K_P * 1024; p.op[chk[2] - 16] = p.op[chk[3] - 16] = K_P * 1024;
    p.act[chk[0]] = A_CHK2; p.act[chk[2]] = A_CHK3; p.act[chk[3]] = A_CHK4;
    req[chk[0]] = 372; req[chk[2]] = chk[2] - 16; req[chk[3]] = chk[3] - 16;
  }
  if (s == 2) { p.act[56] = A_MOD; }
  if (s != 2) { p.op[98] = K_T * 1024 + s - 1; p.op[122] = K_T * 1024 + s; }
  if (s == 3) for (int i = 0; i < 4; ++i) p.op[146 + 32 * i] = p.op[274 + 32 * i] = K_T * 1024 + 4 + i;
  if (s == 1) p.op[130] = K_C * 1024;
  if (s == 3) { p.op[48] = K_C * 1024; p.op[226] = p.op[354] = K_C * 1024 + 1; }
  for (int H = 0; H < NH; ++H) {
    if (isbar(s, H)) lim = (H >> 3) + 7;
    if (p.op[H]) continue;
    if (s == 2 && H >= 36 && H < 40) { p.op[H] = K_M * 1024 + H - 36; req[56] = H; continue; }
    if (na < 64) { atA[na] = H; p.op[H] = K_A * 1024 + na++; continue; }
    const bool dok = nd < 4 * NT && (nd >> 2) <= lim;
    if (s != 3 || H < 384) {
      if (dok && ((nd >> 2) < lim || (H & 1))) { atD[nd] = H; p.op[H] = K_D * 1024 + nd++; }
      continue;
    }
    if (H >= hx0 && nxs < 8) {
      p.op[H] = K_XS * 1024 + nxs++;
      if (nxs == 8) {
        const int hf = (H + XD1 + 1) & ~1; hchk = (hf + XD2 + XD3 + 1) & ~1;
        p.act[hf] = A_FLAG4; req[hf] = H; p.op[hf + XD2] = K_P * 1024 + 1; p.act[hchk] = A_CHKX; req[hchk] = hf + XD2;
      }
      continue;
    }
    if (H >= hchk && nxl < 8) { p.op[H] = K_XL * 1024 + nxl++; continue; }
    bool aok = false;
    if (n4 < 112) {
      const int j = n4 >> 3, i = (n4 >> 1) & 3;
      aok = H >= (j < 8 ? 384 + 8 * j : H4 + 8 * g4last(j - 8)) + 2 * i + 2 && H >= chk[j >> 2];
    }
    const int dd = 8 * ((nd >> 2) - 1) + 4, da = H4 + 8 * g4first(n4 >> 3) - 4;
    if (aok && (!dok || da <= dd)) { atA4[n4] = H; p.op[H] = K_A4 * 1024 + n4++; }
    else if (dok) { atD[nd] = H; p.op[H] = K_D * 1024 + nd++; }
  }
  p.dend = nd;
  for (int H = 0; H < NH; ++H) {
    if (H < 64 && !(H & 1) && atA[H + 1] > req[H]) req[H] = atA[H + 1];
    const int T = (H >> 3) + 1;
    if (isbar(s, H) && T >= 8 && T < NH / 8 && atD[4 * T + 3] > req[H]) req[H] = atD[4 * T + 3];
  }
  if (s == 3) for (int j = 0; j < 14; ++j) { const int H = H4 + 8 * g4first(j) - 4; if (atA4[8 * j + 7] > req[H]) req[H] = atA4[8 * j + 7]; }
  int f = 0;
  for (int q = 0; q < 4 * NH; ++q) {
    const int H = q >> 2, M = q & 3, k = p.op[H] / 1024, i = p.op[H] % 1024;
    if (q == 4 * reg0(s, H)) f = 0;
    const bool prep = (k == K_D && !(i & 3)) || ((k == K_A || k == K_A4) && !(i & 1)) || k == K_T || k == K_C;
    const bool busy = (M == 2 && k) || (M == 1 && prep) || (M == 0 && k == K_D && !(i & 3)) || (M == 3 && (H & 1)) || q < 4 * reg0(s, H) + 12;
    p.f[q] = busy ? -1 : f++;
    if (M == 2 && k) nvm[H + OF]++;
    if (hh_store(s, H, p.f[q])) {
      nvm[H + OF]++;
      if (p.f[q] == (H >= H4 ? 109 : 93)) {
        int hb = H + (H < 384 ? 36 : 22); while (!isbar(s, hb)) ++hb;
        p.act[hb] = A_FLAG; if (H > req[hb]) req[hb] = H;
      }
    }
  }
  for (int H = 0; H <= NH + OF; ++H) cum[H + 1] = cum[H] + nvm[H];
  p.slack = 1000;
  for (int H = 0; H <= NH; ++H) {
    const int n = req[H] == -1000 ? -1 : cum[H + OF] - cum[req[H] + 1 + OF]; p.vw[H] = n > 63 ? 63 : n;
    if (H - req[H] < p.slack) p.slack = H - req[H];
  }
  if (s == 3 && (n4 < 112 || nxl < 8)) p.slack = -1;
  for (int u = 1; u < 7 && u < NH / 64; ++u) { int m = 0; for (int q = 256 * u; q < 256 * u + 256; ++q) if (p.f[q] > m) m = p.f[q]; if (m < (u == 6 ? 96 : 128)) p.slack = -2; }
  return p;
}
constexpr Plan P1 = mk(1), P2 = mk(2), P3 = mk(3);
constexpr const Plan& PL(int s) { return s == 1 ? P1 : s == 2 ? P2 : P3; }
constexpr bool same_iter() {
  for (int H = 128; H < 256; ++H) {
    const int a = P3.op[H], b = P3.op[H + 128];
    if (!(a == b || (a / 1024 == K_D && b == a + 64)) || P3.act[H + 1] != P3.act[H + 129]) return false;
    for (int m = 0; m < 4; ++m) if (P3.f[4 * H + m] != P3.f[4 * H + 512 + m]) return false;
  }
  return P3.dend == 4 * 84;
}
static_assert(P1.slack >= 8 && P2.slack >= 8 && P3.slack >= 8, "every load is issued well before it is needed");
static_assert(same_iter(), "the loop's two iterations must have the same program");
constexpr int F_HALO = 100, F_GEMV = 120;
constexpr int after_halo() { int n = 9; bool on = false; for (int H = 128; H < 192; ++H) { if (on && P1.op[H]) ++n; for (int m = 0; m < 4; ++m) if (P1.f[4 * H + m] == F_HALO) on = true; } return n; }
constexpr int vwait(int s, int H) {
  const int a = PL(s).vw[H];
  if (s == 3 && H > 128 && H <= 256) { const int b = P3.vw[H + 128]; return a < 0 ? b : b < 0 ? a : a < b ? a : b; }
  return a;
}

template <int S, int H, int R> DI void rd(Ctx& c) {
  constexpr bool mx = S == 3 && H >= 384 && H < H4;
  constexpr int cb = R >> 1, hi = R & 1, off = ((H >> 3) & 7) * SLOT + ((H >> 1) & 3) * 4096 + (H & 1) * (mx ? 1024 : 256) + hi * 2048;
  const unsigned ad = S == 3 && H >= 256 && H < 260 ? c.bX[cb][H & 1] + off - (H & 1) * 256 : (mx ? c.bM[cb][off >> 16] : c.bW[cb][off >> 16]) + (off & 65535);
  if constexpr (hi) c.wh[H & 7][cb] = ldsT(ad); else c.wl[H & 7][cb] = ldsT(ad);
}
template <int S, int T> DI void dma_off(Ctx& c) {
  constexpr int st = T & 7;
  if constexpr (S == 1 && T < 24) c.sB = st * 128 * 6144 + (T < 8 ? 2048 : T < 16 ? 4096 : 0);
  else if constexpr (S == 1) c.sB = c.sB4[0][0] + st * 128 * 2048;
  else if constexpr (S == 2) c.sB = st * 128 * F_ * 2;
  else if constexpr (T < 56) { if constexpr (st) c.sB += 128 * F_ * 2; else if constexpr (T & 8) c.sB -= 7 * 128 * F_ * 2; else c.sB += c.dG; }
  else c.sB = c.sB4[g4j(T - 56) >= 12][g4h(T - 56)] + g4kb(g4j(T - 56)) * 32 * 2048;
  pins(c.sB);
}
template <int S, int D> DI void dma(Ctx& c) {
  constexpr int T = D >> 2, i = D & 3;
  if constexpr ((S == 1 && T >= 24) || S == 2 || (S == 3 && T >= 56)) bdma<i * 1024>(c.vN[i], c.rN, c.sB);
  else if constexpr (S == 3 && !(T & 8)) bdma<i * 1024>(c.vG[i], (i & 1) ? c.rG : c.rB, c.sB);
  else if constexpr (S == 3) bdma<i * 1024>(c.vB[i], mk_rsrc(c.w3, D_ * F_ * 2), c.sB);
  else bdma<i * 1024>(c.vB[i], c.rB, c.sB);
}
template <int S, int D> DI void dma1(Ctx& c) {
  if constexpr (!(D & 3)) { dma_off<S, (D >> 2)>(c); m0_add<((D >> 2) & 7) * SLOT>(c.mB); nops<1>(); }
  dma<S, D>(c);
}
template <int S, int D0, int... Ds> DI void dmas(Ctx& c, iseq<Ds...>) { (dma1<S, D0 + Ds>(c), ...); }

template <int I, int S, int B> DI void e_gate(Ctx& c) {
  constexpr int e = I & 7, ph = I >> 3;
  if constexpr (ph == 0) { float r; v_mulc(r, c.pv[S][B][e >> 2][e & 3]); c.sg[S][B][e >> 2][e & 3] = r; }
  else if constexpr (ph == 1) v_exp(c.t[e], c.sg[S][B][e >> 2][e & 3]);
  else if constexpr (ph == 2) v_fmac(c.t[e]);
  else if constexpr (ph == 3) v_rcp(c.t[e]);
  else { float r; v_mul(r, c.sg[S][B][e >> 2][e & 3], c.t[e]); c.sg[S][B][e >> 2][e & 3] = r; }
}
template <int I, int S, int B, int G> DI void e_up(Ctx& c) {
  constexpr int e = I & 7;
  if constexpr (I < 8) v_mul(c.t[e], c.pv[S][B][e >> 2][e & 3], c.sg[G][B][e >> 2][e & 3]);
  else if constexpr (I < 12) c.o[e] = v_cvt(c.t[2 * e], c.t[2 * e + 1]);
  else if constexpr (I == 13) asm volatile("buffer_store_dwordx4 %0, %1, %2, %3 offen offset:%4 sc1" ::"v"(c.o), "v"(c.vA), "s"(c.rs), "s"(c.so), "n"(B * 1024) : "memory");
  else if constexpr (I == 14 && B == 1) { c.so += (S ? 31 : 1) * KBS; pins(c.so); }
}
template <int U, int F> DI void e_kv(Ctx& c) {
  u32x4 (&d)[2][2] = U ? c.vf : c.kf;
  if constexpr (F < 16) d[F >> 3][(F >> 2) & 1][F & 3] = v_cvt(c.pv[F >> 3][(F >> 2) & 1][(F >> 1) & 1][2 * (F & 1)], c.pv[F >> 3][(F >> 2) & 1][(F >> 1) & 1][2 * (F & 1) + 1]);
  else if constexpr (F == 18 || F == 22) lds_st(c.kvw + 4096 + U * 2048 + (F == 22) * 1024, d[F == 22][1]);
  else if constexpr (F == 17) {
    if (c.wave == 3) {
#pragma unroll
      for (int h = 0; h < 2; ++h) {
        const u32x4 v = d[h][1];
        st16(c, c.vA + c.hso + 65536 + U * 2048 + h * 1024, (u32x4){min(v[0], POISON - 1), min(v[1], POISON - 1), min(v[2], POISON - 1), min(v[3], POISON - 1)});
      }
    }
  } else if constexpr (U == 0 && F == 40) {
    const int tid = opaque_tid(), g = (tid >> 4) & 3; int tm, tn; map_g2(opaque_bid(), tm, tn);
    const char* rp = (const char*)c.x + ((tm * 128 + c.wave * 32 + (tid & 15)) * D_ + tn * 64 + (g & 1) * 16 + (g >> 1) * 8) * 2;
#pragma unroll
    for (int i = 0; i < 4; ++i) c.xr[i >> 1][i & 1] = *(const u32x4*)(rp + (i >> 1) * 16 * D_ * 2 + (i & 1) * 64);
  } else if constexpr (U == 1 && F == F_HALO) {
    asm volatile("s_cmp_eq_u32 %4, 0\n\ts_cbranch_scc1 1f\n\tds_read_b128 %0, %5\n\tds_read_b128 %1, %5 offset:1024\n\tds_read_b128 %2, %5 offset:2048\n\tds_read_b128 %3, %5 offset:3072\n\ts_branch 2f\n"
                 "1:\n\tbuffer_load_dwordx4 %0, %6, %7, %8 offen sc1\n\tbuffer_load_dwordx4 %1, %6, %7, %8 offen offset:1024 sc1\n\tbuffer_load_dwordx4 %2, %6, %7, %8 offen offset:2048 sc1\n\tbuffer_load_dwordx4 %3, %6, %7, %8 offen offset:3072 sc1\n2:"
                 : "=&v"(c.hl[0]), "=&v"(c.hl[1]), "=&v"(c.hl[2]), "=&v"(c.hl[3]) : "s"(c.wave), "v"(c.kvw), "v"(c.vA), "s"(c.rs), "s"(c.hso < W_HALO ? W_END : c.hso) : "scc", "memory");
  } else if constexpr (U == 1 && F == F_GEMV) {
    const int tid = opaque_tid(), b = opaque_bid(), k0 = (b & 3) * 256 + (tid >> 3) * 8;
    const rsrc_t rw = mk_rsrc(c.wmod, 1024 * 6144 * 2);
    const unsigned vo = (k0 * 6144 + 2048 + (b >> 2) * 64 + (tid & 7) * 8) * 2;
    asm volatile("global_load_dwordx4 %0, %1, %2" : "=v"(c.lc) : "v"(k0 * 2), "s"(c.cc) : "memory");
#pragma unroll
    for (int i = 0; i < 8; ++i) asm volatile("buffer_load_dwordx4 %0, %1, %2, %3 offen nt" : "=v"(c.lw[i]) : "v"(vo), "s"(rw), "s"(i * 6144 * 2) : "memory");
  }
}
DI void switch_mixed(Ctx& c);
template <int W> DI void warm(Ctx& c) {
  if constexpr (W < 4) asm volatile("buffer_load_dword %0, %1, %2, %3 offen" : "+v"(c.tch) : "v"(c.vT), "s"(mk_rsrc((W & 1) ? c.w3 : c.w1, D_ * F_ * 2)), "s"(c.sT * (F_ * 2) + (W / 2 + 1) * 2048) : "memory");
  else if constexpr (W < 6) asm volatile("buffer_load_dword %0, %1, %2, %3 offen" : "+v"(c.tch) : "v"(c.vT), "s"(mk_rsrc(c.pT[W - 4], D_ * F_ * 2)), "s"(c.oT[W - 4]) : "memory");
  else {
    asm volatile("buffer_load_dword %0, %1, %2, %3 offen" : "+v"(c.tch) : "v"(c.vT), "s"(c.rN), "s"(c.oT[2] + (W - 6) * (2 << 20)) : "memory");
    if constexpr (W == 7) { c.pT[0] = c.pT[1] = c.w2; c.oT[0] = c.oT[2] + (4 << 20); c.oT[1] = c.oT[2] + (6 << 20); c.oT[2] += 8 << 20; }
  }
}
template <int W> DI void warm_code(Ctx& c) {
  unsigned o;
  if constexpr (W == 0) asm volatile("s_mov_b32 %0, .-.Lblk_begin" : "=s"(o)); else asm volatile("s_mov_b32 %0, .Lblk_tail-.Lblk_begin" : "=s"(o));
  asm volatile("buffer_load_dword %0, %1, %2, %3 offen" : "+v"(c.tch) : "v"(c.vC), "s"(c.rC), "s"(o) : "memory");
}
template <int S, int H, int M> DI void epi(Ctx& c) {
  constexpr int f = PL(S).f[4 * H + M], u = H >> 6;
  if constexpr (f >= 0 && S == 1 && u >= 1) e_kv<u - 1, f>(c);
  if constexpr (f >= 0 && S == 3 && u >= 1) {
    if constexpr (H >= H4) {
      if constexpr (f < 80) e_gate<f % 40, 0, f / 40>(c);
      else if constexpr (f < 112) e_up<(f - 80) % 16, 1, (f - 80) / 16, 0>(c);
    } else if constexpr (u & 1) { if constexpr (f < 120) e_gate<f % 40, f / 80, (f / 40) & 1>(c); else if constexpr (f < 128) e_gate<f - 120, 1, 1>(c); }
    else if constexpr (f < 32) e_gate<f + 8, 1, 1>(c);
    else if constexpr (f < 96) e_up<f % 16, (f - 32) / 32, (f / 16) & 1, (f - 32) / 32>(c);
    else if constexpr (u == 2 && f == 120) switch_mixed(c);
  }
}
DI void mod_vectors(Ctx& c);
template <int A> DI void action(Ctx& c) {
  if constexpr (A == A_FLAG) { c.flv += 1; set_flag(c, c.fl3, c.flv); }
  else if constexpr (A == A_FLAG4) set_flag(c, c.fl4, c.flv);
  else if constexpr (A == A_MOD) mod_vectors(c);
  else {
    constexpr unsigned need = A == A_CHKX ? 1 : A - A_CHK2 + 2;
    pin(c.pollv);
    if (__builtin_expect(!__all(c.pollv >= need), 0)) spin(c, c.pollo + (A == A_CHKX ? (c.fl4 ^ 128) : 0), need);
    if constexpr (A == A_CHK4) { c.pollo = 0; pin(c.pollo); }
  }
}
template <int S, int H> DI void waits(Ctx& c) {
  if constexpr (!(H & 1)) wait_lgkm<(H + 4 <= seg_nh(S) ? 8 : 0)>();
  if constexpr (vwait(S, H) >= 0) wait_vm<vwait(S, H)>();
  if constexpr (isbar(S, H)) bar();
  GAP();
  if constexpr (PL(S).act[H] != 0) { action<PL(S).act[H]>(c); GAP(); }
}
template <int S, int H, int M> DI void slot(Ctx& c) {
  constexpr bool g4 = S == 3 && H >= H4;
  constexpr int cb = M >> 1, rb = M & 1, s4 = (H - H4) >> 3, k = PL(S).op[H] / 1024, i = PL(S).op[H] % 1024;
  constexpr int set = (H & 1) + (g4 ? 2 * g4h(s4) : 0), ks = g4 ? (g4j(s4) & 7) * 4 + ((H >> 1) & 3) : (H >> 1) & 31;
  constexpr bool first = g4 ? s4 < 2 && ks == 0 : ks == 0;
  if constexpr (first && H >= 64 && set < 2) c.pv[set][rb][cb] = c.acc[set][rb][cb];
  mfa<2 * ks + rb, (S == 1 && (H >> 6) == 1), first>(c.acc[set][rb][cb], cat(c.wl[H & 7][cb], c.wh[H & 7][cb]));
  GAP();
  if constexpr (H + RD < seg_nh(S)) rd<S, H + RD, M>(c);
  GAP();
  if constexpr (M == 0 && k == K_D && !(i & 3)) dma_off<S, (i >> 2)>(c);
  if constexpr (M == 1) {
    if constexpr (k == K_D && !(i & 3)) m0_add<((i >> 2) & 7) * SLOT>(c.mB);
    if constexpr (k == K_A && !(i & 1)) { c.sA += KBS; pins(c.sA); }
    if constexpr (k == K_A4 && !(i & 1)) { c.sA = c.sA4[i >= 96] + (g4kb(i >> 3) + ((i >> 1) & 3)) * KBS; pins(c.sA); }
  }
  if constexpr (M == 2) {
    if constexpr (k == K_D) dma<S, i>(c);
    if constexpr (k == K_T) warm<i>(c);
    if constexpr (k == K_C) warm_code<i>(c);
    if constexpr (k == K_A || k == K_A4) ld_act<i & 63>(c.vA, c.rs, c.sA);
    if constexpr (k == K_P) asm volatile("buffer_load_dword %0, %1, %2, %3 offen sc1" : "+v"(c.pollv) : "v"(c.pollo), "s"(c.rs), "s"(i ? c.fl4 ^ 128 : 0) : "memory");
    if constexpr (k == K_M) asm volatile("buffer_load_dwordx4 %0, %1, %2, %3 offen sc1" : "=v"(c.mp[i]) : "v"(c.mo), "s"(c.rs), "s"(i * 24576) : "memory");
    if constexpr (k == K_XS) asm volatile("buffer_store_dwordx4 %0, %1, %2, %3 offen offset:%4 sc1" ::"v"(c.acc[i >> 2][(i >> 1) & 1][i & 1]), "v"(c.vA), "s"(c.rs), "s"(c.xs4 + (i >> 2) * 4096), "n"((i & 3) * 1024) : "memory");
    if constexpr (k == K_XL) asm volatile("buffer_load_dwordx4 %0, %1, %2, %3 offen offset:%4 sc1" : "=v"(c.xl[i]) : "v"(c.vA), "s"(c.rs), "s"((c.xs4 ^ 32768) + (i >> 2) * 4096), "n"((i & 3) * 1024) : "memory");
  }
  GAP();
  if constexpr (M == 3 && H + 1 < seg_nh(S)) waits<S, H + 1>(c);
  epi<S, H, M>(c);
  GAP();
}
template <int S, int H0, int... Hs> DI void hsteps(Ctx& c, iseq<Hs...>) { ((slot<S, H0 + Hs, 0>(c), slot<S, H0 + Hs, 1>(c), slot<S, H0 + Hs, 2>(c), slot<S, H0 + Hs, 3>(c)), ...); }
template <int S, int... Is> DI void rd0(Ctx& c, iseq<Is...>) { (rd<S, Is / 4, Is % 4>(c), ...); }
template <int... Is> DI void ld0(Ctx& c, iseq<Is...>) { (((Is & 1) ? void() : (c.sA += KBS, pins(c.sA)), ld_act<Is>(c.vA, c.rs, c.sA)), ...); }
template <int S> DI void start(Ctx& c, unsigned base) {
  c.sA = base - KBS;
  ld0(c, make_iseq<2 * PRE>{});
  bar();
  rd0<S>(c, make_iseq<4 * RD>{});
  GAP();
  waits<S, 0>(c);
}
DI void lane_off(unsigned (&v)[4], int lane, int wave, int pitch, int col) {
  const int g = lane >> 4, q = (lane >> 2) & 3, p = lane & 3;
#pragma unroll
  for (int i = 0; i < 4; ++i) { v[i] = (32 * wave + 8 * i + 4 * (g >> 1) + q) * pitch + col + (g & 1) * 64 + (p ^ (g & 2)) * 16 - 1024 * i; pin(v[i]); }
}
DI void switch_mixed(Ctx& c) {
  if (c.it) {
    const int lane = opaque_tid() & 63, g = lane >> 4, q = (lane >> 2) & 3, p = lane & 3; int tm, tn; map_g3(opaque_bid(), tm, tn);
#pragma unroll
    for (int i = 0; i < 4; ++i) c.vG[i] = (32 * c.wave + 16 * (i >> 1) + 4 * g + q) * (F_ * 2) + (96 + tn) * 64 + (p ^ (2 * (g & 1))) * 16 - 1024 * i;
#pragma unroll
    for (int i = 0; i < 4; ++i) c.bX[i >> 1][i & 1] = c.bM[i >> 1][0] + (i & 1) * 1024;
    c.rG = mk_rsrc(c.w3, D_ * F_ * 2);
    c.dG = -4096 - 7 * 128 * F_ * 2;
  }
}
DI void mod_vectors(Ctx& c) {
  const int lane = opaque_tid() & 63, g = lane >> 4;
  pin(c.mp[0]); pin(c.mp[1]); pin(c.mp[2]); pin(c.mp[3]);
  retry(c, c.mp, c.mo, 24576);
  const f32x4 a = (__builtin_bit_cast(f32x4, c.mp[0]) + __builtin_bit_cast(f32x4, c.mp[1])) + (__builtin_bit_cast(f32x4, c.mp[2]) + __builtin_bit_cast(f32x4, c.mp[3]));
  const unsigned pb = c.lds0 + L_WV + c.wave * 1024;
  lds_st(pb + lane * 8, (u32x2){pk2(a[0], a[1]), pk2(a[2], a[3])});
#pragma unroll
  for (int i = 0; i < 4; ++i) {
    const u32x2 gv = lds_ld<u32x2>(pb + i * 32 + g * 8);
    c.eg[i >> 1][2 * (i & 1)] = unpk(gv[0]); c.eg[i >> 1][2 * (i & 1) + 1] = unpk(gv[1]);
    const u32x4 r = swz_rows(c.xr[i >> 1][i & 1]);
#pragma unroll
    for (int e = 0; e < 4; ++e) { c.er[i >> 1][i & 1][e] = unpk(r[e]); pin(c.er[i >> 1][i & 1][e]); }
  }
#pragma unroll
  for (int i = 0; i < 8; ++i) pin(c.eg[i >> 2][i & 3]);
}
}

DI u32x4 gated_resid(const f32x4& a0, const f32x4& a1, const f32x2 (&eg)[4], const f32x2 (&er)[4]) {
  u32x4 o;
#pragma unroll
  for (int j = 0; j < 4; ++j) {
    const f32x4& v = j < 2 ? a0 : a1;
    const f32x2 r = (f32x2){v[2 * (j & 1)], v[2 * (j & 1) + 1]} * eg[j] + er[j];
    o[j] = pk2(r[0], r[1]);
  }
  return o;
}

DI void phase1(Ctx& c) {
  using namespace en;
  const int tid = opaque_tid(), lane = tid & 63, b = opaque_bid(), wave = c.wave;
  const int fr = lane & 15, g = lane >> 4, q = (lane >> 2) & 3, p = lane & 3;
  int tm, tn; map_g1(b, tm, tn);
  const unsigned off = 2 * (b & 15) * KBS + ((b >> 4) * 8 + 2 * wave) * 1024 + lane * 16;
  u32x4 v[4]; f32x4 m[2];
  asm volatile("global_load_dwordx4 %0, %6, %9 offset:-4096\n\tglobal_load_dwordx4 %1, %6, %9\n\tglobal_load_dwordx4 %2, %7, %9\n\tglobal_load_dwordx4 %3, %7, %9 offset:1024\n\t"
               "global_load_dwordx4 %4, %8, %9\n\tglobal_load_dwordx4 %5, %8, %9 offset:1024"
               : "=&v"(m[0]), "=&v"(m[1]), "=&v"(v[0]), "=&v"(v[1]), "=&v"(v[2]), "=&v"(v[3])
               : "v"(W_PART + g * 24576 + 4096 + ((b & 15) * 64 + fr * 4) * 4), "v"(W_N1 + off), "v"(W_N1 + off + KBS), "s"(c.ws) : "memory");
  lane_off(c.vB, lane, wave, 6144, tn * 128);
  c.rB = mk_rsrc(c.wqkv, D_ * 3 * D_ * 2);
  c.mB = c.lds0 + wave * 4096;
  c.vC = ((b >> 3) * 4 + wave) * 256 + (lane & 1) * 128; c.tch = 0;
  warm_code<0>(c);
  dmas<1, 0>(c, make_iseq<8>{});
  asm volatile("s_waitcnt vmcnt(9)" : "+v"(m[0]), "+v"(m[1]), "+v"(v[0]), "+v"(v[1]), "+v"(v[2]), "+v"(v[3])::"memory");
  {
    f32x4 r;
#pragma unroll
    for (int e = 0; e < 4; ++e) r[e] = add2(m[0][e], m[1][e]);
    const f32x2 one = (g & 1) ? (f32x2){1.f, 1.f} : (f32x2){0.f, 0.f};
    const f32x2 m0 = rbf2(rbf2((f32x2){r[0], r[1]}) + one), m1 = rbf2(rbf2((f32x2){r[2], r[3]}) + one);
    const unsigned Mt = c.lds0 + L_WV + wave * 1024;
    lds_st(Mt + (lane & 31) * 16, (f32x4){m0[0], m0[1], m1[0], m1[1]});
#pragma unroll
    for (int s = 0; s < 2; ++s) {
      const unsigned a = Mt + s * 128 + g * 16;
      const f32x4 sh0 = lds_ld<f32x4>(a), sh1 = lds_ld<f32x4>(a + 64), sc0 = lds_ld<f32x4>(a + 256), sc1 = lds_ld<f32x4>(a + 320);
#pragma unroll
      for (int i = 0; i < 2; ++i) {
        const u32x4& w = v[2 * s + i];
        const f32x2 a0 = rbf2(unpk(w[0]) * (f32x2){sc0[0], sc0[1]}) + (f32x2){sh0[0], sh0[1]}, a1 = rbf2(unpk(w[1]) * (f32x2){sc0[2], sc0[3]}) + (f32x2){sh0[2], sh0[3]};
        const f32x2 a2 = rbf2(unpk(w[2]) * (f32x2){sc1[0], sc1[1]}) + (f32x2){sh1[0], sh1[1]}, a3 = rbf2(unpk(w[3]) * (f32x2){sc1[2], sc1[3]}) + (f32x2){sh1[2], sh1[3]};
        st16(c, W_H1 + off + s * KBS + i * 1024, (u32x4){pk2(a0[0], a0[1]), pk2(a1[0], a1[1]), pk2(a2[0], a2[1]), pk2(a3[0], a3[1])});
      }
    }
  }
  GAP();
  STAMP();
  c.vA = lane * 16; pin(c.vA);
  c.vT = lane < 16 ? lane * 128 : 0x7fffffff; pin(c.vT); c.sT = b * 4 + wave;
#pragma unroll
  for (int w = 0; w < 2; ++w) {
    const unsigned bw = c.lds0 + w * 65536 + g * 512 + q * 64 + p * 8;
    c.bW[0][w] = bw + (g & 1) * 32; c.bW[1][w] = bw + ((g & 1) ^ 1) * 32;
    pin(c.bW[0][w]); pin(c.bW[1][w]);
  }
  lane_off(c.vN, lane, wave, 2048, 0);
  c.rN = mk_rsrc(c.wo, D_ * D_ * 2);
  { int tm2, tn2; map_g2(b, tm2, tn2); c.sB4[0][0] = tn2 * 128; }
  c.kvw = c.lds0 + L_KV + wave * 4096 + lane * 16; pin(c.kvw);
  c.hso = W_HALO + ((tm - 1) * 16 + tn) * 4096;
  unsigned e_sel[4]; int e_base; f32x2 e_bias;
  {
    e_base = (fr & ~1) - 4 * g;
    const bool first = tm == 0 && wave == 0;
    e_bias = (f32x2){first && e_base < 0 ? -3.0e38f : 0.f, first && e_base < 2 ? -3.0e38f : 0.f};
    const unsigned s0 = (fr & 1) ? 0x01000c0cu : 0x0c0c0100u, s1 = (fr & 1) ? 0x03020c0cu : 0x0c0c0302u, z = 0x0c0c0c0cu;
    e_sel[0] = e_base >= 0 ? z : s0; e_sel[1] = e_base >= 2 ? z : s1; e_sel[2] = e_base >= 0 ? s0 : z; e_sel[3] = e_base >= 2 ? s1 : z;
    pin(e_base); pin(e_bias); pin(e_sel[0]); pin(e_sel[1]); pin(e_sel[2]); pin(e_sel[3]);
  }
  drain_vm();
  set_flag(c, flag_off(b >> 4, wave, b & 15, PH_LN1), 1);
  STAMP();
  dmas<1, 8>(c, make_iseq<24>{});
  spin(c, flag_off(tm, wave, fr, PH_LN1), 1);
  STAMP();
  start<1>(c, W_H1 + (tm * 8 + 2 * wave) * 1024);
  STAMP();
  hsteps<1, 0>(c, make_iseq<64>{});
  STAMP();
  hsteps<1, 64>(c, make_iseq<64>{});
  STAMP();
  hsteps<1, 128>(c, make_iseq<64>{});
  mfma_fence();
  STAMP();
  {
    u32x4 qf[2][2];
#pragma unroll
    for (int i = 0; i < 4; ++i) qf[i >> 1][i & 1] = pack8(c.acc[i >> 1][i & 1][0], c.acc[i >> 1][i & 1][1]);
    asm volatile("s_waitcnt vmcnt(%4) lgkmcnt(0)" : "+v"(c.hl[0]), "+v"(c.hl[1]), "+v"(c.hl[2]), "+v"(c.hl[3]) : "n"(after_halo()) : "memory");
    if (wave == 0) retry(c, c.hl, c.vA + c.hso, 1024);
    u32x4 vo[2][2][2];
#pragma unroll
    for (int h = 0; h < 2; ++h)
#pragma unroll
      for (int j = 0; j < 2; ++j) {
        vo[h][0][j] = (u32x4){c.hl[2 + h][2 * j], c.hl[2 + h][2 * j + 1], c.vf[h][0][2 * j], c.vf[h][0][2 * j + 1]};
        vo[h][1][j] = (u32x4){c.vf[h][0][2 * j], c.vf[h][0][2 * j + 1], c.vf[h][1][2 * j], c.vf[h][1][2 * j + 1]};
      }
#pragma unroll
    for (int i = 0; i < 4; ++i) { pin(qf[i >> 1][i & 1]); pin(c.hl[i]); }
    GAP(); nops<2>();
    f32x4 sp[2][2], sc[2][2];
#pragma unroll
    for (int h = 0; h < 2; ++h) { mfv(sp[h][0], c.hl[h], qf[h][0]); mfv(sc[h][0], c.kf[h][0], qf[h][0]); mfv(sp[h][1], c.kf[h][0], qf[h][1]); mfv(sc[h][1], c.kf[h][1], qf[h][1]); }
    GAP();
#pragma unroll
    for (int i = 0; i < 8; ++i) pin(vo[i >> 2][(i >> 1) & 1][i & 1]);
    mfma_fence();
    const bool par = fr & 1, cur0 = e_base >= 0, cur1 = e_base >= 2;
    auto red2 = [](float& u, float& v, auto op) {
      auto r = __builtin_amdgcn_permlane16_swap(__float_as_uint(u), __float_as_uint(v), false, false);
      const float m = op(__uint_as_float(r[0]), __uint_as_float(r[1]));
      auto r2 = __builtin_amdgcn_permlane32_swap(__float_as_uint(m), __float_as_uint(m), false, false);
      const float t = op(__uint_as_float(r2[0]), __uint_as_float(r2[1]));
      auto r3 = __builtin_amdgcn_permlane16_swap(__float_as_uint(t), __float_as_uint(t), false, false);
      u = __uint_as_float(r3[0]); v = __uint_as_float(r3[1]);
    };
    f32x2 l[2][2]; float mx[2][2], sm[2][2]; u32x4 pw[2][2];
#pragma unroll
    for (int h = 0; h < 2; ++h)
#pragma unroll
      for (int i = 0; i < 2; ++i) {
        const float a0 = sp[h][i][0], a1 = sp[h][i][1], a2 = sp[h][i][2], a3 = sp[h][i][3], b0 = sc[h][i][0], b1 = sc[h][i][1], b2 = sc[h][i][2], b3 = sc[h][i][3];
        const float x0 = cur0 ? b0 : a0, x1 = cur0 ? b1 : a1, x2 = cur1 ? b2 : a2, x3 = cur1 ? b3 : a3;
        const float y0 = par ? x1 : x0, y1 = par ? x3 : x2;
        l[h][i] = rbf2(rbf2((f32x2){y0, y1}) * 0.17677669529663687f);
        if (i == 0) l[h][i] += e_bias;
        mx[h][i] = fmax1(l[h][i][0], l[h][i][1]);
      }
#pragma unroll
    for (int h = 0; h < 2; ++h) red2(mx[h][0], mx[h][1], [](float x, float y) { return fmax1(x, y); });
#pragma unroll
    for (int h = 0; h < 2; ++h)
#pragma unroll
      for (int i = 0; i < 2; ++i) {
        const f32x2 t = (l[h][i] - mx[h][i]) * 1.4426950408889634f;
        l[h][i] = (f32x2){__builtin_amdgcn_exp2f(t[0]), __builtin_amdgcn_exp2f(t[1])};
        sm[h][i] = l[h][i][0] + l[h][i][1];
      }
#pragma unroll
    for (int h = 0; h < 2; ++h) red2(sm[h][0], sm[h][1], [](float x, float y) { return x + y; });
#pragma unroll
    for (int h = 0; h < 2; ++h)
#pragma unroll
      for (int i = 0; i < 2; ++i) {
        const f32x2 pr = l[h][i] * __builtin_amdgcn_rcpf(sm[h][i]);
        const unsigned pp = pk2(pr[0], pr[1]);
        pw[h][i] = (u32x4){__builtin_amdgcn_perm(pp, pp, e_sel[0]), __builtin_amdgcn_perm(pp, pp, e_sel[1]), __builtin_amdgcn_perm(pp, pp, e_sel[2]), __builtin_amdgcn_perm(pp, pp, e_sel[3])};
        pin(pw[h][i]);
      }
    GAP(); nops<2>();
    f32x4 o[2][2][2];
#pragma unroll
    for (int i = 0; i < 8; ++i) mfv(o[i >> 2][(i >> 1) & 1][i & 1], vo[i >> 2][(i >> 1) & 1][i & 1], pw[i >> 2][(i >> 1) & 1]);
    mfma_fence();
    const unsigned oo = W_O + 2 * tn * KBS + (tm * 8 + 2 * wave) * 1024 + c.vA;
#pragma unroll
    for (int i = 0; i < 4; ++i) st16(c, oo + (i >> 1) * KBS + (i & 1) * 1024, pack8(o[i >> 1][i & 1][0], o[i >> 1][i & 1][1]));
  }
  GAP();
  STAMP();
  drain_vm();
  set_flag(c, flag_off(tm, wave, tn, PH_G1), 1);
  STAMP();
}

DI void phase2(Ctx& c) {
  using namespace en;
  const int tid = opaque_tid(), lane = tid & 63, b = opaque_bid(), wave = c.wave;
  const int fr = lane & 15, g = lane >> 4;
  int tm, tn; map_g2(b, tm, tn);
  {
    f32x2 acc[4];
#pragma unroll
    for (int j = 0; j < 4; ++j) acc[j] = (f32x2){0.f, 0.f};
#pragma unroll
    for (int i = 0; i < 8; ++i) {
      pin(c.lw[i]); pin(c.lc);
      const float sv = silu_bf((u16)(c.lc[i >> 1] >> (16 * (i & 1))));
#pragma unroll
      for (int j = 0; j < 4; ++j) acc[j] += unpk(c.lw[i][j]) * sv;
    }
    const unsigned rb = c.lds0 + L_RED;
    lds_st(rb + wave * 256 + (lane & 7) * 32 + g * 8, red8(acc));
    wait_lgkm<0>(); bar();
    dmas<1, P1.dend>(c, make_iseq<128 - P1.dend>{});
    if (wave == 3) {
      const float sum = (lds_ld<float>(rb + lane * 4) + lds_ld<float>(rb + 256 + lane * 4)) + (lds_ld<float>(rb + 512 + lane * 4) + lds_ld<float>(rb + 768 + lane * 4));
      __builtin_amdgcn_raw_buffer_store_b32(min(__float_as_uint(sum), POISON - 1), c.rs, W_PART + (b & 3) * 24576 + (2048 + (b >> 2) * 64 + lane) * 4, 0, AUX_SC1);
    }
  }
  { int tm3, tn3; map_g3(b, tm3, tn3); lane_off(c.vN, lane, wave, F_ * 2, tn3 * 128); }
  c.rN = mk_rsrc(c.w1, D_ * F_ * 2);
  c.mo = W_PART + (2048 + g * 1024 + tn * 64 + fr * 4) * 4; pin(c.mo);
  STAMP();
  spin(c, flag_off(tm, wave, fr, PH_G1), 1);
  STAMP();
  start<2>(c, W_O + (tm * 8 + 2 * wave) * 1024);
  STAMP();
  hsteps<2, 0>(c, make_iseq<64>{});
  mfma_fence();
  STAMP();
  f32x2 d[2][8]; float s1[2];
#pragma unroll
  for (int i = 0; i < 2; ++i) {
#pragma unroll
    for (int s = 0; s < 2; ++s) {
      c.xr[i][s] = gated_resid(c.acc[s][i][0], c.acc[s][i][1], c.eg[s], c.er[i][s]);
#pragma unroll
      for (int j = 0; j < 4; ++j) d[i][4 * s + j] = unpk(c.xr[i][s][j]);
    }
    const f32x2 s2 = ((d[i][0] + d[i][1]) + (d[i][2] + d[i][3])) + ((d[i][4] + d[i][5]) + (d[i][6] + d[i][7]));
    s1[i] = s2[0] + s2[1];
  }
  const f32x2 mu = spread2(add2(s1[0], s1[1]) * (1.f / 64.f));
#pragma unroll
  for (int i = 0; i < 2; ++i) {
    f32x2 q2 = {0.f, 0.f};
#pragma unroll
    for (int j = 0; j < 8; ++j) { d[i][j] -= mu[i]; q2 += d[i][j] * d[i][j]; }
    s1[i] = q2[0] + q2[1];
  }
  const f32x2 m2 = spread2(add2(s1[0], s1[1]));
  const unsigned sb = W_STATS + (tm * 4 + wave) * 4096;
  if (g == 0) st16(c, sb + tn * 256 + fr * 16, (u32x4){min(__float_as_uint(mu[0]), POISON - 1), min(__float_as_uint(m2[0]), POISON - 1), min(__float_as_uint(mu[1]), POISON - 1), min(__float_as_uint(m2[1]), POISON - 1)});
  GAP();
  f32x2 sc1[8], sh[8];
  {
    const unsigned pb = c.lds0 + L_WV + wave * 1024 + g * 8;
#pragma unroll
    for (int i = 0; i < 4; ++i) {
      const u32x2 a = lds_ld<u32x2>(pb + 128 + i * 32), s = lds_ld<u32x2>(pb + 256 + i * 32);
#pragma unroll
      for (int e = 0; e < 2; ++e) { sh[2 * i + e] = unpk(a[e]); sc1[2 * i + e] = rbf2(unpk(s[e]) + 1.f); pin(sh[2 * i + e]); pin(sc1[2 * i + e]); }
    }
  }
  STAMP();
  u32x4 pv[4];
  __builtin_amdgcn_s_sleep(8);
  ld16x4(c, pv, sb + c.vA, 1024);
  retry(c, pv, sb + c.vA, 1024);
  STAMP();
  {
    f32x2 um[4], s2 = {0.f, 0.f}, q2 = {0.f, 0.f};
#pragma unroll
    for (int t = 0; t < 4; ++t) { um[t] = (f32x2){__uint_as_float(pv[t][0]), __uint_as_float(pv[t][2])}; s2 += um[t]; q2 += (f32x2){__uint_as_float(pv[t][1]), __uint_as_float(pv[t][3])}; }
    const float mg = add2(s2[0], s2[1]) * (1.f / 16.f);
    const f32x2 mean = spread2(mg);
    f32x2 e2 = {0.f, 0.f};
#pragma unroll
    for (int t = 0; t < 4; ++t) { const f32x2 dd = um[t] - mean; e2 += dd * dd; }
    q2 += e2 * 64.f;
    const f32x2 rs = spread2(ln_rstd(add2(q2[0], q2[1]) * (1.f / 1024.f))), mb = rbf2(mean);
    const unsigned ob = W_H2 + 2 * tn * KBS + (tm * 8 + 2 * wave) * 1024 + c.vA;
#pragma unroll
    for (int i = 0; i < 2; ++i) {
      const float dm = (mu[i] - mb[i]) * rs[i];
#pragma unroll
      for (int s = 0; s < 2; ++s) {
        u32x4 o;
#pragma unroll
        for (int j = 0; j < 4; ++j) {
          const f32x2 y = (d[i][4 * s + j] * rs[i] + dm) * sc1[4 * s + j] + sh[4 * s + j];
          o[j] = pk2(y[0], y[1]);
        }
        st16(c, ob + s * KBS + i * 1024, o);
      }
    }
  }
  GAP();
  STAMP();
  drain_vm();
  set_flag(c, flag_off(tm, wave, tn, PH_G2), 1);
  STAMP();
}

DI void phase3(Ctx& c) {
  using namespace en;
  int tid = opaque_tid(), lane = tid & 63; const int b = opaque_bid(), wave = c.wave;
  int tm, tn, tm4, tn4; map_g3(b, tm, tn); map_g2(b, tm4, tn4);
  bar();
  dmas<2, P2.dend>(c, make_iseq<64 - P2.dend>{});
  {
    const int g = lane >> 4;
#pragma unroll
    for (int i = 0; i < 4; ++i) { c.vB[i] = c.vN[i]; c.vG[i] = c.vN[i]; pin(c.vG[i]); c.bM[i >> 1][i & 1] = c.bW[i >> 1][i & 1] - g * 256; pin(c.bM[i >> 1][i & 1]); c.bX[i >> 1][i & 1] = c.bW[i >> 1][0] + (i & 1) * 256; pin(c.bX[i >> 1][i & 1]); }
    lane_off(c.vN, lane, wave, 2048, 0);
    c.rB = c.rN; c.rG = c.rN; c.rN = mk_rsrc(c.w2, D_ * F_ * 2);
    c.dG = 2048 - 7 * 128 * F_ * 2;
    c.pT[0] = c.w1; c.pT[1] = c.w3; c.oT[0] = c.oT[1] = c.sT * (F_ * 2) + 6144; c.oT[2] = c.sT * 2048;
    const int kh = tn4 & 1;
#pragma unroll
    for (int i = 0; i < 4; ++i) { c.sB4[i >> 1][i & 1] = (tn4 ^ 1 ^ (i & 1)) * 128 + kh * (16 >> (i >> 1)) * 32 * 2048; pins(c.sB4[i >> 1][i & 1]); }
#pragma unroll
    for (int i = 0; i < 2; ++i) { c.sA4[i] = W_HH + kh * (16 >> i) * KBS + (tm4 * 8 + 2 * wave) * 1024; pins(c.sA4[i]); }
    c.so = W_HH + 2 * tn * KBS + (tm * 8 + 2 * wave) * 1024;
    c.fl3 = flag_off(tm, wave, tn, PH_G3); c.fl4 = flag_off(tm4, wave, tn4, PH_G4);
    c.xs4 = W_XCH + (tm4 * 16 + tn4) * 32768 + wave * 8192;
    c.pollo = flag_off(tm4, wave, kh * 8 + (lane & 7), PH_G3); c.flv = 0; c.pollv = 0;
    pin(c.pollo); pin(c.flv); pins(c.so); pins(c.fl3); pins(c.fl4); pins(c.xs4);
  }
  STAMP();
  spin(c, flag_off(tm, wave, lane & 15, PH_G2), 1);
  STAMP();
  start<3>(c, W_H2 + (tm * 8 + 2 * wave) * 1024);
  STAMP();
  hsteps<3, 0>(c, make_iseq<64>{});
  STAMP();
  hsteps<3, 64>(c, make_iseq<64>{});
#pragma unroll 1
  for (c.it = 0; c.it < 2; ++c.it) { STAMP(); hsteps<3, 128>(c, make_iseq<64>{}); STAMP(); hsteps<3, 192>(c, make_iseq<64>{}); }
  STAMP();
  asm volatile(".Lblk_tail:");
  hsteps<3, 384>(c, make_iseq<64>{});
  STAMP();
  c.so -= tn * KBS;
  hsteps<3, H4>(c, make_iseq<64>{});
  STAMP();
  hsteps<3, H4 + 64>(c, make_iseq<64>{});
  STAMP();
  hsteps<3, H4 + 128>(c, make_iseq<48>{});
  STAMP();
  hsteps<3, H4 + 176>(c, make_iseq<NHM - H4 - 184>{});
  tid = opaque_tid(); lane = tid & 63;
  const int g = lane >> 4;
  {
    const unsigned pb = c.lds0 + L_WV + wave * 1024 + 384 + g * 8;
#pragma unroll
    for (int i = 0; i < 4; ++i) {
      const u32x2 gv = lds_ld<u32x2>(pb + i * 32);
      c.eg[i >> 1][2 * (i & 1)] = unpk(gv[0]); c.eg[i >> 1][2 * (i & 1) + 1] = unpk(gv[1]);
#pragma unroll
      for (int e = 0; e < 4; ++e) { c.er[i >> 1][i & 1][e] = unpk(c.xr[i >> 1][i & 1][e]); pin(c.er[i >> 1][i & 1][e]); }
    }
#pragma unroll
    for (int i = 0; i < 8; ++i) pin(c.eg[i >> 2][i & 3]);
  }
  unsigned oo = ((tm4 * 128 + wave * 32 + (lane & 15)) * D_ + tn4 * 64 + (g & 1) * 16 + (g >> 1) * 8) * 2;
  pin(oo);
  GAP();
  hsteps<3, NHM - 8>(c, make_iseq<8>{});
  mfma_fence();
  STAMP();
  wait_vm<0>();
  pin(c.tch);
#pragma unroll
  for (int u = 0; u < 8; ++u) pin(c.xl[u]);
#pragma unroll
  for (int i = 0; i < 4; ++i) {
    const int s = i & 1, rb = i >> 1;
    const u32x4 o = gated_resid(c.acc[2 + s][rb][0] + __builtin_bit_cast(f32x4, c.xl[4 * s + 2 * rb]), c.acc[2 + s][rb][1] + __builtin_bit_cast(f32x4, c.xl[4 * s + 2 * rb + 1]), c.eg[s], c.er[rb][s]);
    *(u32x4*)((char*)c.out + oo + rb * 16 * D_ * 2 + s * 64) = swz_rows(o);
  }
  STAMP();
}

__global__ __launch_bounds__(256, 1) void block_kernel(const u16* x, const u16* wqkv, const u16* wo, const u16* w1, const u16* w3, const u16* w2,
                                                       const u16* cc, const u16* wmod, u16* out, char* ws) {
  extern __shared__ __attribute__((aligned(16))) char smem[];
  asm volatile("" ::: ACLOB);
  Ctx c;
  c.x = x; c.wqkv = wqkv; c.wo = wo; c.w1 = w1; c.w3 = w3; c.w2 = w2; c.cc = cc; c.wmod = wmod; c.out = out; c.ws = ws;
  c.lds0 = (unsigned)(uintptr_t)(LDS_PTR(char))smem;
  c.rs = mk_rsrc(ws, W_END);
  {
    unsigned long long pc; unsigned n;
    asm volatile("s_getpc_b64 %0\n.Lblk_begin:\n\ts_mov_b32 %1, .Lblk_end-.Lblk_begin" : "=s"(pc), "=s"(n));
    c.rC = mk_rsrc((const void*)pc, n);
  }
  c.wave = __builtin_amdgcn_readfirstlane(threadIdx.x >> 6);
  STAMP0();
  phase1(c);
  phase2(c);
  phase3(c);
  asm volatile(".Lblk_end:");
}

extern "C" void kernel_launch(void* const* d_in, const int* in_sizes, int n_in, void* d_out, int out_size, void* d_ws,
                              size_t ws_size, hipStream_t stream) {
  (void)hipFuncSetAttribute((const void*)block_kernel, hipFuncAttributeMaxDynamicSharedMemorySize, LDS_TOTAL);
  pre_kernel<<<384, 256, 0, stream>>>((const u16*)d_in[7], (const u16*)d_in[8], (char*)d_ws, (const u16*)d_in[0], (const u16*)d_in[1], (const u16*)d_in[2], (const u16*)d_in[4], (const u16*)d_in[5]);
  block_kernel<<<256, 256, LDS_TOTAL, stream>>>((const u16*)d_in[0], (const u16*)d_in[1], (const u16*)d_in[2], (const u16*)d_in[4],
                                                (const u16*)d_in[5], (const u16*)d_in[6], (const u16*)d_in[7], (const u16*)d_in[8], (u16*)d_out, (char*)d_ws);
}
```

```cpp
#include <hip/hip_runtime.h>
#include <hip/hip_bf16.h>
#include <stdint.h>
#include <utility>

typedef unsigned short u16;
typedef short bf16x8 __attribute__((ext_vector_type(8)));
typedef short bf16x4 __attribute__((ext_vector_type(4)));
typedef float f32x4 __attribute__((ext_vector_type(4)));
typedef float f32x2 __attribute__((ext_vector_type(2)));
typedef unsigned int u32x4 __attribute__((ext_vector_type(4)));
typedef unsigned int u32x2 __attribute__((ext_vector_type(2)));
typedef __amdgpu_buffer_rsrc_t rsrc_t;
#define LDS_PTR(T) __attribute__((address_space(3))) T*
#define DI __device__ __forceinline__
#define GAP() __builtin_amdgcn_sched_barrier(0)
template <int... Is> using iseq = std::integer_sequence<int, Is...>;
template <int N> using make_iseq = std::make_integer_sequence<int, N>;

constexpr int S_ = 2048, D_ = 1024, F_ = 3584;
constexpr int KBS = (S_ / 16) * 1024;

DI float bf2f(u16 v) { return __uint_as_float(((unsigned)v) << 16); }
typedef __bf16 bf16x2_t __attribute__((ext_vector_type(2)));
DI unsigned pk2(float lo, float hi) { const f32x2 v = {lo, hi}; return __builtin_bit_cast(unsigned, __builtin_convertvector(v, bf16x2_t)); }
DI float rbf(float v) { return __uint_as_float(pk2(v, 0.f) << 16); }
DI float lo2f(unsigned u) { return __uint_as_float(u << 16); }
DI float hi2f(unsigned u) { return __uint_as_float(u & 0xffff0000u); }
template <int N> DI void wait_vm() { asm volatile("s_waitcnt vmcnt(%0)" ::"n"(N) : "memory"); }
DI void drain_vm() { wait_vm<0>(); __builtin_amdgcn_s_waitcnt(0x0F70); asm volatile("" ::: "memory"); }
template <int N> DI void wait_lgkm() { __builtin_amdgcn_s_waitcnt(0xC07F | (N << 8)); asm volatile("" ::: "memory"); }
DI void bar() { asm volatile("" ::: "memory"); __builtin_amdgcn_s_barrier(); asm volatile("" ::: "memory"); }
template <int IMM> DI void m0_add(unsigned base) { asm volatile("s_add_u32 m0, %0, %1" ::"s"(base), "n"(IMM) : "scc", "memory"); }
template <int N> DI void nops() { asm volatile("s_nop %0" ::"n"(N - 1)); }
template <class T> DI void pin(T& v) { asm volatile("" : "+v"(v)); }
template <class T> DI void pins(T& v) { asm volatile("" : "+s"(v)); }
DI rsrc_t mk_rsrc(const void* p, unsigned bytes) { return __builtin_amdgcn_make_buffer_rsrc((void*)p, 0, bytes, 0x00020000); }
template <int OFF> DI void bdma(unsigned voff, const rsrc_t& rs, unsigned soff) {
  asm volatile("buffer_load_dwordx4 %0, %1, %2 offen offset:%3 lds" ::"v"(voff), "s"(rs), "s"(soff), "n"(OFF) : "memory");
}
DI void touch_line(const void* p, unsigned lds_scratch) {
  asm volatile("s_mov_b32 m0, %1\n\ts_nop 0\n\tglobal_load_lds_dword %0, off" ::"v"(p), "s"(lds_scratch) : "memory");
}
#define ACLOB "a0","a1","a2","a3","a4","a5","a6","a7","a8","a9","a10","a11","a12","a13","a14","a15","a16","a17","a18","a19","a20","a21","a22","a23","a24","a25","a26","a27","a28","a29","a30","a31","a32","a33","a34","a35","a36","a37","a38","a39","a40","a41","a42","a43","a44","a45","a46","a47","a48","a49","a50","a51","a52","a53","a54","a55","a56","a57","a58","a59","a60","a61","a62","a63","a64","a65","a66","a67","a68","a69","a70","a71","a72","a73","a74","a75","a76","a77","a78","a79","a80","a81","a82","a83","a84","a85","a86","a87","a88","a89","a90","a91","a92","a93","a94","a95","a96","a97","a98","a99","a100","a101","a102","a103","a104","a105","a106","a107","a108","a109","a110","a111","a112","a113","a114","a115","a116","a117","a118","a119","a120","a121","a122","a123","a124","a125","a126","a127","a128","a129","a130","a131","a132","a133","a134","a135","a136","a137","a138","a139","a140","a141","a142","a143","a144","a145","a146","a147","a148","a149","a150","a151","a152","a153","a154","a155","a156","a157","a158","a159","a160","a161","a162","a163","a164","a165","a166","a167","a168","a169","a170","a171","a172","a173","a174","a175","a176","a177","a178","a179","a180","a181","a182","a183","a184","a185","a186","a187","a188","a189","a190","a191","a192","a193","a194","a195","a196","a197","a198","a199","a200","a201","a202","a203","a204","a205","a206","a207","a208","a209","a210","a211","a212","a213","a214","a215","a216","a217","a218","a219","a220","a221","a222","a223","a224","a225","a226","a227","a228","a229","a230","a231","a232","a233","a234","a235","a236","a237","a238","a239","a240","a241","a242","a243","a244","a245","a246","a247","a248","a249","a250","a251","a252","a253","a254","a255"
template <int R> DI void ld_act(unsigned voff, const rsrc_t& rs, unsigned soff) {
  asm volatile("buffer_load_dwordx4 a[%c3:%c4], %0, %1, %2 offen offset:%5 sc1" ::"v"(voff), "s"(rs), "s"(soff), "i"(4 * R), "i"(4 * R + 3), "n"((R & 1) * 1024) : "memory");
}
template <int R, bool SW, bool FIRST> DI void mfa(f32x4& c, bf16x8 w) {
  if constexpr (FIRST && SW) asm volatile("v_mfma_f32_16x16x32_bf16 %0, a[%c2:%c3], %1, 0" : "=&v"(c) : "v"(w), "i"(4 * R), "i"(4 * R + 3));
  else if constexpr (FIRST) asm volatile("v_mfma_f32_16x16x32_bf16 %0, %1, a[%c2:%c3], 0" : "=&v"(c) : "v"(w), "i"(4 * R), "i"(4 * R + 3));
  else if constexpr (SW) asm volatile("v_mfma_f32_16x16x32_bf16 %0, a[%c2:%c3], %1, %0" : "+v"(c) : "v"(w), "i"(4 * R), "i"(4 * R + 3));
  else asm volatile("v_mfma_f32_16x16x32_bf16 %0, %1, a[%c2:%c3], %0" : "+v"(c) : "v"(w), "i"(4 * R), "i"(4 * R + 3));
}
DI void mfv(f32x4& d, const u32x4& a, const u32x4& b) { asm volatile("v_mfma_f32_16x16x32_bf16 %0, %1, %2, 0" : "=&v"(d) : "v"(a), "v"(b)); }
DI void mfma_fence() { GAP(); asm volatile("s_nop 7\n\ts_nop 1" ::: "memory"); GAP(); }
DI bf16x4 ldsT(unsigned addr) { return __builtin_amdgcn_ds_read_tr16_b64_v4i16((LDS_PTR(bf16x4))(uintptr_t)addr); }
DI bf16x8 cat(bf16x4 lo, bf16x4 hi) { return __builtin_shufflevector(lo, hi, 0, 1, 2, 3, 4, 5, 6, 7); }
DI u32x4 pack8(const f32x4& a, const f32x4& b) { return (u32x4){pk2(a[0], a[1]), pk2(a[2], a[3]), pk2(b[0], b[1]), pk2(b[2], b[3])}; }
DI void v_mul(float& d, float a, float b) { asm volatile("v_mul_f32 %0, %1, %2" : "=v"(d) : "v"(a), "v"(b)); }
DI void v_mulc(float& d, float a) { asm volatile("v_mul_f32 %0, 0xbfb8aa3b, %1" : "=v"(d) : "v"(a)); }
DI void v_exp(float& d, float a) { asm volatile("v_exp_f32 %0, %1" : "=v"(d) : "v"(a)); }
DI void v_fmac(float& d) { asm volatile("v_fma_f32 %0, %0, %1, %1" : "+v"(d) : "s"(-1.4426950408889634f)); }
DI void v_rcp(float& d) { asm volatile("v_rcp_f32 %0, %0" : "+v"(d)); }
DI unsigned v_cvt(float a, float b) { unsigned d; asm volatile("v_cvt_pk_bf16_f32 %0, %1, %2" : "=v"(d) : "v"(a), "v"(b)); return d; }

DI float fmax1(float a, float b) { return __builtin_amdgcn_fmed3f(a, b, __builtin_inff()); }
DI float xadd16(float v) { auto r = __builtin_amdgcn_permlane16_swap(__float_as_uint(v), __float_as_uint(v), false, false); return __uint_as_float(r[0]) + __uint_as_float(r[1]); }
DI float xadd32(float v) { auto r = __builtin_amdgcn_permlane32_swap(__float_as_uint(v), __float_as_uint(v), false, false); return __uint_as_float(r[0]) + __uint_as_float(r[1]); }
DI float add2(float u, float v) {
  auto r = __builtin_amdgcn_permlane16_swap(__float_as_uint(u), __float_as_uint(v), false, false);
  return xadd32(__uint_as_float(r[0]) + __uint_as_float(r[1]));
}
DI f32x2 spread2(float t) {
  auto r = __builtin_amdgcn_permlane16_swap(__float_as_uint(t), __float_as_uint(t), false, false);
  return (f32x2){__uint_as_float(r[0]), __uint_as_float(r[1])};
}
template <int CTRL> DI float dpp_add(float v) { return v + __uint_as_float(__builtin_amdgcn_update_dpp(0, __float_as_uint(v), CTRL, 0xf, 0xf, true)); }
DI f32x2 red8(const f32x2 (&a)[4]) {
  f32x2 o;
#pragma unroll
  for (int e = 0; e < 2; ++e) {
    auto r0 = __builtin_amdgcn_permlane16_swap(__float_as_uint(a[0][e]), __float_as_uint(a[1][e]), false, false);
    auto r1 = __builtin_amdgcn_permlane16_swap(__float_as_uint(a[2][e]), __float_as_uint(a[3][e]), false, false);
    const float u = __uint_as_float(r0[0]) + __uint_as_float(r0[1]), v = __uint_as_float(r1[0]) + __uint_as_float(r1[1]);
    auto r2 = __builtin_amdgcn_permlane32_swap(__float_as_uint(u), __float_as_uint(v), false, false);
    o[e] = dpp_add<0x128>(__uint_as_float(r2[0]) + __uint_as_float(r2[1]));
  }
  return o;
}
DI float row16_sum(float v) {
  v = dpp_add<0xB1>(v);
  v = dpp_add<0x4E>(v);
  v = dpp_add<0x141>(v);
  v = dpp_add<0x140>(v);
  return v;
}
DI f32x2 unpk(unsigned u) { return (f32x2){lo2f(u), hi2f(u)}; }
DI f32x2 rbf2(f32x2 v) { return unpk(pk2(v[0], v[1])); }
DI float ln_rstd(float var) { return __builtin_amdgcn_rcpf(rbf(__builtin_amdgcn_sqrtf(rbf(rbf(var) + 1e-5f)))); }
DI float silu_bf(u16 c) { const float x = bf2f(c); return rbf(x * __builtin_amdgcn_rcpf(1.f + __builtin_amdgcn_exp2f(x * -1.4426950408889634f))); }
DI unsigned umax4(const u32x4& v) { return max(max(v[0], v[1]), max(v[2], v[3])); }
DI unsigned umax16(const u32x4 (&v)[4]) { return max(max(umax4(v[0]), umax4(v[1])), max(umax4(v[2]), umax4(v[3]))); }
template <class T> DI T lds_ld(unsigned a) { return *(const LDS_PTR(T))(uintptr_t)a; }
template <class T> DI void lds_st(unsigned a, const T& v) { *(LDS_PTR(T))(uintptr_t)a = v; }
DI u32x4 swz_rows(const u32x4& o) {
  auto r0 = __builtin_amdgcn_permlane16_swap(o[0], o[2], false, false);
  auto r1 = __builtin_amdgcn_permlane16_swap(o[1], o[3], false, false);
  return (u32x4){r0[0], r1[0], r0[1], r1[1]};
}

constexpr unsigned W_H1 = 1u << 16, W_O = W_H1 + (4u << 20), W_X1 = W_O + (4u << 20), W_H2 = W_X1 + (4u << 20), W_HH = W_H2 + (4u << 20);
constexpr unsigned W_PART = 32u << 20, W_FLAGS = W_PART + (128u << 10), W_STATS = W_FLAGS + (128u << 10), W_HALO = W_STATS + (256u << 10);
constexpr unsigned W_XCH = 34u << 20, W_END = 42u << 20, W_LN1 = W_PART + (96u << 10);
enum { PH_LN1 = 0, PH_G1, PH_G2, PH_G3, PH_G4 };
constexpr unsigned POISON = 0xffffffffu;
constexpr int AUX_SC1 = 16;
constexpr unsigned SPIN_CAP = 1u << 17;
constexpr int SLOT = 16384, L_KV = 8 * SLOT, L_WV = L_KV + 20480, L_RED = L_WV + 4096, L_TCH = L_RED + 1024, LDS_TOTAL = 163840;

#define STAMP()
#define STAMP0()
DI int opaque_tid() { int t = threadIdx.x; asm volatile("" : "+v"(t)); return t; }
DI int opaque_bid() { int b = blockIdx.x; asm volatile("" : "+s"(b)); return b; }

#define K1W 64
__global__ __launch_bounds__(256) void pre_kernel(const u16* __restrict__ c, const u16* __restrict__ wmod, char* __restrict__ ws,
                                                  const u16* __restrict__ x, const u16* __restrict__ wqkv) {
  __shared__ float red[4][64];
  const int t = threadIdx.x;
#define K1END()
  auto side = [&]() {
    const int gl = blockIdx.x * 256 + t;
    if (gl < 81920) *(u32x4*)(ws + W_STATS + gl * 16) = (u32x4){POISON, POISON, POISON, POISON};
    else if (gl < 86016) *(u32x4*)(ws + W_PART + ((gl >> 10) & 3) * 24576 + 8192 + (gl & 1023) * 16) = (u32x4){POISON, POISON, POISON, POISON};
    else if (gl < 88064) *(u32x4*)(ws + W_FLAGS + ((gl - 86016) >> 1) * 128 + (gl & 1) * 16) = (u32x4){0u, 0u, 0u, 0u};
    const int bb = blockIdx.x;
    unsigned r = 0;
    if (bb >= 128 && bb < 128 + K1W) r = *(const unsigned*)((const char*)wqkv + ((gl >> 4) & 1023) * 6144 + ((bb >> 6) + 2) % 3 * 2048 + (gl & 15) * 128);
    if (bb >= 344 && bb < 348) {
      unsigned long long pc; int d; unsigned n;
      asm volatile("s_getpc_b64 %0\n1:\n\ts_mov_b32 %1, .Lblk_begin-1b\n\ts_mov_b32 %2, .Lblk_end-.Lblk_begin" : "=s"(pc), "=s"(d), "=s"(n));
      r += __builtin_amdgcn_raw_buffer_load_b32(mk_rsrc((const void*)(pc + d), n), (gl - 344 * 256) * 128, 0, 0);
    }
    return r;
  };
  if (blockIdx.x >= 128) {
    const int rb = blockIdx.x - 128, r = t >> 5, l = t & 31;
    u32x4 v[4];
#pragma unroll
    for (int i = 0; i < 4; ++i) v[i] = *(const u32x4*)(x + (size_t)(rb * 8 + r) * D_ + (l + 32 * i) * 8);
    const unsigned pf = side();
    f32x2 f[16], s2 = {0.f, 0.f};
#pragma unroll
    for (int i = 0; i < 16; ++i) { f[i] = unpk(v[i >> 2][i & 3]); s2 += f[i]; }
    const float mean = xadd16(row16_sum(s2[0] + s2[1])) * (1.f / 1024.f);
    f32x2 q2 = {0.f, 0.f};
#pragma unroll
    for (int i = 0; i < 16; ++i) { const f32x2 d = f[i] - mean; q2 += d * d; }
    const float rstd = ln_rstd(xadd16(row16_sum(q2[0] + q2[1])) * (1.f / 1024.f)), mb = rbf(mean);
    const int row = rb * 8 + r;
    if (l == 0) *(f32x2*)(ws + W_LN1 + (row >> 5) * 256 + (row & 15) * 16 + ((row >> 4) & 1) * 8) = (f32x2){mb, rstd};
    asm volatile("" ::"v"(pf));
    K1END();
    return;
  }
  const int tc = t & 7, tr = t >> 3, cg = blockIdx.x >> 2, kq = blockIdx.x & 3;
  const int n0 = cg * 64 + tc * 8, k0 = kq * 256 + tr;
  u32x4 w[8]; u16 cv[8];
#pragma unroll
  for (int i = 0; i < 8; ++i) w[i] = __builtin_nontemporal_load((const u32x4*)(wmod + (size_t)(k0 + 32 * i) * 6144 + n0));
#pragma unroll
  for (int i = 0; i < 8; ++i) cv[i] = c[k0 + 32 * i];
  const unsigned pf = side();
  f32x2 acc[4];
#pragma unroll
  for (int j = 0; j < 4; ++j) acc[j] = (f32x2){0.f, 0.f};
#pragma unroll
  for (int i = 0; i < 8; ++i) {
    const float sv = silu_bf(cv[i]);
#pragma unroll
    for (int j = 0; j < 4; ++j) acc[j] += unpk(w[i][j]) * sv;
  }
  *(f32x2*)&red[t >> 6][tc * 8 + ((t >> 4) & 3) * 2] = red8(acc);
  __syncthreads();
  if (t < 64) ((float*)(ws + W_PART))[kq * 6144 + cg * 64 + t] = (red[0][t] + red[1][t]) + (red[2][t] + red[3][t]);
  asm volatile("" ::"v"(pf));
  K1END();
}

DI void map_g1(int b, int& tm, int& tn) { const int xcd = b & 7, idx = b >> 3; tm = (xcd & 1) * 8 + (idx & 7); tn = (xcd >> 1) * 4 + (idx >> 3); }
DI void map_g2(int b, int& tm, int& tn) { const int xcd = b & 7, idx = b >> 3; tm = (xcd & 3) * 4 + (idx >> 3); tn = (xcd >> 2) * 8 + (idx & 7); }
DI void map_g3(int b, int& tm, int& tn) { const int xcd = b & 7, idx = b >> 3; tm = (xcd >> 2) * 8 + (idx >> 2); tn = (xcd & 3) * 4 + (idx & 3); }

struct Ctx {
  const u16 *x, *wqkv, *wo, *w1, *w3, *w2, *cc, *wmod; u16* out; char* ws;
  rsrc_t rs, rB, rN, rG, rC;
  f32x4 acc[4][2][2];
  f32x4 pv[2][2][2], sg[2][2][2];
  bf16x4 wl[8][2], wh[8][2];
  float t[8]; u32x4 o;
  unsigned bW[2][2], bM[2][2], bX[2][2], vA, vB[4], vN[4], vG[4];
  unsigned lds0, oT, sB, sA, mB, so, dG, sB4[2][2], sA4[2], fl3, fl4, xs4;
  int wave, it;
  u32x4 kf[2][2], vf[2][2], hl[4]; unsigned kvw, hso;
  u32x4 xr[2][2], lw[8], lc;
  u32x4 mp[4]; unsigned mo; f32x2 eg[2][4], er[2][2][4];
  unsigned pollv, pollo, flv, tch, vT2, vT3, vT4, vT5, vC, vG0; u32x4 xl[8];
};
DI void st16(const Ctx& c, unsigned off, const u32x4& v) { __builtin_amdgcn_raw_buffer_store_b128(v, c.rs, off, 0, AUX_SC1); }
DI void ld16x4(const Ctx& c, u32x4 (&v)[4], unsigned off, unsigned step) {
  asm volatile("buffer_load_dwordx4 %0, %4, %5, 0 offen sc1\n\tbuffer_load_dwordx4 %1, %4, %5, %6 offen sc1\n\tbuffer_load_dwordx4 %2, %4, %5, %7 offen sc1\n\tbuffer_load_dwordx4 %3, %4, %5, %8 offen sc1\n\ts_waitcnt vmcnt(0)"
               : "=&v"(v[0]), "=&v"(v[1]), "=&v"(v[2]), "=&v"(v[3]) : "v"(off), "s"(c.rs), "s"(step), "s"(2 * step), "s"(3 * step) : "memory");
}
DI void retry(const Ctx& c, u32x4 (&v)[4], unsigned off, unsigned step) {
  if (__builtin_expect(__any(umax16(v) == POISON), 0)) {
#pragma unroll 1
    for (unsigned spins = 0; spins < SPIN_CAP; ++spins) { ld16x4(c, v, off, step); if (!__any(umax16(v) == POISON)) break; __builtin_amdgcn_s_sleep(1); }
  }
}
DI unsigned flag_off(int tm, int wave, int col, int ph) { return W_FLAGS + ((tm * 4 + wave) * 16 + col) * 128 + ph * 4; }
DI void set_flag(const Ctx& c, unsigned off, unsigned val) {
  asm volatile("s_mov_b64 exec, 1\n\tbuffer_store_dword %0, off, %1, %2 sc1\n\ts_mov_b64 exec, -1" ::"v"(val), "s"(c.rs), "s"(off) : "memory");
}
DI void spin(const Ctx& c, unsigned voff, unsigned need) {
#pragma unroll 1
  for (unsigned spins = 0; spins < SPIN_CAP; ++spins) {
    if (__all(__builtin_amdgcn_raw_buffer_load_b32(c.rs, voff, 0, AUX_SC1) >= need)) break;
    __builtin_amdgcn_s_sleep(1);
    asm volatile("" ::: "memory");
  }
  asm volatile("" ::: "memory");
}

#define NFD 0
#define NSD 0
#define L4 6
namespace en {
constexpr int RD = 4, PRE = 8, NHM = 672, H4 = 448, NB4 = 14 - L4;
constexpr int seg_nh(int s) { return s == 1 ? 192 : s == 2 ? 64 : NHM; }
constexpr int seg_nt(int s) { return s == 1 ? 32 : s == 2 ? 16 : 84; }
constexpr int g4j(int s) { return s < 2 * NB4 ? s / 2 : s < 2 * NB4 + L4 ? s - NB4 : s - NB4 - L4; }
constexpr int g4h(int s) { return s < 2 * NB4 ? s & 1 : s >= 2 * NB4 + L4; }
constexpr int g4first(int j) { return j < NB4 ? 2 * j : j + NB4; }
constexpr int g4last(int j) { return j < NB4 ? 2 * j + 1 : j + NB4 + L4; }
constexpr int g4kb(int j) { return j < 12 ? 32 * (j / 4) + 4 * (j % 4) : 96 + 4 * (j - 12); }
constexpr bool isbar(int s, int H) { return (H & 7) == 8 - RD && (s != 3 || H < NHM - 8); }
enum { K_A = 1, K_D, K_A4, K_P, K_XS, K_XL, K_M, K_T, K_C, K_G };
enum { A_FLAG = 1, A_CHK2, A_CHK3, A_CHK4, A_FLAG4, A_CHKX, A_MOD };
constexpr int reg0(int s, int H) { return s == 3 && H >= H4 ? H4 : H & ~63; }
constexpr bool hh_store(int s, int H, int f) { return s == 3 && f >= 0 && (H >= H4 ? f >= 80 && f < 112 && (f - 80) % 16 == 13 : H >= 128 && !((H >> 6) & 1) && f >= 32 && f < 96 && f % 16 == 13); }
#define QW 16
#define HP1 6
#define HP2 13
#define XD1 14
#define XD2 4
#define XD3 10
struct Plan { short op[NHM + 1], f[4 * NHM + 4], act[NHM + 1], vw[NHM + 1], dend, slack; };
constexpr Plan mk(int s) {
  Plan p{};
  const int NH = seg_nh(s), NT = seg_nt(s), OF = 2 * PRE;
  int atA[64] = {}, atD[4 * 84] = {}, atA4[112] = {}, req[NHM + 1] = {}, nvm[NHM + 2 * PRE + 1] = {}, cum[NHM + 2 * PRE + 2] = {};
  for (int H = 0; H <= NH; ++H) req[H] = -1000;
  for (int n = 0; n < OF; ++n) { atA[n] = n - OF; nvm[n] = 1; }
  int na = OF, nd = 32, n4 = 0, nxs = 0, nxl = 0, lim = 7, hchk = 100000;
  const int chk[4] = {386, 386, H4 + 8 * HP1 + 16, H4 + 8 * HP2 + 16}, hx0 = H4 + 8 * (2 * NB4 + L4) + 2;
  if (s == 3) {
    p.op[244] = p.op[372] = K_P * 1024; p.op[chk[2] - 16] = p.op[chk[3] - 16] = K_P * 1024;
    p.act[chk[0]] = A_CHK2; p.act[chk[2]] = A_CHK3; p.act[chk[3]] = A_CHK4;
    req[chk[0]] = 372; req[chk[2]] = chk[2] - 16; req[chk[3]] = chk[3] - 16;
  }
  if (s == 2) { p.act[56] = A_MOD; }
  bool quiet[NHM + 64] = {};
  auto window = [&](int H, int o0, int o1 = 0, int o2 = 0, int o3 = 0, int o4 = 0, int o5 = 0) {
    const int o[6] = {o0, o1, o2, o3, o4, o5};
    for (int i = 0; i < 6 && o[i]; ++i) p.op[H++] = o[i];
    for (int i = 0; i < QW; ++i) quiet[H + i] = true;
  };
  constexpr int T = K_T * 1024, G = K_G * 1024, C = K_C * 1024;
  if (s == 1) { window(84, G, G + 1, G + 2, T); window(124, C, T + 1, T + 2, T + 3); window(186, G + 8, G + 3, G + 4, G + 5, G + 6, G + 7); }
  if (s == 3) {
    window(84, C, T + 4, T + 5);
    for (int i = 0; i < 256; i += 128) { window(168 + i, T + 6, T + 7); window(206 + i, C + 1, T + 10, T + 8, T + 9); }
    p.op[400] = T + 11; p.op[456] = T + 12; p.op[500] = T + 13;
  }
  for (int H = 0; H < NH; ++H) {
    if (isbar(s, H)) lim = (H >> 3) + 7;
    if (p.op[H] || quiet[H]) continue;
    if (s == 2 && H >= 36 && H < 40) { p.op[H] = K_M * 1024 + H - 36; req[56] = H; continue; }
    if (na < 64) { atA[na] = H; p.op[H] = K_A * 1024 + na++; continue; }
    const bool dok = nd < 4 * NT && (nd >> 2) <= lim;
    if (s != 3 || H < 384) {
      if (dok && H >= 8 * ((nd >> 2) - 7) + 5 + 2 * (nd & 3)) { atD[nd] = H; p.op[H] = K_D * 1024 + nd++; }
      continue;
    }
    if (H >= hx0 && nxs < 8) {
      p.op[H] = K_XS * 1024 + nxs++;
      if (nxs == 8) {
        const int hf = (H + XD1 + 1) & ~1; hchk = (hf + XD2 + XD3 + 1) & ~1;
        p.act[hf] = A_FLAG4; req[hf] = H; p.op[hf + XD2] = K_P * 1024 + 1; p.act[hchk] = A_CHKX; req[hchk] = hf + XD2;
      }
      continue;
    }
    if (H >= hchk && nxl < 8) { p.op[H] = K_XL * 1024 + nxl++; continue; }
    bool aok = false;
    if (n4 < 112) {
      const int j = n4 >> 3, i = (n4 >> 1) & 3;
      aok = H >= (j < 8 ? 384 + 8 * j : H4 + 8 * g4last(j - 8)) + 2 * i + 2 && H >= chk[j >> 2];
    }
    const int dd = 8 * ((nd >> 2) - 1) + 4, da = H4 + 8 * g4first(n4 >> 3) - 4;
    if (aok && (!dok || da <= dd)) { atA4[n4] = H; p.op[H] = K_A4 * 1024 + n4++; }
    else if (dok) { atD[nd] = H; p.op[H] = K_D * 1024 + nd++; }
  }
  p.dend = nd;
  for (int H = 0; H < NH; ++H) {
    if (H < 64 && !(H & 1) && atA[H + 1] > req[H]) req[H] = atA[H + 1];
    const int T = (H >> 3) + 1;
    if (isbar(s, H) && T >= 8 && T < NH / 8 && atD[4 * T + 3] > req[H]) req[H] = atD[4 * T + 3];
  }
  if (s == 3) for (int j = 0; j < 14; ++j) { const int H = H4 + 8 * g4first(j) - 4; if (atA4[8 * j + 7] > req[H]) req[H] = atA4[8 * j + 7]; }
  int f = 0;
  for (int q = 0; q < 4 * NH; ++q) {
    const int H = q >> 2, M = q & 3, k = p.op[H] / 1024, i = p.op[H] % 1024;
    if (q == 4 * reg0(s, H)) f = 0;
    const bool prep = (k == K_D && !(i & 3)) || ((k == K_A || k == K_A4) && !(i & 1)) || k == K_T || k == K_C || k == K_G;
    const bool busy = (M == 2 && k) || (M == 1 && prep) || (M == 0 && k == K_D && !(i & 3)) || (M == 3 && (H & 1)) || q < 4 * reg0(s, H) + 12;
    p.f[q] = busy ? -1 : f++;
    if (M == 2 && k) nvm[H + OF]++;
    if (hh_store(s, H, p.f[q])) {
      nvm[H + OF]++;
      if (p.f[q] == (H >= H4 ? 109 : 93)) {
        int hb = H + (H < 384 ? 36 : 22); while (!isbar(s, hb)) ++hb;
        p.act[hb] = A_FLAG; if (H > req[hb]) req[hb] = H;
      }
    }
  }
  for (int H = 0; H <= NH + OF; ++H) cum[H + 1] = cum[H] + nvm[H];
  p.slack = 1000;
  for (int H = 0; H <= NH; ++H) {
    const int n = req[H] == -1000 ? -1 : cum[H + OF] - cum[req[H] + 1 + OF]; p.vw[H] = n > 63 ? 63 : n;
    if (H - req[H] < p.slack) p.slack = H - req[H];
  }
  if (s == 3 && (n4 < 112 || nxl < 8)) p.slack = -1;
  for (int u = 1; u < 7 && u < NH / 64; ++u) { int m = 0; for (int q = 256 * u; q < 256 * u + 256; ++q) if (p.f[q] > m) m = p.f[q]; if (m < (u == 6 ? 96 : 128)) p.slack = -2; }
  return p;
}
constexpr Plan P1 = mk(1), P2 = mk(2), P3 = mk(3);
constexpr const Plan& PL(int s) { return s == 1 ? P1 : s == 2 ? P2 : P3; }
constexpr bool same_iter() {
  for (int H = 128; H < 256; ++H) {
    const int a = P3.op[H], b = P3.op[H + 128];
    if (!(a == b || (a / 1024 == K_D && b == a + 64)) || P3.act[H + 1] != P3.act[H + 129]) return false;
    for (int m = 0; m < 4; ++m) if (P3.f[4 * H + m] != P3.f[4 * H + 512 + m]) return false;
  }
  return P3.dend == 4 * 84;
}
static_assert(P1.slack >= 8 && P2.slack >= 8 && P3.slack >= 8, "every load is issued well before it is needed");
static_assert(same_iter(), "the loop's two iterations must have the same program");
constexpr int F_HALO = 100;
constexpr int after_halo() { int n = 0; bool on = false; for (int H = 128; H < 192; ++H) { if (on && P1.op[H]) ++n; for (int m = 0; m < 4; ++m) if (P1.f[4 * H + m] == F_HALO) on = true; } return n; }
constexpr int vwait(int s, int H) {
  const int a = PL(s).vw[H];
  if (s == 3 && H > 128 && H <= 256) { const int b = P3.vw[H + 128]; return a < 0 ? b : b < 0 ? a : a < b ? a : b; }
  return a;
}

template <int S, int H, int R> DI void rd(Ctx& c) {
  constexpr bool mx = S == 3 && H >= 384 && H < H4;
  constexpr int cb = R >> 1, hi = R & 1, off = ((H >> 3) & 7) * SLOT + ((H >> 1) & 3) * 4096 + (H & 1) * (mx ? 1024 : 256) + hi * 2048;
  const unsigned ad = S == 3 && H >= 256 && H < 260 ? c.bX[cb][H & 1] + off - (H & 1) * 256 : (mx ? c.bM[cb][off >> 16] : c.bW[cb][off >> 16]) + (off & 65535);
  if constexpr (hi) c.wh[H & 7][cb] = ldsT(ad); else c.wl[H & 7][cb] = ldsT(ad);
}
template <int S, int T> DI void dma_off(Ctx& c) {
  constexpr int st = T & 7;
  if constexpr (S == 1 && T < 24) c.sB = st * 128 * 6144 + (T < 8 ? 2048 : T < 16 ? 4096 : 0);
  else if constexpr (S == 1) c.sB = c.sB4[0][0] + st * 128 * 2048;
  else if constexpr (S == 2) c.sB = st * 128 * F_ * 2;
  else if constexpr (T < 56) { if constexpr (st) c.sB += 128 * F_ * 2; else if constexpr (T & 8) c.sB -= 7 * 128 * F_ * 2; else c.sB += c.dG; }
  else c.sB = c.sB4[g4j(T - 56) >= 12][g4h(T - 56)] + g4kb(g4j(T - 56)) * 32 * 2048;
  pins(c.sB);
}
template <int S, int D> DI void dma(Ctx& c) {
  constexpr int T = D >> 2, i = D & 3;
  if constexpr ((S == 1 && T >= 24) || S == 2 || (S == 3 && T >= 56)) bdma<i * 1024>(c.vN[i], c.rN, c.sB);
  else if constexpr (S == 3 && !(T & 8)) bdma<i * 1024>(c.vG[i], (i & 1) ? c.rG : c.rB, c.sB);
  else if constexpr (S == 3) bdma<i * 1024>(c.vB[i], mk_rsrc(c.w3, D_ * F_ * 2), c.sB);
  else bdma<i * 1024>(c.vB[i], c.rB, c.sB);
}
template <int S, int D> DI void dma1(Ctx& c) {
  if constexpr (!(D & 3)) { dma_off<S, (D >> 2)>(c); m0_add<((D >> 2) & 7) * SLOT>(c.mB); nops<1>(); }
  dma<S, D>(c);
}
template <int S, int D0, int... Ds> DI void dmas(Ctx& c, iseq<Ds...>) { (dma1<S, D0 + Ds>(c), ...); }

template <int I, int S, int B> DI void e_gate(Ctx& c) {
  constexpr int e = I & 7, ph = I >> 3;
  if constexpr (ph == 0) { float r; v_mulc(r, c.pv[S][B][e >> 2][e & 3]); c.sg[S][B][e >> 2][e & 3] = r; }
  else if constexpr (ph == 1) v_exp(c.t[e], c.sg[S][B][e >> 2][e & 3]);
  else if constexpr (ph == 2) v_fmac(c.t[e]);
  else if constexpr (ph == 3) v_rcp(c.t[e]);
  else { float r; v_mul(r, c.sg[S][B][e >> 2][e & 3], c.t[e]); c.sg[S][B][e >> 2][e & 3] = r; }
}
template <int I, int S, int B, int G> DI void e_up(Ctx& c) {
  constexpr int e = I & 7;
  if constexpr (I < 8) v_mul(c.t[e], c.pv[S][B][e >> 2][e & 3], c.sg[G][B][e >> 2][e & 3]);
  else if constexpr (I < 12) c.o[e] = v_cvt(c.t[2 * e], c.t[2 * e + 1]);
  else if constexpr (I == 13) asm volatile("buffer_store_dwordx4 %0, %1, %2, %3 offen offset:%4 sc1" ::"v"(c.o), "v"(c.vA), "s"(c.rs), "s"(c.so), "n"(B * 1024) : "memory");
  else if constexpr (I == 14 && B == 1) { c.so += (S ? 31 : 1) * KBS; pins(c.so); }
}
template <int U, int F> DI void e_kv(Ctx& c) {
  u32x4 (&d)[2][2] = U ? c.vf : c.kf;
  if constexpr (F < 16) d[F >> 3][(F >> 2) & 1][F & 3] = v_cvt(c.pv[F >> 3][(F >> 2) & 1][(F >> 1) & 1][2 * (F & 1)], c.pv[F >> 3][(F >> 2) & 1][(F >> 1) & 1][2 * (F & 1) + 1]);
  else if constexpr (F == 18 || F == 22) lds_st(c.kvw + 4096 + U * 2048 + (F == 22) * 1024, d[F == 22][1]);
  else if constexpr (F == 17) {
    if (c.wave == 3) {
#pragma unroll
      for (int h = 0; h < 2; ++h) {
        const u32x4 v = d[h][1];
        st16(c, c.vA + c.hso + 65536 + U * 2048 + h * 1024, (u32x4){min(v[0], POISON - 1), min(v[1], POISON - 1), min(v[2], POISON - 1), min(v[3], POISON - 1)});
      }
    }
  } else if constexpr (U == 1 && F == F_HALO) {
    asm volatile("s_cmp_eq_u32 %4, 0\n\ts_cbranch_scc1 1f\n\tds_read_b128 %0, %5\n\tds_read_b128 %1, %5 offset:1024\n\tds_read_b128 %2, %5 offset:2048\n\tds_read_b128 %3, %5 offset:3072\n\ts_branch 2f\n"
                 "1:\n\tbuffer_load_dwordx4 %0, %6, %7, %8 offen sc1\n\tbuffer_load_dwordx4 %1, %6, %7, %8 offen offset:1024 sc1\n\tbuffer_load_dwordx4 %2, %6, %7, %8 offen offset:2048 sc1\n\tbuffer_load_dwordx4 %3, %6, %7, %8 offen offset:3072 sc1\n2:"
                 : "=&v"(c.hl[0]), "=&v"(c.hl[1]), "=&v"(c.hl[2]), "=&v"(c.hl[3]) : "s"(c.wave), "v"(c.kvw), "v"(c.vA), "s"(c.rs), "s"(c.hso < W_HALO ? W_END : c.hso) : "scc", "memory");
  }
}
DI void switch_mixed(Ctx& c);
DI void touch(Ctx& c, unsigned v, const rsrc_t& r, unsigned o) { asm volatile("buffer_load_dword %0, %1, %2, %3 offen" : "+v"(c.tch) : "v"(v), "s"(r), "s"(o) : "memory"); }
template <int W> DI void warm(Ctx& c) {
  const rsrc_t r1 = mk_rsrc(c.w1, D_ * F_ * 2), r3 = mk_rsrc(c.w3, D_ * F_ * 2);
  if constexpr (W == 0) touch(c, c.vT2, c.rN, 0);
  else if constexpr (W == 1) touch(c, c.vT3, r1, 0);
  else if constexpr (W == 2) touch(c, c.vT3, r3, 0);
  else if constexpr (W == 3) touch(c, c.vT3, r1, 2048);
  else if constexpr (W == 4) touch(c, c.vT3, r3, 2048);
  else if constexpr (W == 5) touch(c, c.vT3, r1, 4096);
  else if constexpr (W == 6) { touch(c, c.vT3, r3, 4096); c.vT3 = c.vT4; }
  else if constexpr (W == 7) touch(c, c.vT4, r1, 4096);
  else if constexpr (W < 10) { touch(c, c.vT5, c.rN, c.oT); c.oT += 2 << 20; }
  else touch(c, c.vT2, c.rN, (W - 10) * (2 << 20));
}
DI unsigned slow_col(const void* base, int first, int n) {
  const unsigned k = (3 - (unsigned)((uintptr_t)base >> 7) - first) & 7;
  return k < n ? (first + k) * 128 : 0x40000000u;
}
template <int W> DI void warm_code(Ctx& c) {
  unsigned o;
  if constexpr (W == 0) asm volatile("s_mov_b32 %0, .-.Lblk_begin" : "=s"(o)); else asm volatile("s_mov_b32 %0, .Lblk_tail-.Lblk_begin" : "=s"(o));
  asm volatile("buffer_load_dword %0, %1, %2, %3 offen" : "+v"(c.tch) : "v"(c.vC), "s"(c.rC), "s"(o) : "memory");
}
template <int S, int H, int M> DI void epi(Ctx& c) {
  constexpr int f = PL(S).f[4 * H + M], u = H >> 6;
  if constexpr (f >= 0 && S == 1 && u >= 1) e_kv<u - 1, f>(c);
  if constexpr (f >= 0 && S == 3 && u >= 1) {
    if constexpr (H >= H4) {
      if constexpr (f < 80) e_gate<f % 40, 0, f / 40>(c);
      else if constexpr (f < 112) e_up<(f - 80) % 16, 1, (f - 80) / 16, 0>(c);
    } else if constexpr (u & 1) { if constexpr (f < 120) e_gate<f % 40, f / 80, (f / 40) & 1>(c); else if constexpr (f < 128) e_gate<f - 120, 1, 1>(c); }
    else if constexpr (f < 32) e_gate<f + 8, 1, 1>(c);
    else if constexpr (f < 96) e_up<f % 16, (f - 32) / 32, (f / 16) & 1, (f - 32) / 32>(c);
    else if constexpr (u == 2 && f == 120) switch_mixed(c);
  }
}
DI void mod_vectors(Ctx& c);
template <int A> DI void action(Ctx& c) {
  if constexpr (A == A_FLAG) { c.flv += 1; set_flag(c, c.fl3, c.flv); }
  else if constexpr (A == A_FLAG4) set_flag(c, c.fl4, c.flv);
  else if constexpr (A == A_MOD) mod_vectors(c);
  else {
    constexpr unsigned need = A == A_CHKX ? 1 : A - A_CHK2 + 2;
    pin(c.pollv);
    if (__builtin_expect(!__all(c.pollv >= need), 0)) spin(c, c.pollo + (A == A_CHKX ? (c.fl4 ^ 128) : 0), need);
    if constexpr (A == A_CHK4) { c.pollo = 0; pin(c.pollo); }
  }
}
template <int S, int H> DI void waits(Ctx& c) {
  if constexpr (!(H & 1)) wait_lgkm<(H + 4 <= seg_nh(S) ? 8 : 0)>();
  if constexpr (vwait(S, H) >= 0) wait_vm<vwait(S, H)>();
  if constexpr (isbar(S, H)) bar();
  GAP();
  if constexpr (PL(S).act[H] != 0) { action<PL(S).act[H]>(c); GAP(); }
}
template <int S, int H, int M> DI void slot(Ctx& c) {
  constexpr bool g4 = S == 3 && H >= H4;
  constexpr int cb = M >> 1, rb = M & 1, s4 = (H - H4) >> 3, k = PL(S).op[H] / 1024, i = PL(S).op[H] % 1024;
  constexpr int set = (H & 1) + (g4 ? 2 * g4h(s4) : 0), ks = g4 ? (g4j(s4) & 7) * 4 + ((H >> 1) & 3) : (H >> 1) & 31;
  constexpr bool first = g4 ? s4 < 2 && ks == 0 : ks == 0;
  if constexpr (first && H >= 64 && set < 2) c.pv[set][rb][cb] = c.acc[set][rb][cb];
  mfa<2 * ks + rb, (S == 1 && (H >> 6) == 1), first>(c.acc[set][rb][cb], cat(c.wl[H & 7][cb], c.wh[H & 7][cb]));
  GAP();
  if constexpr (H + RD < seg_nh(S)) rd<S, H + RD, M>(c);
  GAP();
  if constexpr (M == 0 && k == K_D && !(i & 3)) dma_off<S, (i >> 2)>(c);
  if constexpr (M == 1) {
    if constexpr (k == K_D && !(i & 3)) m0_add<((i >> 2) & 7) * SLOT>(c.mB);
    if constexpr (k == K_A && !(i & 1)) { c.sA += KBS; pins(c.sA); }
    if constexpr (k == K_A4 && !(i & 1)) { c.sA = c.sA4[i >= 96] + (g4kb(i >> 3) + ((i >> 1) & 3)) * KBS; pins(c.sA); }
  }
  if constexpr (M == 2) {
    if constexpr (k == K_D) dma<S, i>(c);
    if constexpr (k == K_T) warm<i>(c);
    if constexpr (k == K_G && i < 8) asm volatile("buffer_load_dwordx4 %0, %1, %2, %3 offen nt" : "=v"(c.lw[i & 7]) : "v"(c.vG0), "s"(mk_rsrc(c.wmod, 1024 * 6144 * 2)), "s"(i * 6144 * 2) : "memory");
    if constexpr (k == K_G && i == 8) asm volatile("global_load_dwordx4 %0, %1, %2" : "=v"(c.lc) : "v"(((c.vA >> 7) + c.wave * 8) * 16), "s"(c.cc + (blockIdx.x & 3) * 256) : "memory");
    if constexpr (k == K_C) warm_code<i>(c);
    if constexpr (k == K_A || k == K_A4) ld_act<i & 63>(c.vA, c.rs, c.sA);
    if constexpr (k == K_P) asm volatile("buffer_load_dword %0, %1, %2, %3 offen sc1" : "+v"(c.pollv) : "v"(c.pollo), "s"(c.rs), "s"(i ? c.fl4 ^ 128 : 0) : "memory");
    if constexpr (k == K_M) asm volatile("buffer_load_dwordx4 %0, %1, %2, %3 offen sc1" : "=v"(c.mp[i]) : "v"(c.mo), "s"(c.rs), "s"(i * 24576) : "memory");
    if constexpr (k == K_XS) asm volatile("buffer_store_dwordx4 %0, %1, %2, %3 offen offset:%4 sc1" ::"v"(c.acc[i >> 2][(i >> 1) & 1][i & 1]), "v"(c.vA), "s"(c.rs), "s"(c.xs4 + (i >> 2) * 4096), "n"((i & 3) * 1024) : "memory");
    if constexpr (k == K_XL) asm volatile("buffer_load_dwordx4 %0, %1, %2, %3 offen offset:%4 sc1" : "=v"(c.xl[i]) : "v"(c.vA), "s"(c.rs), "s"((c.xs4 ^ 32768) + (i >> 2) * 4096), "n"((i & 3) * 1024) : "memory");
  }
  GAP();
  if constexpr (M == 3 && H + 1 < seg_nh(S)) waits<S, H + 1>(c);
  epi<S, H, M>(c);
  GAP();
}
template <int S, int H0, int... Hs> DI void hsteps(Ctx& c, iseq<Hs...>) { ((slot<S, H0 + Hs, 0>(c), slot<S, H0 + Hs, 1>(c), slot<S, H0 + Hs, 2>(c), slot<S, H0 + Hs, 3>(c)), ...); }
template <int S, int... Is> DI void rd0(Ctx& c, iseq<Is...>) { (rd<S, Is / 4, Is % 4>(c), ...); }
template <int... Is> DI void ld0(Ctx& c, iseq<Is...>) { (((Is & 1) ? void() : (c.sA += KBS, pins(c.sA)), ld_act<Is>(c.vA, c.rs, c.sA)), ...); }
template <int S> DI void start(Ctx& c, unsigned base) {
  c.sA = base - KBS;
  ld0(c, make_iseq<2 * PRE>{});
  bar();
  rd0<S>(c, make_iseq<4 * RD>{});
  GAP();
  waits<S, 0>(c);
}
DI void lane_off(unsigned (&v)[4], int lane, int wave, int pitch, int col) {
  const int g = lane >> 4, q = (lane >> 2) & 3, p = lane & 3;
#pragma unroll
  for (int i = 0; i < 4; ++i) { v[i] = (32 * wave + 8 * i + 4 * (g >> 1) + q) * pitch + col + (g & 1) * 64 + (p ^ (g & 2)) * 16 - 1024 * i; pin(v[i]); }
}
DI void switch_mixed(Ctx& c) {
  if (c.it) {
    const int lane = opaque_tid() & 63, g = lane >> 4, q = (lane >> 2) & 3, p = lane & 3; int tm, tn; map_g3(opaque_bid(), tm, tn);
#pragma unroll
    for (int i = 0; i < 4; ++i) c.vG[i] = (32 * c.wave + 16 * (i >> 1) + 4 * g + q) * (F_ * 2) + (96 + tn) * 64 + (p ^ (2 * (g & 1))) * 16 - 1024 * i;
#pragma unroll
    for (int i = 0; i < 4; ++i) c.bX[i >> 1][i & 1] = c.bM[i >> 1][0] + (i & 1) * 1024;
    c.rG = mk_rsrc(c.w3, D_ * F_ * 2);
    c.dG = -4096 - 7 * 128 * F_ * 2;
  }
}
DI void mod_vectors(Ctx& c) {
  const int lane = opaque_tid() & 63, g = lane >> 4;
  pin(c.mp[0]); pin(c.mp[1]); pin(c.mp[2]); pin(c.mp[3]);
  retry(c, c.mp, c.mo, 24576);
  const f32x4 a = (__builtin_bit_cast(f32x4, c.mp[0]) + __builtin_bit_cast(f32x4, c.mp[1])) + (__builtin_bit_cast(f32x4, c.mp[2]) + __builtin_bit_cast(f32x4, c.mp[3]));
  const unsigned pb = c.lds0 + L_WV + c.wave * 1024;
  lds_st(pb + lane * 8, (u32x2){pk2(a[0], a[1]), pk2(a[2], a[3])});
#pragma unroll
  for (int i = 0; i < 4; ++i) {
    const u32x2 gv = lds_ld<u32x2>(pb + i * 32 + g * 8);
    c.eg[i >> 1][2 * (i & 1)] = unpk(gv[0]); c.eg[i >> 1][2 * (i & 1) + 1] = unpk(gv[1]);
    const u32x4 r = c.xr[i >> 1][i & 1];
#pragma unroll
    for (int e = 0; e < 4; ++e) { c.er[i >> 1][i & 1][e] = unpk(r[e]); pin(c.er[i >> 1][i & 1][e]); }
  }
#pragma unroll
  for (int i = 0; i < 8; ++i) pin(c.eg[i >> 2][i & 3]);
}
}

DI u32x4 gated_resid(const f32x4& a0, const f32x4& a1, const f32x2 (&eg)[4], const f32x2 (&er)[4]) {
  u32x4 o;
#pragma unroll
  for (int j = 0; j < 4; ++j) {
    const f32x4& v = j < 2 ? a0 : a1;
    const f32x2 r = (f32x2){v[2 * (j & 1)], v[2 * (j & 1) + 1]} * eg[j] + er[j];
    o[j] = pk2(r[0], r[1]);
  }
  return o;
}

DI void phase1(Ctx& c) {
  using namespace en;
  const int tid = opaque_tid(), lane = tid & 63, b = opaque_bid(), wave = c.wave;
  const int fr = lane & 15, g = lane >> 4, q = (lane >> 2) & 3, p = lane & 3;
  int tm, tn; map_g1(b, tm, tn);
  int tm2, tn2; map_g2(b, tm2, tn2);
  f32x4 m[2], ln;
  asm volatile("global_load_dwordx4 %0, %7, %11 offset:-4096\n\tglobal_load_dwordx4 %1, %7, %11\n\tglobal_load_dwordx4 %2, %8, %12\n\tglobal_load_dwordx4 %3, %8, %12 offset:64\n\t"
               "global_load_dwordx4 %4, %9, %12\n\tglobal_load_dwordx4 %5, %9, %12 offset:64\n\tglobal_load_dwordx4 %6, %10, %11"
               : "=&v"(m[0]), "=&v"(m[1]), "=&v"(c.xr[0][0]), "=&v"(c.xr[0][1]), "=&v"(c.xr[1][0]), "=&v"(c.xr[1][1]), "=&v"(ln)
               : "v"(W_PART + g * 24576 + 4096 + (tn2 * 64 + fr * 4) * 4), "v"((fr * D_ + (g & 1) * 16 + (g >> 1) * 8) * 2), "v"(((fr + 16) * D_ + (g & 1) * 16 + (g >> 1) * 8) * 2),
                 "v"(W_LN1 + (tm2 * 4 + wave) * 256 + fr * 16), "s"(c.ws), "s"(c.x + (tm2 * 128 + wave * 32) * D_ + tn2 * 64) : "memory");
  lane_off(c.vB, lane, wave, 6144, tn * 128);
  c.rB = mk_rsrc(c.wqkv, D_ * 3 * D_ * 2);
  c.mB = c.lds0 + wave * 4096;
  const int j8 = ((b >> 3) * 4 + wave) * 8;
  const unsigned slow1 = (j8 + (lane & 7)) * 6144 + slow_col(c.wqkv, ((b >> 1) & 3) * 4, 4);
  c.vC = j8 * 32 + (lane & 1) * 128; c.tch = 0;
  warm_code<0>(c);
  touch(c, lane < 32 ? (b * 4 + wave) * 6144 + (lane >> 4) * 4096 + fr * 128 : lane < 56 ? slow1 + ((lane >> 3) & 3) * 2048 : 0x7fffffff, c.rB, 0);
  dmas<1, 0>(c, make_iseq<NFD>{});
  asm volatile("s_waitcnt vmcnt(%7)" : "+v"(m[0]), "+v"(m[1]), "+v"(c.xr[0][0]), "+v"(c.xr[0][1]), "+v"(c.xr[1][0]), "+v"(c.xr[1][1]), "+v"(ln) : "n"(NFD + 2) : "memory");
  {
    f32x4 r;
#pragma unroll
    for (int e = 0; e < 4; ++e) r[e] = add2(m[0][e], m[1][e]);
    const f32x2 one = (g & 1) ? (f32x2){1.f, 1.f} : (f32x2){0.f, 0.f};
    const f32x2 m0 = rbf2(rbf2((f32x2){r[0], r[1]}) + one), m1 = rbf2(rbf2((f32x2){r[2], r[3]}) + one);
    const unsigned Mt = c.lds0 + L_WV + wave * 1024, off = W_H1 + 2 * tn2 * KBS + (tm2 * 8 + 2 * wave) * 1024 + lane * 16;
    lds_st(Mt + (lane & 31) * 16, (f32x4){m0[0], m0[1], m1[0], m1[1]});
#pragma unroll
    for (int s = 0; s < 2; ++s) {
      const unsigned a = Mt + s * 128 + g * 16;
      const f32x4 sh0 = lds_ld<f32x4>(a), sh1 = lds_ld<f32x4>(a + 64), sc0 = lds_ld<f32x4>(a + 256), sc1 = lds_ld<f32x4>(a + 320);
      const f32x2 sc[4] = {{sc0[0], sc0[1]}, {sc0[2], sc0[3]}, {sc1[0], sc1[1]}, {sc1[2], sc1[3]}}, sh[4] = {{sh0[0], sh0[1]}, {sh0[2], sh0[3]}, {sh1[0], sh1[1]}, {sh1[2], sh1[3]}};
#pragma unroll
      for (int i = 0; i < 2; ++i) {
        GAP();
        if (s == 0 && i == 0) dmas<1, NFD>(c, make_iseq<NSD>{});
        if (s == 0 && i == 1) dmas<1, NFD + NSD>(c, make_iseq<NSD>{});
        if (s == 1 && i == 0) dmas<1, NFD + 2 * NSD>(c, make_iseq<NSD>{});
        if (s == 1 && i == 1) dmas<1, NFD + 3 * NSD>(c, make_iseq<NSD>{});
        GAP();
        c.xr[i][s] = swz_rows(c.xr[i][s]);
        u32x4 o;
#pragma unroll
        for (int j = 0; j < 4; ++j) {
          const f32x2 y = rbf2(rbf2(rbf2(unpk(c.xr[i][s][j]) - ln[2 * i]) * ln[2 * i + 1]) * sc[j]) + sh[j];
          o[j] = pk2(y[0], y[1]);
        }
        st16(c, off + s * KBS + i * 1024, o);
      }
    }
  }
  GAP();
  STAMP();
  c.vA = lane * 16; pin(c.vA);
  c.vT2 = lane < 16 ? (b * 4 + wave) * 2048 + lane * 128 : lane < 24 ? (j8 + lane - 16) * 2048 + slow_col(c.wo, ((b >> 2) & 1) * 8, 8) : 0x7fffffff;
  c.vT3 = lane < 16 ? (b * 4 + wave) * (F_ * 2) + lane * 128 : lane < 24 ? (j8 + lane - 16) * (F_ * 2) + slow_col(c.w1, (b & 3) * 4, 4) : 0x7fffffff;
  c.vG0 = (((b & 3) * 256 + (tid >> 3) * 8) * 6144 + 2048 + (b >> 2) * 64 + (tid & 7) * 8) * 2;
  pin(c.vG0);
  pin(c.vT2); pin(c.vT3);
#pragma unroll
  for (int w = 0; w < 2; ++w) {
    const unsigned bw = c.lds0 + w * 65536 + g * 512 + q * 64 + p * 8;
    c.bW[0][w] = bw + (g & 1) * 32; c.bW[1][w] = bw + ((g & 1) ^ 1) * 32;
    pin(c.bW[0][w]); pin(c.bW[1][w]);
  }
  lane_off(c.vN, lane, wave, 2048, 0);
  c.rN = mk_rsrc(c.wo, D_ * D_ * 2);
  c.sB4[0][0] = tn2 * 128;
  c.kvw = c.lds0 + L_KV + wave * 4096 + lane * 16; pin(c.kvw);
  c.hso = W_HALO + ((tm - 1) * 16 + tn) * 4096;
  unsigned e_sel[4]; int e_base; f32x2 e_bias;
  {
    e_base = (fr & ~1) - 4 * g;
    const bool first = tm == 0 && wave == 0;
    e_bias = (f32x2){first && e_base < 0 ? -3.0e38f : 0.f, first && e_base < 2 ? -3.0e38f : 0.f};
    const unsigned s0 = (fr & 1) ? 0x01000c0cu : 0x0c0c0100u, s1 = (fr & 1) ? 0x03020c0cu : 0x0c0c0302u, z = 0x0c0c0c0cu;
    e_sel[0] = e_base >= 0 ? z : s0; e_sel[1] = e_base >= 2 ? z : s1; e_sel[2] = e_base >= 0 ? s0 : z; e_sel[3] = e_base >= 2 ? s1 : z;
    pin(e_base); pin(e_bias); pin(e_sel[0]); pin(e_sel[1]); pin(e_sel[2]); pin(e_sel[3]);
  }
  wait_vm<0>();
  set_flag(c, flag_off(tm2, wave, tn2, PH_LN1), 1);
  STAMP();
  bar();
  dmas<1, NFD + 4 * NSD>(c, make_iseq<32 - NFD - 4 * NSD>{});
  spin(c, flag_off(tm, wave, fr, PH_LN1), 1);
  STAMP();
  start<1>(c, W_H1 + (tm * 8 + 2 * wave) * 1024);
  STAMP();
  hsteps<1, 0>(c, make_iseq<64>{});
  STAMP();
  hsteps<1, 64>(c, make_iseq<64>{});
  STAMP();
  hsteps<1, 128>(c, make_iseq<64>{});
  mfma_fence();
  STAMP();
  {
    u32x4 qf[2][2];
#pragma unroll
    for (int i = 0; i < 4; ++i) qf[i >> 1][i & 1] = pack8(c.acc[i >> 1][i & 1][0], c.acc[i >> 1][i & 1][1]);
    asm volatile("s_waitcnt vmcnt(%4) lgkmcnt(0)" : "+v"(c.hl[0]), "+v"(c.hl[1]), "+v"(c.hl[2]), "+v"(c.hl[3]) : "n"(after_halo()) : "memory");
    if (wave == 0) retry(c, c.hl, c.vA + c.hso, 1024);
    u32x4 vo[2][2][2];
#pragma unroll
    for (int h = 0; h < 2; ++h)
#pragma unroll
      for (int j = 0; j < 2; ++j) {
        vo[h][0][j] = (u32x4){c.hl[2 + h][2 * j], c.hl[2 + h][2 * j + 1], c.vf[h][0][2 * j], c.vf[h][0][2 * j + 1]};
        vo[h][1][j] = (u32x4){c.vf[h][0][2 * j], c.vf[h][0][2 * j + 1], c.vf[h][1][2 * j], c.vf[h][1][2 * j + 1]};
      }
#pragma unroll
    for (int i = 0; i < 4; ++i) { pin(qf[i >> 1][i & 1]); pin(c.hl[i]); }
    GAP(); nops<2>();
    f32x4 sp[2][2], sc[2][2];
#pragma unroll
    for (int h = 0; h < 2; ++h) { mfv(sp[h][0], c.hl[h], qf[h][0]); mfv(sc[h][0], c.kf[h][0], qf[h][0]); mfv(sp[h][1], c.kf[h][0], qf[h][1]); mfv(sc[h][1], c.kf[h][1], qf[h][1]); }
    GAP();
#pragma unroll
    for (int i = 0; i < 8; ++i) pin(vo[i >> 2][(i >> 1) & 1][i & 1]);
    mfma_fence();
    const bool par = fr & 1, cur0 = e_base >= 0, cur1 = e_base >= 2;
    auto red2 = [](float& u, float& v, auto op) {
      auto r = __builtin_amdgcn_permlane16_swap(__float_as_uint(u), __float_as_uint(v), false, false);
      const float m = op(__uint_as_float(r[0]), __uint_as_float(r[1]));
      auto r2 = __builtin_amdgcn_permlane32_swap(__float_as_uint(m), __float_as_uint(m), false, false);
      const float t = op(__uint_as_float(r2[0]), __uint_as_float(r2[1]));
      auto r3 = __builtin_amdgcn_permlane16_swap(__float_as_uint(t), __float_as_uint(t), false, false);
      u = __uint_as_float(r3[0]); v = __uint_as_float(r3[1]);
    };
    f32x2 l[2][2]; float mx[2][2], sm[2][2]; u32x4 pw[2][2];
#pragma unroll
    for (int h = 0; h < 2; ++h)
#pragma unroll
      for (int i = 0; i < 2; ++i) {
        const float a0 = sp[h][i][0], a1 = sp[h][i][1], a2 = sp[h][i][2], a3 = sp[h][i][3], b0 = sc[h][i][0], b1 = sc[h][i][1], b2 = sc[h][i][2], b3 = sc[h][i][3];
        const float x0 = cur0 ? b0 : a0, x1 = cur0 ? b1 : a1, x2 = cur1 ? b2 : a2, x3 = cur1 ? b3 : a3;
        const float y0 = par ? x1 : x0, y1 = par ? x3 : x2;
        l[h][i] = rbf2(rbf2((f32x2){y0, y1}) * 0.17677669529663687f);
        if (i == 0) l[h][i] += e_bias;
        mx[h][i] = fmax1(l[h][i][0], l[h][i][1]);
      }
#pragma unroll
    for (int h = 0; h < 2; ++h) red2(mx[h][0], mx[h][1], [](float x, float y) { return fmax1(x, y); });
#pragma unroll
    for (int h = 0; h < 2; ++h)
#pragma unroll
      for (int i = 0; i < 2; ++i) {
        const f32x2 t = (l[h][i] - mx[h][i]) * 1.4426950408889634f;
        l[h][i] = (f32x2){__builtin_amdgcn_exp2f(t[0]), __builtin_amdgcn_exp2f(t[1])};
        sm[h][i] = l[h][i][0] + l[h][i][1];
      }
#pragma unroll
    for (int h = 0; h < 2; ++h) red2(sm[h][0], sm[h][1], [](float x, float y) { return x + y; });
#pragma unroll
    for (int h = 0; h < 2; ++h)
#pragma unroll
      for (int i = 0; i < 2; ++i) {
        const f32x2 pr = l[h][i] * __builtin_amdgcn_rcpf(sm[h][i]);
        const unsigned pp = pk2(pr[0], pr[1]);
        pw[h][i] = (u32x4){__builtin_amdgcn_perm(pp, pp, e_sel[0]), __builtin_amdgcn_perm(pp, pp, e_sel[1]), __builtin_amdgcn_perm(pp, pp, e_sel[2]), __builtin_amdgcn_perm(pp, pp, e_sel[3])};
        pin(pw[h][i]);
      }
    GAP(); nops<2>();
    f32x4 o[2][2][2];
#pragma unroll
    for (int i = 0; i < 8; ++i) mfv(o[i >> 2][(i >> 1) & 1][i & 1], vo[i >> 2][(i >> 1) & 1][i & 1], pw[i >> 2][(i >> 1) & 1]);
    mfma_fence();
    const unsigned oo = W_O + 2 * tn * KBS + (tm * 8 + 2 * wave) * 1024 + c.vA;
#pragma unroll
    for (int i = 0; i < 4; ++i) st16(c, oo + (i >> 1) * KBS + (i & 1) * 1024, pack8(o[i >> 1][i & 1][0], o[i >> 1][i & 1][1]));
  }
  GAP();
  STAMP();
  wait_vm<0>();
  set_flag(c, flag_off(tm, wave, tn, PH_G1), 1);
  STAMP();
}

DI void phase2(Ctx& c) {
  using namespace en;
  const int tid = opaque_tid(), lane = tid & 63, b = opaque_bid(), wave = c.wave;
  const int fr = lane & 15, g = lane >> 4;
  int tm, tn; map_g2(b, tm, tn);
  {
    f32x2 acc[4];
#pragma unroll
    for (int j = 0; j < 4; ++j) acc[j] = (f32x2){0.f, 0.f};
#pragma unroll
    for (int i = 0; i < 8; ++i) {
      pin(c.lw[i]); pin(c.lc);
      const float sv = silu_bf((u16)(c.lc[i >> 1] >> (16 * (i & 1))));
#pragma unroll
      for (int j = 0; j < 4; ++j) acc[j] += unpk(c.lw[i][j]) * sv;
    }
    const unsigned rb = c.lds0 + L_RED;
    lds_st(rb + wave * 256 + (lane & 7) * 32 + g * 8, red8(acc));
    wait_lgkm<0>(); bar();
    if (wave == 3) {
      const float sum = (lds_ld<float>(rb + lane * 4) + lds_ld<float>(rb + 256 + lane * 4)) + (lds_ld<float>(rb + 512 + lane * 4) + lds_ld<float>(rb + 768 + lane * 4));
      __builtin_amdgcn_raw_buffer_store_b32(min(__float_as_uint(sum), POISON - 1), c.rs, W_PART + (b & 3) * 24576 + (2048 + (b >> 2) * 64 + lane) * 4, 0, AUX_SC1);
    }
  }
  dmas<1, P1.dend>(c, make_iseq<128 - P1.dend>{});
  { int tm3, tn3; map_g3(b, tm3, tn3); lane_off(c.vN, lane, wave, F_ * 2, tn3 * 128); }
  c.rN = mk_rsrc(c.w1, D_ * F_ * 2);
  c.mo = W_PART + (2048 + g * 1024 + tn * 64 + fr * 4) * 4; pin(c.mo);
  STAMP();
  spin(c, flag_off(tm, wave, fr, PH_G1), 1);
  STAMP();
  start<2>(c, W_O + (tm * 8 + 2 * wave) * 1024);
  STAMP();
  hsteps<2, 0>(c, make_iseq<64>{});
  mfma_fence();
  STAMP();
  f32x2 d[2][8]; float s1[2];
#pragma unroll
  for (int i = 0; i < 2; ++i) {
#pragma unroll
    for (int s = 0; s < 2; ++s) {
      c.xr[i][s] = gated_resid(c.acc[s][i][0], c.acc[s][i][1], c.eg[s], c.er[i][s]);
#pragma unroll
      for (int j = 0; j < 4; ++j) d[i][4 * s + j] = unpk(c.xr[i][s][j]);
    }
    const f32x2 s2 = ((d[i][0] + d[i][1]) + (d[i][2] + d[i][3])) + ((d[i][4] + d[i][5]) + (d[i][6] + d[i][7]));
    s1[i] = s2[0] + s2[1];
  }
  const f32x2 mu = spread2(add2(s1[0], s1[1]) * (1.f / 64.f));
#pragma unroll
  for (int i = 0; i < 2; ++i) {
    f32x2 q2 = {0.f, 0.f};
#pragma unroll
    for (int j = 0; j < 8; ++j) { d[i][j] -= mu[i]; q2 += d[i][j] * d[i][j]; }
    s1[i] = q2[0] + q2[1];
  }
  const f32x2 m2 = spread2(add2(s1[0], s1[1]));
  const unsigned sb = W_STATS + (tm * 4 + wave) * 4096;
  if (g == 0) st16(c, sb + tn * 256 + fr * 16, (u32x4){min(__float_as_uint(mu[0]), POISON - 1), min(__float_as_uint(m2[0]), POISON - 1), min(__float_as_uint(mu[1]), POISON - 1), min(__float_as_uint(m2[1]), POISON - 1)});
  GAP();
  f32x2 sc1[8], sh[8];
  {
    const unsigned pb = c.lds0 + L_WV + wave * 1024 + g * 8;
#pragma unroll
    for (int i = 0; i < 4; ++i) {
      const u32x2 a = lds_ld<u32x2>(pb + 128 + i * 32), s = lds_ld<u32x2>(pb + 256 + i * 32);
#pragma unroll
      for (int e = 0; e < 2; ++e) { sh[2 * i + e] = unpk(a[e]); sc1[2 * i + e] = rbf2(unpk(s[e]) + 1.f); pin(sh[2 * i + e]); pin(sc1[2 * i + e]); }
    }
  }
  STAMP();
  u32x4 pv[4];
  __builtin_amdgcn_s_sleep(8);
  ld16x4(c, pv, sb + c.vA, 1024);
  retry(c, pv, sb + c.vA, 1024);
  STAMP();
  {
    f32x2 um[4], s2 = {0.f, 0.f}, q2 = {0.f, 0.f};
#pragma unroll
    for (int t = 0; t < 4; ++t) { um[t] = (f32x2){__uint_as_float(pv[t][0]), __uint_as_float(pv[t][2])}; s2 += um[t]; q2 += (f32x2){__uint_as_float(pv[t][1]), __uint_as_float(pv[t][3])}; }
    const float mg = add2(s2[0], s2[1]) * (1.f / 16.f);
    const f32x2 mean = spread2(mg);
    f32x2 e2 = {0.f, 0.f};
#pragma unroll
    for (int t = 0; t < 4; ++t) { const f32x2 dd = um[t] - mean; e2 += dd * dd; }
    q2 += e2 * 64.f;
    const f32x2 rs = spread2(ln_rstd(add2(q2[0], q2[1]) * (1.f / 1024.f))), mb = rbf2(mean);
    const unsigned ob = W_H2 + 2 * tn * KBS + (tm * 8 + 2 * wave) * 1024 + c.vA;
#pragma unroll
    for (int i = 0; i < 2; ++i) {
      const float dm = (mu[i] - mb[i]) * rs[i];
#pragma unroll
      for (int s = 0; s < 2; ++s) {
        u32x4 o;
#pragma unroll
        for (int j = 0; j < 4; ++j) {
          const f32x2 y = (d[i][4 * s + j] * rs[i] + dm) * sc1[4 * s + j] + sh[4 * s + j];
          o[j] = pk2(y[0], y[1]);
        }
        st16(c, ob + s * KBS + i * 1024, o);
      }
    }
  }
  GAP();
  STAMP();
  drain_vm();
  set_flag(c, flag_off(tm, wave, tn, PH_G2), 1);
  STAMP();
}

DI void phase3(Ctx& c) {
  using namespace en;
  int tid = opaque_tid(), lane = tid & 63; const int b = opaque_bid(), wave = c.wave;
  int tm, tn, tm4, tn4; map_g3(b, tm, tn); map_g2(b, tm4, tn4);
  bar();
  dmas<2, P2.dend>(c, make_iseq<64 - P2.dend>{});
  {
    const int g = lane >> 4;
#pragma unroll
    for (int i = 0; i < 4; ++i) { c.vB[i] = c.vN[i]; c.vG[i] = c.vN[i]; pin(c.vG[i]); c.bM[i >> 1][i & 1] = c.bW[i >> 1][i & 1] - g * 256; pin(c.bM[i >> 1][i & 1]); c.bX[i >> 1][i & 1] = c.bW[i >> 1][0] + (i & 1) * 256; pin(c.bX[i >> 1][i & 1]); }
    lane_off(c.vN, lane, wave, 2048, 0);
    c.rB = c.rN; c.rG = c.rN; c.rN = mk_rsrc(c.w2, D_ * F_ * 2);
    c.dG = 2048 - 7 * 128 * F_ * 2;
    const int j8 = ((b >> 3) * 4 + wave) * 8 + lane - 16;
    const unsigned s2 = j8 * 2048 + slow_col(c.w2, ((b >> 2) & 1) * 8, 8);
    c.oT = 0;
    c.vT4 = lane < 8 ? (b * 4 + wave) * (F_ * 2) + 2048 + lane * 128 : lane >= 16 && lane < 24 ? j8 * (F_ * 2) + slow_col(c.w1, 16 + (b & 3) * 2, 2) : 0x7fffffff;
    c.vT5 = lane < 16 ? c.vT2 : 0x7fffffff;
    c.vT2 = lane >= 16 && lane < 24 ? s2 : 0x7fffffff;
    pin(c.vT2); pin(c.vT4); pin(c.vT5);
    const int kh = tn4 & 1;
#pragma unroll
    for (int i = 0; i < 4; ++i) { c.sB4[i >> 1][i & 1] = (tn4 ^ 1 ^ (i & 1)) * 128 + kh * (16 >> (i >> 1)) * 32 * 2048; pins(c.sB4[i >> 1][i & 1]); }
#pragma unroll
    for (int i = 0; i < 2; ++i) { c.sA4[i] = W_HH + kh * (16 >> i) * KBS + (tm4 * 8 + 2 * wave) * 1024; pins(c.sA4[i]); }
    c.so = W_HH + 2 * tn * KBS + (tm * 8 + 2 * wave) * 1024;
    c.fl3 = flag_off(tm, wave, tn, PH_G3); c.fl4 = flag_off(tm4, wave, tn4, PH_G4);
    c.xs4 = W_XCH + (tm4 * 16 + tn4) * 32768 + wave * 8192;
    c.pollo = flag_off(tm4, wave, kh * 8 + (lane & 7), PH_G3); c.flv = 0; c.pollv = 0;
    pin(c.pollo); pin(c.flv); pins(c.so); pins(c.fl3); pins(c.fl4); pins(c.xs4);
  }
  STAMP();
  spin(c, flag_off(tm, wave, lane & 15, PH_G2), 1);
  STAMP();
  start<3>(c, W_H2 + (tm * 8 + 2 * wave) * 1024);
  STAMP();
  hsteps<3, 0>(c, make_iseq<64>{});
  STAMP();
  hsteps<3, 64>(c, make_iseq<64>{});
#pragma unroll 1
  for (c.it = 0; c.it < 2; ++c.it) { STAMP(); hsteps<3, 128>(c, make_iseq<64>{}); STAMP(); hsteps<3, 192>(c, make_iseq<64>{}); }
  STAMP();
  asm volatile(".Lblk_tail:");
  hsteps<3, 384>(c, make_iseq<64>{});
  STAMP();
  c.so -= tn * KBS;
  hsteps<3, H4>(c, make_iseq<64>{});
  STAMP();
  hsteps<3, H4 + 64>(c, make_iseq<64>{});
  STAMP();
  hsteps<3, H4 + 128>(c, make_iseq<48>{});
  STAMP();
  hsteps<3, H4 + 176>(c, make_iseq<NHM - H4 - 184>{});
  tid = opaque_tid(); lane = tid & 63;
  const int g = lane >> 4;
  {
    const unsigned pb = c.lds0 + L_WV + wave * 1024 + 384 + g * 8;
#pragma unroll
    for (int i = 0; i < 4; ++i) {
      const u32x2 gv = lds_ld<u32x2>(pb + i * 32);
      c.eg[i >> 1][2 * (i & 1)] = unpk(gv[0]); c.eg[i >> 1][2 * (i & 1) + 1] = unpk(gv[1]);
#pragma unroll
      for (int e = 0; e < 4; ++e) { c.er[i >> 1][i & 1][e] = unpk(c.xr[i >> 1][i & 1][e]); pin(c.er[i >> 1][i & 1][e]); }
    }
#pragma unroll
    for (int i = 0; i < 8; ++i) pin(c.eg[i >> 2][i & 3]);
  }
  unsigned oo = ((tm4 * 128 + wave * 32 + (lane & 15)) * D_ + tn4 * 64 + (g & 1) * 16 + (g >> 1) * 8) * 2;
  pin(oo);
  GAP();
  hsteps<3, NHM - 8>(c, make_iseq<8>{});
  mfma_fence();
  STAMP();
  wait_vm<0>();
  pin(c.tch);
#pragma unroll
  for (int u = 0; u < 8; ++u) pin(c.xl[u]);
#pragma unroll
  for (int i = 0; i < 4; ++i) {
    const int s = i & 1, rb = i >> 1;
    const u32x4 o = gated_resid(c.acc[2 + s][rb][0] + __builtin_bit_cast(f32x4, c.xl[4 * s + 2 * rb]), c.acc[2 + s][rb][1] + __builtin_bit_cast(f32x4, c.xl[4 * s + 2 * rb + 1]), c.eg[s], c.er[rb][s]);
    *(u32x4*)((char*)c.out + oo + rb * 16 * D_ * 2 + s * 64) = swz_rows(o);
  }
  STAMP();
}

__global__ __launch_bounds__(256, 1) void block_kernel(const u16* x, const u16* wqkv, const u16* wo, const u16* w1, const u16* w3, const u16* w2,
                                                       const u16* cc, const u16* wmod, u16* out, char* ws) {
  extern __shared__ __attribute__((aligned(16))) char smem[];
  asm volatile("" ::: ACLOB);
  Ctx c;
  c.x = x; c.wqkv = wqkv; c.wo = wo; c.w1 = w1; c.w3 = w3; c.w2 = w2; c.cc = cc; c.wmod = wmod; c.out = out; c.ws = ws;
  c.lds0 = (unsigned)(uintptr_t)(LDS_PTR(char))smem;
  c.rs = mk_rsrc(ws, W_END);
  {
    unsigned long long pc; unsigned n;
    asm volatile("s_getpc_b64 %0\n.Lblk_begin:\n\ts_mov_b32 %1, .Lblk_end-.Lblk_begin" : "=s"(pc), "=s"(n));
    c.rC = mk_rsrc((const void*)pc, n);
  }
  c.wave = __builtin_amdgcn_readfirstlane(threadIdx.x >> 6);
  STAMP0();
  phase1(c);
  phase2(c);
  phase3(c);
  asm volatile(".Lblk_end:");
}

extern "C" void kernel_launch(void* const* d_in, const int* in_sizes, int n_in, void* d_out, int out_size, void* d_ws,
                              size_t ws_size, hipStream_t stream) {
  (void)hipFuncSetAttribute((const void*)block_kernel, hipFuncAttributeMaxDynamicSharedMemorySize, LDS_TOTAL);
  pre_kernel<<<384, 256, 0, stream>>>((const u16*)d_in[7], (const u16*)d_in[8], (char*)d_ws, (const u16*)d_in[0], (const u16*)d_in[1]);
  block_kernel<<<256, 256, LDS_TOTAL, stream>>>((const u16*)d_in[0], (const u16*)d_in[1], (const u16*)d_in[2], (const u16*)d_in[4],
                                                (const u16*)d_in[5], (const u16*)d_in[6], (const u16*)d_in[7], (const u16*)d_in[8], (u16*)d_out, (char*)d_ws);
}
```

```cpp
#include <hip/hip_runtime.h>
#include <hip/hip_bf16.h>
#include <stdint.h>
#include <utility>

typedef unsigned short u16;
typedef short bf16x8 __attribute__((ext_vector_type(8)));
typedef short bf16x4 __attribute__((ext_vector_type(4)));
typedef float f32x4 __attribute__((ext_vector_type(4)));
typedef float f32x2 __attribute__((ext_vector_type(2)));
typedef unsigned int u32x4 __attribute__((ext_vector_type(4)));
typedef unsigned int u32x2 __attribute__((ext_vector_type(2)));
#define LDS_PTR(T) __attribute__((address_space(3))) T*
#define DI __device__ __forceinline__
#define GAP() __builtin_amdgcn_sched_barrier(0)
template <int... Is> using iseq = std::integer_sequence<int, Is...>;
template <int N> using make_iseq = std::make_integer_sequence<int, N>;

constexpr int S_ = 2048, D_ = 1024, F_ = 3584;
constexpr int KBS = (S_ / 16) * 1024;

DI float bf2f(u16 v) { return __uint_as_float(((unsigned)v) << 16); }
typedef __bf16 bf16x2_t __attribute__((ext_vector_type(2)));
DI unsigned pk2(float lo, float hi) { const f32x2 v = {lo, hi}; return __builtin_bit_cast(unsigned, __builtin_convertvector(v, bf16x2_t)); }
DI float rbf(float v) { return __uint_as_float(pk2(v, 0.f) << 16); }
DI float lo2f(unsigned u) { return __uint_as_float(u << 16); }
DI float hi2f(unsigned u) { return __uint_as_float(u & 0xffff0000u); }
template <int N> DI void wait_vm() { asm volatile("s_waitcnt vmcnt(%0)" ::"n"(N) : "memory"); }
DI void wait_lgkm0() { __builtin_amdgcn_s_waitcnt(0xC07F); asm volatile("" ::: "memory"); }
DI void bar() { asm volatile("" ::: "memory"); __builtin_amdgcn_s_barrier(); asm volatile("" ::: "memory"); }
template <int IMM> DI void m0_add(unsigned base) { asm volatile("s_add_u32 m0, %0, %1" ::"s"(base), "n"(IMM) : "scc", "memory"); }
DI void set_nop() { asm volatile("s_nop 0"); }
template <class T> DI void pin(T& v) { asm volatile("" : "+v"(v)); }
DI void set_m0(unsigned v) { asm volatile("s_mov_b32 m0, %0\n\ts_nop 0" ::"s"(v) : "memory"); }
template <int OFF, bool DEV = false> DI void glds(unsigned voff, const void* sbase) {
  if constexpr (DEV) asm volatile("global_load_lds_dwordx4 %0, %1 offset:%2 sc1" ::"v"(voff), "s"(sbase), "n"(OFF) : "memory");
  else asm volatile("global_load_lds_dwordx4 %0, %1 offset:%2" ::"v"(voff), "s"(sbase), "n"(OFF) : "memory");
}
DI void glds_q(unsigned voff, const void* sbase) {
  asm volatile("s_mov_b64 exec, 0xffff\n\tglobal_load_lds_dwordx4 %0, %1 sc1\n\ts_mov_b64 exec, -1" ::"v"(voff), "s"(sbase) : "memory");
}
DI void touch_line(const void* p, unsigned lds_scratch) {
  asm volatile("s_mov_b32 m0, %1\n\ts_nop 0\n\tglobal_load_lds_dword %0, off" ::"v"(p), "s"(lds_scratch) : "memory");
}
DI bf16x8 ldsA(unsigned addr) { return *(const LDS_PTR(bf16x8))(uintptr_t)addr; }
DI bf16x4 ldsT(unsigned addr) { return __builtin_amdgcn_ds_read_tr16_b64_v4i16((LDS_PTR(bf16x4))(uintptr_t)addr); }
DI bf16x8 cat(bf16x4 lo, bf16x4 hi) { return __builtin_shufflevector(lo, hi, 0, 1, 2, 3, 4, 5, 6, 7); }
DI f32x4 mfma(bf16x8 a, bf16x8 b, f32x4 c) { return __builtin_amdgcn_mfma_f32_16x16x32_bf16(a, b, c, 0, 0, 0); }
DI void mfma_ip(f32x4& c, bf16x8 a, bf16x8 b) { asm volatile("v_mfma_f32_16x16x32_bf16 %0, %1, %2, %0" : "+v"(c) : "v"(a), "v"(b)); }
DI void mfma_fence() { asm volatile("s_nop 7\n\ts_nop 3" ::: "memory"); GAP(); }
DI bf16x8 pack8(const f32x4& a, const f32x4& b) {
  u32x4 w = {pk2(a[0], a[1]), pk2(a[2], a[3]), pk2(b[0], b[1]), pk2(b[2], b[3])};
  return __builtin_bit_cast(bf16x8, w);
}
DI float fmax1(float a, float b) { return __builtin_amdgcn_fmed3f(a, b, __builtin_inff()); }
DI float xadd16(float v) { auto r = __builtin_amdgcn_permlane16_swap(__float_as_uint(v), __float_as_uint(v), false, false); return __uint_as_float(r[0]) + __uint_as_float(r[1]); }
DI float xadd32(float v) { auto r = __builtin_amdgcn_permlane32_swap(__float_as_uint(v), __float_as_uint(v), false, false); return __uint_as_float(r[0]) + __uint_as_float(r[1]); }
DI float add2(float u, float v) {
  auto r = __builtin_amdgcn_permlane16_swap(__float_as_uint(u), __float_as_uint(v), false, false);
  return xadd32(__uint_as_float(r[0]) + __uint_as_float(r[1]));
}
DI f32x2 spread2(float t) {
  auto r = __builtin_amdgcn_permlane16_swap(__float_as_uint(t), __float_as_uint(t), false, false);
  return (f32x2){__uint_as_float(r[0]), __uint_as_float(r[1])};
}
template <int CTRL> DI float dpp_add(float v);
DI f32x2 red8(const f32x2 (&a)[4]) {
  f32x2 o;
#pragma unroll
  for (int e = 0; e < 2; ++e) {
    auto r0 = __builtin_amdgcn_permlane16_swap(__float_as_uint(a[0][e]), __float_as_uint(a[1][e]), false, false);
    auto r1 = __builtin_amdgcn_permlane16_swap(__float_as_uint(a[2][e]), __float_as_uint(a[3][e]), false, false);
    const float u = __uint_as_float(r0[0]) + __uint_as_float(r0[1]), v = __uint_as_float(r1[0]) + __uint_as_float(r1[1]);
    auto r2 = __builtin_amdgcn_permlane32_swap(__float_as_uint(u), __float_as_uint(v), false, false);
    o[e] = dpp_add<0x128>(__uint_as_float(r2[0]) + __uint_as_float(r2[1]));
  }
  return o;
}
template <int CTRL> DI float dpp_add(float v) { return v + __uint_as_float(__builtin_amdgcn_update_dpp(0, __float_as_uint(v), CTRL, 0xf, 0xf, true)); }
DI float row16_sum(float v) {
  v = dpp_add<0xB1>(v);
  v = dpp_add<0x4E>(v);
  v = dpp_add<0x141>(v);
  v = dpp_add<0x140>(v);
  return v;
}

DI f32x2 unpk(unsigned u) { return (f32x2){lo2f(u), hi2f(u)}; }
DI f32x2 rbf2(f32x2 v) { return unpk(pk2(v[0], v[1])); }
DI float ln_rstd(float var) { return __builtin_amdgcn_rcpf(rbf(__builtin_amdgcn_sqrtf(rbf(rbf(var) + 1e-5f)))); }
DI float silu_bf(u16 c) { const float x = bf2f(c); return rbf(x * __builtin_amdgcn_rcpf(1.f + __builtin_amdgcn_exp2f(x * -1.4426950408889634f))); }
DI unsigned umax4(const u32x4& v) { return max(max(v[0], v[1]), max(v[2], v[3])); }
template <class T> DI T lds_ld(unsigned a) { return *(const LDS_PTR(T))(uintptr_t)a; }
template <class T> DI void lds_st(unsigned a, const T& v) { *(LDS_PTR(T))(uintptr_t)a = v; }

constexpr unsigned W_H1 = 1u << 16, W_O = W_H1 + (4u << 20), W_X1 = W_O + (4u << 20), W_H2 = W_X1 + (4u << 20), W_HH = W_H2 + (4u << 20);
constexpr unsigned W_PART = 32u << 20, W_STATS = W_PART + (128u << 10), W_FLAGS = W_STATS + (512u << 10), W_XCH = 34u << 20, W_N1 = 42u << 20, W_END = 46u << 20;
constexpr int FLS = 32;
constexpr int FL_LN1 = 0, FL_G1 = 256, FL_G2 = FL_G1 + 512, FL_G3 = FL_G2 + 256, FL_G4 = (FL_G3 + 256) * 32, FL_WORDS = FL_G4 + 65536;
#define ARRS 16
constexpr unsigned W_CTL = 0, W_ARR = 4096, W_PART2 = W_PART + 65536;
constexpr unsigned POISON = 0xffffffffu;
constexpr int AUX_SC1 = 16;
constexpr unsigned SPIN_CAP = 1u << 17;
constexpr int PERS = 159744, LDS_TOTAL = 163840;

#define STAMP()
#define STAMP0()

struct Par {
  const u16 *x, *wqkv, *wo, *w1, *w3, *w2, *c, *wmod; u16* out; char* ws;
  u32x4 lw[4]; u16 lc[4];
  unsigned lds0; int pend;
  __amdgpu_buffer_rsrc_t rs;
};
DI int opaque_tid() { int t = threadIdx.x; asm volatile("" : "+v"(t)); return t; }
DI int opaque_bid() { int b = blockIdx.x; asm volatile("" : "+s"(b)); return b; }
DI void st16(const Par& P, unsigned off, const u32x4& v) { __builtin_amdgcn_raw_buffer_store_b128(v, P.rs, off, 0, AUX_SC1); }
DI void st8(const Par& P, unsigned off, const u32x2& v) { __builtin_amdgcn_raw_buffer_store_b64(v, P.rs, off, 0, AUX_SC1); }
DI u32x4 ld16(const Par& P, unsigned off) { return __builtin_amdgcn_raw_buffer_load_b128(P.rs, off, 0, AUX_SC1); }
DI void publish(const Par& P, int word) {
  wait_vm<0>(); __builtin_amdgcn_s_waitcnt(0x0F70); bar();
  if (threadIdx.x == 0) __hip_atomic_store((unsigned*)(P.ws + W_FLAGS) + word * FLS, 1u, __ATOMIC_RELAXED, __HIP_MEMORY_SCOPE_AGENT);
}
DI void await(const Par& P, int wave, int lane, int lo, int hi) {
  if (wave == 0) {
    const unsigned* f = (const unsigned*)(P.ws + W_FLAGS) + min(lo + lane, hi) * FLS;
#pragma unroll 1
    for (unsigned spins = 0; spins < SPIN_CAP; ++spins) {
      if (__all(__hip_atomic_load(f, __ATOMIC_RELAXED, __HIP_MEMORY_SCOPE_AGENT) == 1u)) break;
      __builtin_amdgcn_s_sleep(1);
    }
  }
  bar();
}

DI void phase0(Par& P) {
  const int tid = opaque_tid(), lane = tid & 63, b = opaque_bid();
  const int wave = __builtin_amdgcn_readfirstlane(tid >> 6);
  const unsigned lds0 = P.lds0;
  typedef unsigned long long u64;
  u64* ctl = (u64*)(P.ws + W_CTL);
  const u64 tag = __hip_atomic_load(ctl, __ATOMIC_RELAXED, __HIP_MEMORY_SCOPE_AGENT) + 0x9e3779b97f4a7c15ull;
  const u16* xr = P.x + (size_t)(b * 8 + wave) * D_ + lane * 8;
  const u32x4 v0 = *(const u32x4*)xr, v1 = *(const u32x4*)(xr + 512);
  const int k0 = (b & 7) * 128 + (tid >> 3);
  u32x4 w[2]; u16 cv[2];
#pragma unroll
  for (int i = 0; i < 2; ++i) {
    w[i] = __builtin_nontemporal_load((const u32x4*)(P.wmod + (size_t)(k0 + 64 * i) * 6144 + (b >> 3) * 64 + (tid & 7) * 8));
    cv[i] = P.c[k0 + 64 * i];
  }
  const int gl = b * 512 + tid;
  {
    const uintptr_t a0 = (uintptr_t)P.wqkv, a1 = (uintptr_t)P.wo - 96 * 65536, a2 = (uintptr_t)P.w1 - 128 * 65536, a3 = (uintptr_t)P.w3 - 170 * 65536;
    if (b < 212) touch_line((const char*)((b < 96 ? a0 : b < 128 ? a1 : b < 170 ? a2 : a3) + (size_t)gl * 128), lds0 + PERS + 1024);
  }
  if (wave < 4) {
    const int u = b * 256 + tid;
    const unsigned fill = u < 32768 + 4096 ? POISON : 0u;
    const unsigned o = u < 32768 ? W_STATS + u * 16 : u < 36864 ? W_PART2 + (u - 32768) * 16 : W_FLAGS + min(u - 36864, FL_WORDS / 4 - 1) * 16;
    st16(P, o, (u32x4){fill, fill, fill, fill});
  }
  {
    f32x2 acc[4];
#pragma unroll
    for (int j = 0; j < 4; ++j) acc[j] = unpk(w[0][j]) * silu_bf(cv[0]) + unpk(w[1][j]) * silu_bf(cv[1]);
    lds_st(lds0 + PERS + 2048 + wave * 256 + (lane & 7) * 32 + (lane >> 4) * 8, red8(acc));
  }
  f32x2 f[8], s2 = {0.f, 0.f};
#pragma unroll
  for (int i = 0; i < 8; ++i) { f[i] = unpk(i < 4 ? v0[i] : v1[i - 4]); s2 += f[i]; }
  const float mean = xadd32(xadd16(row16_sum(s2[0] + s2[1]))) * (1.f / 1024.f);
  f32x2 q2 = {0.f, 0.f};
#pragma unroll
  for (int i = 0; i < 8; ++i) { const f32x2 d = f[i] - mean; q2 += d * d; }
  const float rstd = ln_rstd(xadd32(xadd16(row16_sum(q2[0] + q2[1]))) * (1.f / 1024.f)), mb = rbf(mean);
#pragma unroll
  for (int i = 0; i < 2; ++i) {
    const int cch = lane + 64 * i, kb = cch >> 2, j = cch & 3;
    const unsigned p = lds0 + kb * 528 + (j & 1) * 256 + wave * 16 + (j >> 1) * 8;
#pragma unroll
    for (int hf = 0; hf < 2; ++hf) {
      const f32x2 n0 = rbf2(f[4 * i + 2 * hf] - mb) * rstd, n1 = rbf2(f[4 * i + 2 * hf + 1] - mb) * rstd;
      lds_st(p + 128 * hf, (u32x2){pk2(n0[0], n0[1]), pk2(n1[0], n1[1])});
    }
  }
  wait_lgkm0(); bar();
#pragma unroll
  for (int i = 0; i < 2; ++i) {
    const int idx = tid + 512 * i, kb = idx >> 5, g = (idx >> 3) & 3, fr = idx & 7;
    st16(P, W_N1 + (b >> 1) * 1024 + (b & 1) * 128 + kb * KBS + g * 256 + fr * 16, lds_ld<u32x4>(lds0 + kb * 528 + g * 128 + fr * 16));
  }
  if (wave == 7) {
    float sum = 0.f;
#pragma unroll
    for (int v = 0; v < 8; ++v) sum += lds_ld<float>(lds0 + PERS + 2048 + v * 256 + lane * 4);
    __builtin_amdgcn_raw_buffer_store_b32(__float_as_uint(sum), P.rs, W_PART + (b & 7) * 8192 + ((b >> 3) * 64 + lane) * 4, 0, AUX_SC1);
  }
  STAMP();
  wait_vm<0>(); __builtin_amdgcn_s_waitcnt(0x0F70); bar();
  u64* arr = (u64*)(P.ws + W_ARR);
  if (tid == 0) __hip_atomic_store(arr + b * ARRS, tag, __ATOMIC_RELAXED, __HIP_MEMORY_SCOPE_AGENT);
  STAMP();
  if (wave < 4) {
#pragma unroll 1
    for (unsigned spins = 0; spins < SPIN_CAP; ++spins) {
      if (__all(__hip_atomic_load(arr + tid * ARRS, __ATOMIC_RELAXED, __HIP_MEMORY_SCOPE_AGENT) == tag)) break;
      __builtin_amdgcn_s_sleep(1);
    }
  }
  bar();
  if (b == 0 && tid == 0) __hip_atomic_store(ctl, tag, __ATOMIC_RELAXED, __HIP_MEMORY_SCOPE_AGENT);
  STAMP();
}

DI void map_g1(int b, int& tm, int& tn) { const int xcd = b & 7, idx = b >> 3; tm = (xcd & 1) * 8 + (idx & 7); tn = (xcd >> 1) * 4 + (idx >> 3); }
DI void map_g2(int b, int& tm, int& tn) { const int xcd = b & 7, idx = b >> 3; tm = (xcd & 3) * 4 + (idx >> 3); tn = (xcd >> 2) * 8 + (idx & 7); }
DI void map_g3(int b, int& tm, int& tn) { const int idx = b >> 3; tm = idx & 7; tn = (b & 7) * 4 + (idx >> 3); }
DI void map_g4(int b, int& tm, int& tn, int& kh) { const int xcd = b & 7, idx = b >> 3; tm = (xcd & 3) * 4 + (idx & 3); tn = (xcd >> 2) * 4 + ((idx >> 2) & 3); kh = idx >> 4; }

namespace g1 {
constexpr int STAGE = 43008, OFF_H = 16384, OFF_B = 18432, NST = 3;
struct Fr { bf16x8 a[4], ah; bf16x4 wl[3], wh[3]; };
struct Ctx {
  f32x4 acc[4][3], acch;
  unsigned bA[2], bH[2], bW[3][2];
  unsigned voffA[3], voffB[3][3];
  const char *sA, *sH, *sB;
  unsigned mA, mH, mB;
};
constexpr int WIN = 65536;
template <int OFF> DI unsigned at(const unsigned (&b)[2]) { return b[OFF / WIN] + OFF % WIN; }
template <int SL, int T, int R> DI void rd(Ctx& c, Fr& f) {
  if constexpr (R < 6) {
    constexpr int j = R >> 1, hi = R & 1;
    const unsigned ad = at<SL * STAGE + T * 12288 + hi * 6144>(c.bW[j]);
    if constexpr (hi) f.wh[j] = ldsT(ad); else f.wl[j] = ldsT(ad);
  } else if constexpr (R < 10) f.a[R - 6] = ldsA(at<SL * STAGE + T * 8192 + (R - 6) * 1024>(c.bA));
  else f.ah = ldsA(at<SL * STAGE + T * 1024>(c.bH));
}
template <int SL, int Dm> DI void dmaA(Ctx& c) {
  if constexpr (Dm == 0) m0_add<SL * STAGE>(c.mA);
  else if constexpr (Dm == 1) glds<0, true>(c.voffA[SL], c.sA);
  else if constexpr (Dm == 3) glds<1024, true>(c.voffA[SL], c.sA);
  else if constexpr (Dm == 4) m0_add<SL * STAGE>(c.mH);
  else if constexpr (Dm == 5) { glds_q(c.voffA[SL], c.sH); if constexpr (SL == 2) { c.sA += 6 * KBS; c.sH += 6 * KBS; } }
}
template <int SL, int Dm> DI void dmaB(Ctx& c) {
  if constexpr (Dm == 0) m0_add<SL * STAGE>(c.mB);
  else if constexpr (Dm & 1) { glds<(Dm >> 1) * 1024>(c.voffB[SL][Dm >> 1], c.sB); if constexpr (SL == 2 && Dm == 5) c.sB += 3 * 64 * 3 * D_ * 2; }
}
template <int SL> DI void stageA(Ctx& c) { dmaA<SL, 0>(c); asm volatile("s_nop 0"); dmaA<SL, 1>(c); dmaA<SL, 3>(c); dmaA<SL, 4>(c); asm volatile("s_nop 0"); dmaA<SL, 5>(c); }
template <int SL> DI void stageB(Ctx& c) { dmaB<SL, 0>(c); asm volatile("s_nop 0"); dmaB<SL, 1>(c); dmaB<SL, 3>(c); dmaB<SL, 5>(c); }
template <int SL, int T, bool DMA, int VM, bool READ, int M> DI void slot(Ctx& c, const Fr& cur, Fr& nxt) {
  constexpr int PB = T == 1 ? 2 : 0, RS = T == 1 ? (SL + 1) % NST : SL;
  if constexpr (T == 1 && M == PB && READ) { wait_vm<VM>(); bar(); GAP(); }
  if constexpr (M < 12) mfma_ip(c.acc[M / 3][M % 3], cat(cur.wl[M % 3], cur.wh[M % 3]), cur.a[M / 3]);
  else mfma_ip(c.acch, cat(cur.wl[1], cur.wh[1]), cur.ah);
  GAP();
  if constexpr (READ && M >= PB) {
    constexpr int u = M - PB;
    if constexpr (u < 3) { rd<RS, T ^ 1, 2 * u>(c, nxt); rd<RS, T ^ 1, 2 * u + 1>(c, nxt); }
    else if constexpr (u < 8) rd<RS, T ^ 1, u + 3>(c, nxt);
  }
  if constexpr (DMA && (M & 3) >= 2) { constexpr int d = 2 * (M >> 2) + (M & 1); if constexpr (T == 0) dmaA<(SL + 2) % NST, d>(c); else dmaB<SL, d>(c); }
  GAP();
}
template <int SL, int T, bool DMA, int VM, bool READ, int... Ms> DI void sub_(Ctx& c, const Fr& cur, Fr& nxt, iseq<Ms...>) {
  wait_lgkm0();
  GAP();
  (slot<SL, T, DMA, VM, READ, Ms>(c, cur, nxt), ...);
}
template <int SL, bool DA, bool DB, int VM, bool READ = true> DI void stage(Ctx& c, Fr& f0, Fr& f1) {
  sub_<SL, 0, DA, 0, true>(c, f0, f1, make_iseq<13>{});
  sub_<SL, 1, DB, VM, READ>(c, f1, f0, make_iseq<13>{});
}
template <int... Rs> DI void rd_all(Ctx& c, Fr& f, iseq<Rs...>) { (rd<0, 0, Rs>(c, f), ...); }
}

DI void phase1(Par& P) {
  using namespace g1;
  const int tid = opaque_tid(), lane = tid & 63, b = opaque_bid();
  const int wave = __builtin_amdgcn_readfirstlane(tid >> 6);
  const unsigned lds0 = P.lds0;
  const int wm = wave >> 2, wn = wave & 3;
  int tm, tn; map_g1(b, tm, tn);
  Ctx c;
  const int kb = b >> 3;
  const unsigned off = kb * KBS + ((b & 7) * 16 + 2 * wave) * 1024 + lane * 16;
  u32x4 v0, v1; f32x4 m, mb2;
  asm volatile("global_load_dwordx4 %0, %4, %7 sc1\n\tglobal_load_dwordx4 %1, %5, %7 sc1\n\tglobal_load_dwordx4 %2, %6, %7 sc1\n\tglobal_load_dwordx4 %3, %6, %7 offset:1024 sc1"
               : "=&v"(m), "=&v"(mb2), "=&v"(v0), "=&v"(v1)
               : "v"(W_PART + (lane >> 4) * 8192 + (((lane >> 3) & 1) * 1024 + kb * 32 + (lane & 7) * 4) * 4),
                 "v"(W_PART + 32768 + (lane >> 4) * 8192 + (((lane >> 3) & 1) * 1024 + kb * 32 + (lane & 7) * 4) * 4), "v"(W_N1 + off), "s"(P.ws) : "memory");
#pragma unroll
  for (int t = 0; t < 3; ++t) {
    const int sbk = 4 * (3 * wave + t) + (lane >> 4), kq = sbk / 6, cb = sbk - 6 * kq, q = (lane >> 2) & 3, ch = (lane & 3) ^ (2 * (kq & 1));
    const unsigned vo = (unsigned)(((4 * kq + q) * (3 * D_) + (cb >> 1) * D_ + tn * 64 + (cb & 1) * 32 + 8 * ch) * 2 + 2048 - 1024 * t);
#pragma unroll
    for (int s = 0; s < 3; ++s) { c.voffB[s][t] = vo + s * (64 * 3 * D_ * 2); pin(c.voffB[s][t]); }
  }
  c.sB = (const char*)P.wqkv - 2048;
  c.mB = lds0 + OFF_B + 3 * wave * 1024;
  stageB<0>(c);
  asm volatile("s_waitcnt vmcnt(3)" : "+v"(m), "+v"(mb2), "+v"(v0), "+v"(v1)::"memory");
  m += mb2;
  const int fr = lane & 15, g = lane >> 4, q = (lane >> 2) & 3, p = lane & 3;
  {
#pragma unroll
    for (int e = 0; e < 4; ++e) m[e] = xadd32(xadd16(m[e]));
    const f32x2 one = (lane & 8) ? (f32x2){1.f, 1.f} : (f32x2){0.f, 0.f};
    const f32x2 m0 = rbf2(rbf2((f32x2){m[0], m[1]}) + one), m1 = rbf2(rbf2((f32x2){m[2], m[3]}) + one);
    const unsigned Mt = lds0 + wave * 256;
    lds_st(Mt + (lane & 15) * 16, (f32x4){m0[0], m0[1], m1[0], m1[1]});
    const f32x4 sh0 = lds_ld<f32x4>(Mt + g * 16), sh1 = lds_ld<f32x4>(Mt + 64 + g * 16), sc0 = lds_ld<f32x4>(Mt + 128 + g * 16), sc1 = lds_ld<f32x4>(Mt + 192 + g * 16);
    auto mod8 = [&](const u32x4& v) {
      const f32x2 a = rbf2(unpk(v[0]) * (f32x2){sc0[0], sc0[1]}) + (f32x2){sh0[0], sh0[1]}, bb = rbf2(unpk(v[1]) * (f32x2){sc0[2], sc0[3]}) + (f32x2){sh0[2], sh0[3]};
      const f32x2 cc = rbf2(unpk(v[2]) * (f32x2){sc1[0], sc1[1]}) + (f32x2){sh1[0], sh1[1]}, d = rbf2(unpk(v[3]) * (f32x2){sc1[2], sc1[3]}) + (f32x2){sh1[2], sh1[3]};
      return (u32x4){pk2(a[0], a[1]), pk2(bb[0], bb[1]), pk2(cc[0], cc[1]), pk2(d[0], d[1])};
    };
    st16(P, W_H1 + off, mod8(v0)); st16(P, W_H1 + off + 1024, mod8(v1));
  }
  GAP();
  STAMP();
#pragma unroll
  for (int s = 0; s < 3; ++s) { c.voffA[s] = lane * 16 + s * 2 * KBS; pin(c.voffA[s]); }
  c.sA = P.ws + W_H1 + (size_t)(wave >> 2) * KBS + (tm * 8 + 2 * (wave & 3)) * 1024;
  c.mA = lds0 + wave * 2048;
  c.sH = P.ws + W_H1 + (size_t)(wave >> 2) * KBS + (tm ? tm * 8 - 1 : 0) * 1024 + (wave & 3) * 256;
  c.mH = lds0 + OFF_H + wave * 256;
#pragma unroll
  for (int w = 0; w < 2; ++w) {
    c.bA[w] = lds0 + w * WIN + wm * 4096 + lane * 16;
    c.bH[w] = lds0 + w * WIN + OFF_H + lane * 16;
    c.bW[0][w] = lds0 + w * WIN + OFF_B + g * 1536 + (wn >> 1) * 256 + q * 64 + ((wn ^ g) & 1) * 32 + p * 8;
    c.bW[1][w] = c.bW[0][w] + 512 + wm * 512;
    c.bW[2][w] = c.bW[0][w] + 1024 - wm * 512;
    pin(c.bA[w]); pin(c.bH[w]); pin(c.bW[0][w]); pin(c.bW[1][w]); pin(c.bW[2][w]);
  }
  publish(P, FL_LN1 + (b & 7) * 32 + kb);
  STAMP();
  stageB<1>(c); stageB<2>(c);
  await(P, wave, lane, FL_LN1 + (tm ? (tm - 1) >> 1 : 0) * 32, FL_LN1 + (tm >> 1) * 32 + 31);
  STAMP();
  stageA<0>(c); stageA<1>(c);
#pragma unroll
  for (int i = 0; i < 4; ++i)
#pragma unroll
    for (int j = 0; j < 3; ++j) { c.acc[i][j] = (f32x4){0.f, 0.f, 0.f, 0.f}; pin(c.acc[i][j]); }
  c.acch = (f32x4){0.f, 0.f, 0.f, 0.f}; pin(c.acch);
  constexpr int HB = 26624, OQ = STAGE, OK_ = OQ + 8192, OV = OK_ + 9216;
  unsigned e_qp, e_ph, e_p1, e_p2, e_rq, e_v0, e_v1, e_oo, e_sel[4]; int e_base; f32x2 e_bias;
  {
    const unsigned hb = lds0 + (wn >> 1) * HB;
    e_qp = hb + OQ + wm * 4096 + lane * 16 + (wn & 1) * 8;
    const unsigned kp = hb + OK_ + lane * 16 + (wn & 1) * 8;
    const unsigned vp = hb + OV + fr * 64 + 8 * g + (((fr >> 2) ^ wn) & 1) * 32;
    e_ph = wm ? vp : kp; e_p1 = e_ph + (1 + 4 * wm) * 1024; e_p2 = (wm ? kp : vp) + (1 + 4 * wm) * 1024;
    const int hd = wave & 1, tb = (wave >> 1) * 2;
    e_rq = lds0 + hd * HB + OQ + tb * 1024 + lane * 16;
    const unsigned vb = lds0 + hd * HB + OV + (16 * tb + 4 * g + q) * 64 + p * 8;
    e_v0 = vb + (g & 1) * 32; e_v1 = vb + ((g & 1) ^ 1) * 32;
    e_oo = W_O + (tn * 2 + hd) * KBS + (tm * 8 + tb) * 1024 + lane * 16;
    e_base = (fr & ~1) - 4 * g;
    const bool first = tm == 0 && tb == 0;
    e_bias = (f32x2){first && e_base < 0 ? -3.0e38f : 0.f, first && e_base < 2 ? -3.0e38f : 0.f};
    const unsigned s0 = (fr & 1) ? 0x01000c0cu : 0x0c0c0100u, s1 = (fr & 1) ? 0x03020c0cu : 0x0c0c0302u, z = 0x0c0c0c0cu;
    e_sel[0] = e_base >= 0 ? z : s0; e_sel[1] = e_base >= 2 ? z : s1; e_sel[2] = e_base >= 0 ? s0 : z; e_sel[3] = e_base >= 2 ? s1 : z;
    pin(e_qp); pin(e_ph); pin(e_p1); pin(e_p2); pin(e_rq); pin(e_v0); pin(e_v1); pin(e_oo); pin(e_base); pin(e_bias);
    pin(e_sel[0]); pin(e_sel[1]); pin(e_sel[2]); pin(e_sel[3]);
  }
  Fr f0, f1;
  wait_vm<3>(); bar();
  rd_all(c, f0, make_iseq<11>{});
  STAMP();
  stage<0, true, true, 3>(c, f0, f1);
#pragma unroll 1
  for (int s = 1; s < 13; s += 3) { stage<1, true, true, 6>(c, f0, f1); stage<2, true, true, 6>(c, f0, f1); stage<0, true, true, 6>(c, f0, f1); }
  stage<1, true, false, 6>(c, f0, f1);
  stage<2, false, false, 0>(c, f0, f1);
  {
    const int k0 = (b & 3) * 256 + (tid >> 3);
#pragma unroll
    for (int i = 0; i < 4; ++i) {
      P.lw[i] = __builtin_nontemporal_load((const u32x4*)(P.wmod + (size_t)(k0 + 64 * i) * 6144 + 2048 + (b >> 2) * 64 + (tid & 7) * 8));
      P.lc[i] = P.c[k0 + 64 * i];
    }
    GAP();
  }
  stage<0, false, false, 0, false>(c, f0, f1);
  mfma_fence();
  STAMP();
  {
    auto pk4 = [](const f32x4& v) { return (u32x2){pk2(v[0], v[1]), pk2(v[2], v[3])}; };
#pragma unroll
    for (int i = 0; i < 4; ++i) {
      lds_st(e_qp + i * 1024, pk4(c.acc[i][0]));
      lds_st(e_p1 + i * 1024, pk4(c.acc[i][1]));
      lds_st(e_p2 + i * 1024, pk4(c.acc[i][2]));
    }
    lds_st(e_ph, pk4(c.acch));
  }
  wait_lgkm0(); bar();
  {
    bf16x8 qf[2], kf[3]; bf16x4 vt[3][2];
    kf[0] = ldsA(e_rq + 8192); qf[0] = ldsA(e_rq); kf[1] = ldsA(e_rq + 8192 + 1024); qf[1] = ldsA(e_rq + 1024); kf[2] = ldsA(e_rq + 8192 + 2048);
#pragma unroll
    for (int i = 0; i < 3; ++i) { vt[i][0] = ldsT(e_v0 + i * 1024); vt[i][1] = ldsT(e_v1 + i * 1024); }
    GAP();
    const bool par = fr & 1, cur0 = e_base >= 0, cur1 = e_base >= 2;
    const f32x4 z4 = {0.f, 0.f, 0.f, 0.f};
    f32x4 sp[2], sc[2];
#pragma unroll
    for (int i = 0; i < 2; ++i) { sp[i] = mfma(kf[i], qf[i], z4); sc[i] = mfma(kf[i + 1], qf[i], z4); }
    f32x2 l[2]; float mx[2];
#pragma unroll
    for (int i = 0; i < 2; ++i) {
      const float a0 = sp[i][0], a1 = sp[i][1], a2 = sp[i][2], a3 = sp[i][3], b0 = sc[i][0], b1 = sc[i][1], b2 = sc[i][2], b3 = sc[i][3];
      const float x0 = cur0 ? b0 : a0, x1 = cur0 ? b1 : a1, x2 = cur1 ? b2 : a2, x3 = cur1 ? b3 : a3;
      const float y0 = par ? x1 : x0, y1 = par ? x3 : x2;
      l[i] = rbf2(rbf2((f32x2){y0, y1}) * 0.17677669529663687f);
      if (i == 0) l[i] += e_bias;
      mx[i] = fmax1(l[i][0], l[i][1]);
    }
    auto red2 = [](float& u, float& v, auto op) {
      auto r = __builtin_amdgcn_permlane16_swap(__float_as_uint(u), __float_as_uint(v), false, false);
      const float m = op(__uint_as_float(r[0]), __uint_as_float(r[1]));
      auto r2 = __builtin_amdgcn_permlane32_swap(__float_as_uint(m), __float_as_uint(m), false, false);
      const float t = op(__uint_as_float(r2[0]), __uint_as_float(r2[1]));
      auto r3 = __builtin_amdgcn_permlane16_swap(__float_as_uint(t), __float_as_uint(t), false, false);
      u = __uint_as_float(r3[0]); v = __uint_as_float(r3[1]);
    };
    red2(mx[0], mx[1], [](float x, float y) { return fmax1(x, y); });
    float sm[2];
#pragma unroll
    for (int i = 0; i < 2; ++i) {
      const f32x2 t = (l[i] - mx[i]) * 1.4426950408889634f;
      l[i] = (f32x2){__builtin_amdgcn_exp2f(t[0]), __builtin_amdgcn_exp2f(t[1])};
      sm[i] = l[i][0] + l[i][1];
    }
    red2(sm[0], sm[1], [](float x, float y) { return x + y; });
    f32x4 o[2][2];
#pragma unroll
    for (int i = 0; i < 2; ++i) {
      const f32x2 pr = l[i] * __builtin_amdgcn_rcpf(sm[i]);
      const unsigned pp = pk2(pr[0], pr[1]);
      const u32x4 pw = {__builtin_amdgcn_perm(pp, pp, e_sel[0]), __builtin_amdgcn_perm(pp, pp, e_sel[1]), __builtin_amdgcn_perm(pp, pp, e_sel[2]), __builtin_amdgcn_perm(pp, pp, e_sel[3])};
      const bf16x8 pf = __builtin_bit_cast(bf16x8, pw);
      o[i][0] = mfma(cat(vt[i][0], vt[i + 1][0]), pf, z4); o[i][1] = mfma(cat(vt[i][1], vt[i + 1][1]), pf, z4);
    }
#pragma unroll
    for (int i = 0; i < 2; ++i) st16(P, e_oo + i * 1024, __builtin_bit_cast(u32x4, pack8(o[i][0], o[i][1])));
  }
  GAP();
  STAMP();
  P.pend = FL_G1 + tm * 32 + tn;
}

DI u32x4 gated_resid(const f32x4& a0, const f32x4& a1, const f32x2 (&eg)[4], const f32x2 (&er)[4]) {
  u32x4 o;
#pragma unroll
  for (int j = 0; j < 4; ++j) {
    const f32x4& v = j < 2 ? a0 : a1;
    const f32x2 r = (f32x2){v[2 * (j & 1)], v[2 * (j & 1) + 1]} * eg[j] + er[j];
    o[j] = pk2(r[0], r[1]);
  }
  return o;
}

namespace g2 {
constexpr int STAGE = 24576, OFF_B = 16384, NST = 6, WIN = 2 * STAGE;
struct Fr { bf16x8 a[2][2]; bf16x4 wl[2][2], wh[2][2]; };
struct Ctx {
  f32x4 acc[2][2];
  unsigned bA[3], bW0[3], bW1[3];
  unsigned voffA[4], voffB[4];
  const char *sA, *sB;
  unsigned mA, mB;
};
template <int OFF> DI unsigned at(const unsigned (&b)[3]) { return b[OFF / WIN] + OFF % WIN; }
template <int V, int R> DI void rd(Ctx& c, Fr& f) {
  constexpr int t = R / 6, r = R % 6, sl = (V % NST) * STAGE;
  if constexpr (r < 4) {
    constexpr int j = r >> 1, hi = r & 1;
    const unsigned ad = at<sl + t * 4096 + hi * 2048>(j ? c.bW1 : c.bW0);
    if constexpr (hi) f.wh[t][j] = ldsT(ad); else f.wl[t][j] = ldsT(ad);
  } else f.a[t][r - 4] = ldsA(at<sl + t * 8192 + (r - 4) * 1024>(c.bA));
}
template <int V> DI void mB(Ctx& c) { m0_add<(V % NST) * STAGE>(c.mB); }
template <int V> DI void mA(Ctx& c) { m0_add<(V % NST) * STAGE>(c.mA); }
template <int V> DI void ldB(Ctx& c) { glds<0>(c.voffB[V & 3], c.sB); if constexpr ((V & 3) == 3) c.sB += 4 * 64 * D_ * 2; }
template <int V, int H> DI void ldA(Ctx& c) { glds<H * 1024, true>(c.voffA[V & 3], c.sA); if constexpr ((V & 3) == 3 && H) c.sA += 8 * KBS; }
template <int V, int A0, int A1, int VM, int M> DI void slot(Ctx& c, const Fr& cur, Fr& nxt) {
  constexpr int t = M >> 2, i = (M >> 1) & 1, j = M & 1;
  if constexpr (M == 1 && V < 15) { wait_vm<VM>(); bar(); GAP(); }
  mfma_ip(c.acc[i][j], cat(cur.wl[t][j], cur.wh[t][j]), cur.a[t][i]);
  GAP();
  if constexpr (A1 > A0) {
    if constexpr (M == 0) mA<A0>(c);
    if constexpr (M == 1) ldA<A0, 0>(c);
    if constexpr (M == 2) { ldA<A0, 1>(c); if constexpr (A1 > A0 + 1) mA<A0 + 1>(c); else mB<V + 6>(c); }
    if constexpr (A1 > A0 + 1) {
      if constexpr (M == 3) ldA<A0 + 1, 0>(c);
      if constexpr (M == 4) { ldA<A0 + 1, 1>(c); mB<V + 6>(c); }
      if constexpr (M == 5) ldB<V + 6>(c);
    } else if constexpr (M == 3) ldB<V + 6>(c);
  }
  if constexpr (V < 15 && M >= 1 && M <= 6) { rd<V + 1, 2 * (M - 1)>(c, nxt); rd<V + 1, 2 * (M - 1) + 1>(c, nxt); }
  GAP();
}
template <int V, int A0, int A1, int VM, int... Ms> DI void step_(Ctx& c, const Fr& cur, Fr& nxt, iseq<Ms...>) {
  wait_lgkm0();
  GAP();
  (slot<V, A0, A1, VM, Ms>(c, cur, nxt), ...);
}
template <int V, int A0, int A1, int VM> DI void step(Ctx& c, Fr& f0, Fr& f1) { step_<V, A0, A1, VM>(c, (V & 1) ? f1 : f0, (V & 1) ? f0 : f1, make_iseq<8>{}); }
template <int... Rs> DI void rd_all(Ctx& c, Fr& f, iseq<Rs...>) { (rd<0, Rs>(c, f), ...); }
template <int... Vs> DI void preB(Ctx& c, iseq<Vs...>) { ((mB<Vs>(c), set_nop(), ldB<Vs>(c)), ...); }
template <int... Vs> DI void preA(Ctx& c, iseq<Vs...>) { ((mA<Vs>(c), set_nop(), ldA<Vs, 0>(c), ldA<Vs, 1>(c)), ...); }
}

DI void phase2(Par& P) {
  using namespace g2;
  const int tid = opaque_tid(), lane = tid & 63, b = opaque_bid();
  const int wave = __builtin_amdgcn_readfirstlane(tid >> 6);
  const unsigned lds0 = P.lds0;
  const int wm = wave >> 1, wn = wave & 1;
  int tm, tn; map_g2(b, tm, tn);
  const int fr = lane & 15, g = lane >> 4, q = (lane >> 2) & 3, p = lane & 3;
  Ctx c;
  {
    const int sbk = 4 * wave + (lane >> 4), kq = sbk >> 1, cb = sbk & 1, ch = (lane & 3) ^ (2 * (kq & 1));
    const unsigned vo = (unsigned)(((4 * kq + q) * D_ + tn * 64 + cb * 32 + 8 * ch) * 2);
#pragma unroll
    for (int s = 0; s < 4; ++s) { c.voffB[s] = vo + s * (64 * D_ * 2); pin(c.voffB[s]); }
  }
  c.sB = (const char*)P.wo;
  c.mB = lds0 + OFF_B + wave * 1024;
#pragma unroll
  for (int s = 0; s < 4; ++s) { c.voffA[s] = lane * 16 + s * 2 * KBS; pin(c.voffA[s]); }
  c.sA = P.ws + W_O + (size_t)(wave >> 2) * KBS + (tm * 8 + 2 * (wave & 3)) * 1024;
  c.mA = lds0 + wave * 2048;
#pragma unroll
  for (int w = 0; w < 3; ++w) {
    c.bA[w] = lds0 + w * WIN + wm * 2048 + lane * 16;
    const unsigned bw = lds0 + w * WIN + OFF_B + g * 512 + wn * 256 + q * 64 + p * 8;
    c.bW0[w] = bw + (g & 1) * 32; c.bW1[w] = bw + ((g & 1) ^ 1) * 32;
    pin(c.bA[w]); pin(c.bW0[w]); pin(c.bW1[w]);
  }
#pragma unroll
  for (int i = 0; i < 2; ++i)
#pragma unroll
    for (int j = 0; j < 2; ++j) { c.acc[i][j] = (f32x4){0.f, 0.f, 0.f, 0.f}; pin(c.acc[i][j]); }
  publish(P, P.pend);
  STAMP();
  preB(c, make_iseq<NST>{});
  {
    f32x2 acc[4];
#pragma unroll
    for (int j = 0; j < 4; ++j) acc[j] = (f32x2){0.f, 0.f};
#pragma unroll
    for (int i = 0; i < 4; ++i) {
      const float sv = silu_bf(P.lc[i]);
#pragma unroll
      for (int j = 0; j < 4; ++j) acc[j] += unpk(P.lw[i][j]) * sv;
    }
    const unsigned rb = lds0 + PERS + 2048;
    lds_st(rb + wave * 256 + (lane & 7) * 32 + g * 8, red8(acc));
    wait_lgkm0(); bar();
    if (wave == 7) {
      float sum = 0.f;
#pragma unroll
      for (int w = 0; w < 8; ++w) sum += lds_ld<float>(rb + w * 256 + lane * 4);
      __builtin_amdgcn_raw_buffer_store_b32(min(__float_as_uint(sum), POISON - 1), P.rs, W_PART2 + (b & 3) * 16384 + ((b >> 2) * 64 + lane) * 4, 0, AUX_SC1);
    }
  }
  await(P, wave, lane, FL_G1 + tm * 32, FL_G1 + tm * 32 + 15);
  STAMP();
  preA(c, make_iseq<3>{});
  Fr f0, f1;
  wait_vm<4>(); bar();
  rd_all(c, f0, make_iseq<12>{});
  STAMP();
  step<0, 3, 5, 2>(c, f0, f1); step<1, 5, 7, 5>(c, f0, f1); step<2, 7, 9, 8>(c, f0, f1); step<3, 9, 10, 11>(c, f0, f1);
  step<4, 10, 11, 11>(c, f0, f1); step<5, 11, 12, 12>(c, f0, f1); step<6, 12, 13, 12>(c, f0, f1); step<7, 13, 14, 12>(c, f0, f1);
  step<8, 14, 15, 12>(c, f0, f1); step<9, 15, 16, 12>(c, f0, f1);
  step<10, 0, 0, 12>(c, f0, f1);
  const unsigned eb = lds0 + 4 * STAGE + wave * 2048;
  set_m0(eb);
  {
    const char* rp = (const char*)P.x + ((size_t)(tm * 128 + 32 * wm) * D_ + tn * 64 + wn * 32) * 2;
    const unsigned vo = (lane >> 2) * (D_ * 2) + (lane & 3) * 16;
    glds<0>(vo, rp); glds<1024>(vo, rp + 16 * D_ * 2 - 1024);
  }
  u32x4 mp[4]; unsigned mo = 0;
  if (wave == 1 || wave == 2) {
    int tm4, tn4, kh; map_g4(b, tm4, tn4, kh);
    const int u = min(tid - 64, 79);
    mo = W_PART2 + (u < 48 ? (u >> 4) * 1024 + tn * 64 + (u & 15) * 4 : 3072 + tn4 * 128 + (u - 48) * 4) * 4;
#pragma unroll
    for (int kq = 0; kq < 4; ++kq) mp[kq] = ld16(P, mo + kq * 16384);
  }
  GAP();
  step<11, 0, 0, 11>(c, f0, f1); step<12, 0, 0, 8>(c, f0, f1); step<13, 0, 0, 5>(c, f0, f1);
  if (wave == 1 || wave == 2) {
#pragma unroll 1
    for (unsigned spins = 0; spins < SPIN_CAP && __any(max(max(umax4(mp[0]), umax4(mp[1])), max(umax4(mp[2]), umax4(mp[3]))) == POISON); ++spins) {
      asm volatile("" ::: "memory");
#pragma unroll
      for (int kq = 0; kq < 4; ++kq) mp[kq] = ld16(P, mo + kq * 16384);
    }
    const f32x4 a = (__builtin_bit_cast(f32x4, mp[0]) + __builtin_bit_cast(f32x4, mp[1])) + (__builtin_bit_cast(f32x4, mp[2]) + __builtin_bit_cast(f32x4, mp[3]));
    lds_st(lds0 + PERS + min(tid - 64, 79) * 8, (u32x2){pk2(a[0], a[1]), pk2(a[2], a[3])});
  }
  step<14, 0, 0, 2>(c, f0, f1);
  wait_vm<0>();
  f32x2 eg[4], er[2][4]; u32x2 esh[2], esc[2];
#pragma unroll
  for (int j = 0; j < 2; ++j) {
    const unsigned pb = lds0 + PERS + wn * 64 + 32 * j + 8 * g;
    const u32x2 gv = lds_ld<u32x2>(pb); esh[j] = lds_ld<u32x2>(pb + 128); esc[j] = lds_ld<u32x2>(pb + 256);
    const u32x2 a = lds_ld<u32x2>(eb + j * 1024 + fr * 64 + 8 * g), bb = lds_ld<u32x2>(eb + j * 1024 + fr * 64 + 32 + 8 * g);
    eg[2 * j] = unpk(gv[0]); eg[2 * j + 1] = unpk(gv[1]);
    er[j][0] = unpk(a[0]); er[j][1] = unpk(a[1]); er[j][2] = unpk(bb[0]); er[j][3] = unpk(bb[1]);
  }
#pragma unroll
  for (int j = 0; j < 4; ++j) { pin(eg[j]); pin(er[0][j]); pin(er[1][j]); }
  const unsigned sb = W_STATS + tm * 32768;
  unsigned so = sb + (tn * 2 + wn) * 1024 + (32 * wm + (lane & 31)) * 8, ob = (tn * 2 + wn) * KBS + (tm * 8 + 2 * wm) * 1024 + lane * 16;
  pin(so); pin(ob);
  step<15, 0, 0, 0>(c, f0, f1);
  mfma_fence();
  STAMP();
  u32x4 xo[2]; f32x2 d[2][4];
  float s1[2];
#pragma unroll
  for (int i = 0; i < 2; ++i) {
    xo[i] = gated_resid(c.acc[i][0], c.acc[i][1], eg, er[i]);
#pragma unroll
    for (int j = 0; j < 4; ++j) d[i][j] = unpk(xo[i][j]);
    const f32x2 s2 = (d[i][0] + d[i][1]) + (d[i][2] + d[i][3]);
    s1[i] = s2[0] + s2[1];
  }
  const float mug = add2(s1[0], s1[1]) * (1.f / 32.f);
  const f32x2 mu = spread2(mug);
#pragma unroll
  for (int i = 0; i < 2; ++i) {
    f32x2 q2 = {0.f, 0.f};
#pragma unroll
    for (int j = 0; j < 4; ++j) { d[i][j] -= mu[i]; q2 += d[i][j] * d[i][j]; }
    s1[i] = q2[0] + q2[1];
  }
  st8(P, so, (u32x2){min(__float_as_uint(mug), POISON - 1), min(__float_as_uint(add2(s1[0], s1[1])), POISON - 1)});
  GAP();
  st16(P, W_X1 + ob, xo[0]); st16(P, W_X1 + ob + 1024, xo[1]);
  f32x2 sc1[4], sh[4];
#pragma unroll
  for (int j = 0; j < 4; ++j) { sc1[j] = rbf2(unpk(esc[j >> 1][j & 1]) + 1.f); sh[j] = unpk(esh[j >> 1][j & 1]); pin(sc1[j]); pin(sh[j]); }
  unsigned po = sb + (lane >> 3) * 1024 + wave * 128 + (lane & 7) * 16, tw = lds0 + 5 * STAGE + wave * 128 + (lane & 7) * 16 + (g & 1) * 8, tr = lds0 + 5 * STAGE + (32 * wm + fr) * 8;
  pin(po); pin(tw); pin(tr);
  u32x4 pv[4];
  __builtin_amdgcn_s_sleep(24);
#pragma unroll 1
  for (unsigned spins = 0; spins < SPIN_CAP; ++spins) {
    asm volatile("" ::: "memory");
#pragma unroll
    for (int t = 0; t < 4; ++t) pv[t] = __builtin_amdgcn_raw_buffer_load_b128(P.rs, po, t * 8192, AUX_SC1);
    if (!__any(max(max(umax4(pv[0]), umax4(pv[1])), max(umax4(pv[2]), umax4(pv[3]))) == POISON)) break;
    __builtin_amdgcn_s_sleep(2);
  }
  STAMP();
  {
    f32x2 um[4], s2 = {0.f, 0.f}, q2 = {0.f, 0.f};
#pragma unroll
    for (int t = 0; t < 4; ++t) { um[t] = (f32x2){__uint_as_float(pv[t][0]), __uint_as_float(pv[t][2])}; s2 += um[t]; q2 += (f32x2){__uint_as_float(pv[t][1]), __uint_as_float(pv[t][3])}; }
    const float mg = dpp_add<0x128>(add2(s2[0], s2[1])) * (1.f / 32.f);
    const f32x2 mean = spread2(mg);
    f32x2 e2 = {0.f, 0.f};
#pragma unroll
    for (int t = 0; t < 4; ++t) { const f32x2 dd = um[t] - mean; e2 += dd * dd; }
    q2 += e2 * 32.f;
    const float vg = dpp_add<0x128>(add2(q2[0], q2[1]));
    lds_st(tw, (f32x2){rbf(mg), ln_rstd(vg * (1.f / 1024.f))});
  }
  wait_lgkm0(); bar();
#pragma unroll
  for (int i = 0; i < 2; ++i) {
    const f32x2 st = lds_ld<f32x2>(tr + i * 128);
    const float dm = (mu[i] - st[0]) * st[1];
    u32x4 o;
#pragma unroll
    for (int j = 0; j < 4; ++j) {
      const f32x2 y = (d[i][j] * st[1] + dm) * sc1[j] + sh[j];
      o[j] = pk2(y[0], y[1]);
    }
    st16(P, W_H2 + ob + i * 1024, o);
  }
  GAP();
  STAMP();
  P.pend = FL_G2 + (tm >> 1) * 32 + (tm & 1) * 16 + tn;
}

namespace g3 {
constexpr int SLAB = 30720, OFF_W1 = 16384, WSTR = 7168, NST = 5, PB = 4, WIN = 2 * SLAB;
struct Fr { bf16x8 a[4]; bf16x4 wl[7], wh[7]; };
struct Ctx {
  f32x4 acc[4][7];
  unsigned bA[3], bW[4][3];
  unsigned voffA, voffB0, voffB1;
  const char *sA, *sB;
  unsigned mA, mB;
};
template <int OFF> DI unsigned at(const unsigned (&b)[3]) { return b[OFF / WIN] + OFF % WIN; }
template <int SL, int R> DI void rd(Ctx& c, Fr& f) {
  if constexpr (R < 4) f.a[R] = ldsA(at<SL * SLAB + R * 1024>(c.bA));
  else {
    constexpr int j = (R - 4) >> 1, hi = (R - 4) & 1;
    unsigned ad;
    if constexpr (j == 6) ad = at<SL * SLAB + hi * 512>(c.bW[3]);
    else ad = at<SL * SLAB + (j >= 3 ? WSTR : 0) + hi * 3072>(c.bW[j % 3]);
    if constexpr (hi) f.wh[j] = ldsT(ad); else f.wl[j] = ldsT(ad);
  }
}
template <int SL> DI void mB(Ctx& c) { m0_add<SL * SLAB>(c.mB); }
template <int SL> DI void mA(Ctx& c) { m0_add<SL * SLAB>(c.mA); }
template <int H> DI void ldB(Ctx& c) { glds<H * 1024>(H ? c.voffB1 : c.voffB0, c.sB); if constexpr (H) c.sB += 32 * F_ * 2; }
template <int H> DI void ldA(Ctx& c) { glds<H * 1024, true>(c.voffA, c.sA); if constexpr (H) c.sA += KBS; }
template <int SL, bool FIRST, int ND, int VM, bool READ, int M> DI void slot(Ctx& c, const Fr& cur, Fr& nxt) {
  constexpr int i = M / 7, j = M % 7;
  if constexpr (M == PB && READ) { wait_vm<VM>(); bar(); GAP(); }
  if constexpr (FIRST) asm volatile("v_mfma_f32_16x16x32_bf16 %0, %1, %2, 0" : "=&v"(c.acc[i][j]) : "v"(cat(cur.wl[j], cur.wh[j])), "v"(cur.a[i]));
  else mfma_ip(c.acc[i][j], cat(cur.wl[j], cur.wh[j]), cur.a[i]);
  GAP();
  if constexpr (READ && M >= PB && M < PB + 18) rd<(SL + 1) % NST, M - PB>(c, nxt);
  if constexpr (ND == 1) {
    if constexpr (M == 6) mB<SL>(c);
    if constexpr (M == 7) ldB<0>(c);
    if constexpr (M == 12) { ldB<1>(c); mA<SL>(c); }
    if constexpr (M == 17) ldA<0>(c);
    if constexpr (M == 22) ldA<1>(c);
  }
  if constexpr (ND == 2) {
    if constexpr (M == 0) mA<3>(c);
    if constexpr (M == 1) ldA<0>(c);
    if constexpr (M == 5) { ldA<1>(c); mA<4>(c); }
    if constexpr (M == 8) ldA<0>(c);
    if constexpr (M == 11) { ldA<1>(c); mB<0>(c); }
    if constexpr (M == 14) ldB<0>(c);
    if constexpr (M == 17) { ldB<1>(c); mA<0>(c); }
    if constexpr (M == 20) ldA<0>(c);
    if constexpr (M == 23) ldA<1>(c);
  }
  GAP();
}
template <int SL, bool FIRST, int ND, int VM, bool READ, int... Ms> DI void step_(Ctx& c, const Fr& cur, Fr& nxt, iseq<Ms...>) {
  wait_lgkm0();
  GAP();
  (slot<SL, FIRST, ND, VM, READ, Ms>(c, cur, nxt), ...);
}
constexpr int vmc(int v) { return v == 0 ? 3 : v == 1 ? 8 : v == 2 ? 10 : v <= 27 ? 12 : 4 * (30 - v); }
template <int V> DI void step(Ctx& c, Fr& f0, Fr& f1) {
  step_<V % NST, V == 0, (V == 0 ? 2 : V <= 26 ? 1 : 0), (V < 31 ? vmc(V) : 0), (V < 31)>(c, (V & 1) ? f1 : f0, (V & 1) ? f0 : f1, make_iseq<28>{});
}
template <int V0, int... Vs> DI void steps(Ctx& c, Fr& f0, Fr& f1, iseq<Vs...>) { (step<V0 + Vs>(c, f0, f1), ...); }
template <int... Rs> DI void rd_all(Ctx& c, Fr& f, iseq<Rs...>) { (rd<0, Rs>(c, f), ...); }
template <int... Vs> DI void preB(Ctx& c, iseq<Vs...>) { ((mB<Vs>(c), set_nop(), ldB<0>(c), ldB<1>(c)), ...); }
template <int... Vs> DI void preA(Ctx& c, iseq<Vs...>) { ((mA<Vs>(c), set_nop(), ldA<0>(c), ldA<1>(c)), ...); }
}

DI void phase3(Par& P) {
  using namespace g3;
  int tid = opaque_tid(), lane = tid & 63; const int b = opaque_bid();
  const int wave = __builtin_amdgcn_readfirstlane(tid >> 6);
  const unsigned lds0 = P.lds0;
  const int wm = wave >> 1, wn = wave & 1;
  int tm, tn; map_g3(b, tm, tn);
  const int fbase = 112 * tn + ((tn & 1) ? 16 : 0), f6 = 112 * tn + ((tn & 1) ? 0 : 96);
  Ctx c;
  {
    const int p0 = min(2 * (wave & 3), 5), up = wave >> 2;
    unsigned vo[2];
#pragma unroll
    for (int t = 0; t < 2; ++t) {
      const int sbk = 4 * (p0 + t) + (lane >> 4), kq = sbk / 3, cb = sbk - 3 * kq, q = (lane >> 2) & 3, ch = (lane & 3) ^ (2 * (kq & 1));
      vo[t] = (unsigned)(((4 * kq + q) * F_ + fbase + 32 * cb + 8 * ch) * 2);
    }
    if (p0 == 5) vo[1] = (unsigned)(((lane >> 1) * F_ + f6 + 8 * ((lane & 1) ^ up)) * 2);
    c.voffB0 = vo[0] + 1024; c.voffB1 = vo[1];
    const uintptr_t w1 = (uintptr_t)P.w1, w3 = (uintptr_t)P.w3;
    c.sB = (const char*)((up ? w3 : w1) - 1024);
    c.mB = lds0 + OFF_W1 + up * WSTR + p0 * 1024;
  }
  c.voffA = lane * 16;
  c.sA = P.ws + W_H2 + (size_t)(tm * 16 + 2 * wave) * 1024;
  c.mA = lds0 + 2 * wave * 1024;
  {
    const int g = lane >> 4, q = (lane >> 2) & 3, p = lane & 3;
    const unsigned bw = lds0 + OFF_W1 + g * 768 + q * 64 + p * 8;
#pragma unroll
    for (int w = 0; w < 3; ++w) {
      c.bA[w] = lds0 + w * WIN + wm * 4096 + lane * 16;
      c.bW[0][w] = bw + w * WIN + wn * 512 + (g & 1) * 32;
      c.bW[1][w] = bw + w * WIN + wn * 512 + ((g & 1) ^ 1) * 32;
      c.bW[2][w] = bw + w * WIN + 256 + ((wn ^ g) & 1) * 32;
      c.bW[3][w] = lds0 + w * WIN + OFF_W1 + 6144 + (p >> 1) * WSTR + (4 * g + q) * 32 + ((wn ^ (p >> 1)) * 16) + (p & 1) * 8;
      pin(c.bA[w]); pin(c.bW[0][w]); pin(c.bW[1][w]); pin(c.bW[2][w]); pin(c.bW[3][w]);
    }
    pin(c.voffA); pin(c.voffB0); pin(c.voffB1);
  }
  publish(P, P.pend);
  STAMP();
  preB(c, make_iseq<NST>{});
  await(P, wave, lane, FL_G2 + tm * 32, FL_G2 + tm * 32 + 31);
  STAMP();
  preA(c, make_iseq<3>{});
  Fr f0, f1;
  wait_vm<4>(); bar();
  rd_all(c, f0, make_iseq<18>{});
  STAMP();
  steps<0>(c, f0, f1, make_iseq<3>{});
  auto warm = [&](int w) {
    if (w == wave) {
      const int ln = opaque_tid() & 63;
      const uintptr_t a0 = (uintptr_t)P.w1 + 21504 * 128, a1 = (uintptr_t)P.w3 + 21504 * 128, a2 = (uintptr_t)P.w2;
      if (wave < 5) touch_line((const char*)((b & 1 ? a1 : a0) + (size_t)(wave * 7168 + (b >> 1) * 56 + min(ln, 55)) * 128), lds0 + PERS + 1024);
      else {
        touch_line((const char*)(a2 + (size_t)((wave - 5) * 16384 + b * 64 + ln) * 128), lds0 + PERS + 1024);
        if (wave == 7) touch_line((const char*)(a2 + (size_t)(49152 + b * 32 + (ln & 31)) * 128), lds0 + PERS + 1024);
      }
    }
  };
#pragma unroll 1
  for (int it = 0; it < 2; ++it) {
    warm(5 * it); steps<3>(c, f0, f1, make_iseq<2>{}); warm(5 * it + 1); steps<5>(c, f0, f1, make_iseq<2>{}); warm(5 * it + 2); steps<7>(c, f0, f1, make_iseq<2>{});
    warm(5 * it + 3); steps<9>(c, f0, f1, make_iseq<2>{}); warm(5 * it + 4); steps<11>(c, f0, f1, make_iseq<2>{});
  }
  steps<23>(c, f0, f1, make_iseq<9>{});
  mfma_fence();
  STAMP();
  tid = opaque_tid(); lane = tid & 63;
  auto sw2 = [](f32x2 gt, f32x2 up) {
    const f32x2 a = gt * -1.4426950408889634f;
    f32x2 e = {__builtin_amdgcn_exp2f(a[0]), __builtin_amdgcn_exp2f(a[1])};
    e = e + 1.f;
    const f32x2 r = {__builtin_amdgcn_rcpf(e[0]), __builtin_amdgcn_rcpf(e[1])};
    const f32x2 o = (gt * up) * r;
    return pk2(o[0], o[1]);
  };
  auto sw4 = [&](const f32x4& gt, const f32x4& up) {
    return (u32x2){sw2((f32x2){gt[0], gt[1]}, (f32x2){up[0], up[1]}), sw2((f32x2){gt[2], gt[3]}, (f32x2){up[2], up[3]})};
  };
  const bool godd = lane & 16;
  const unsigned hb = W_HH + ((fbase >> 4) * 64 + tm * 8 + wm * 2) * 1024 + lane * 16;
#pragma unroll
  for (int ip = 0; ip < 2; ++ip)
#pragma unroll
    for (int j = 0; j < 3; ++j) {
      const u32x2 o0 = sw4(c.acc[2 * ip][j], c.acc[2 * ip][3 + j]), o1 = sw4(c.acc[2 * ip + 1][j], c.acc[2 * ip + 1][3 + j]);
      st16(P, hb + (j == 2 ? 2 + wn : 4 * wn + j) * 65536 + ip * 1024, godd ? (u32x4){o1[0], o1[1], o0[0], o0[1]} : (u32x4){o0[0], o0[1], o1[0], o1[1]});
    }
  {
    u32x2 o[2];
#pragma unroll
    for (int i = 0; i < 2; ++i) {
      f32x4 gt, up;
#pragma unroll
      for (int e = 0; e < 4; ++e) {
        auto r = __builtin_amdgcn_permlane32_swap(__float_as_uint(c.acc[i][6][e]), __float_as_uint(c.acc[i + 2][6][e]), false, false);
        gt[e] = __uint_as_float(r[0]); up[e] = __uint_as_float(r[1]);
      }
      o[i] = sw4(gt, up);
    }
    st16(P, W_HH + ((f6 >> 4) * 64 + tm * 8 + wm * 2 + (lane >> 5)) * 1024 + (wn * 32 + (lane & 31)) * 16, godd ? (u32x4){o[1][0], o[1][1], o[0][0], o[0][1]} : (u32x4){o[0][0], o[0][1], o[1][0], o[1][1]});
  }
  GAP();
  STAMP();
  P.pend = FL_G3 + tm * 32 + tn;
}

namespace g4 {
constexpr int STAGE = 32768, OFF_B = 16384, NST = 4, DD = 4, NF = 28 - DD, NH = 2 * DD, NV = NF + NH, VFLAG = NF + 1, VPOLL = VFLAG + 3;
constexpr int NXS = 4;
struct Fr { bf16x4 al[2][4], ah[2][4], wl[2][2], wh[2][2]; };
struct Ctx {
  f32x4 acc[4][2];
  unsigned bA[4][2], bW[2][2];
  unsigned voffA[4], voffB0[4], voffB1[4];
  const char *sA, *sB;
  unsigned mA, mB;
};
template <int OFF> DI unsigned at(const unsigned (&b)[2]) { return b[OFF / 65536] + OFF % 65536; }
constexpr int nd(int u) { return u >= NV ? 0 : u >= NF ? 3 : 4; }
constexpr int nx(int u) { return u == NF - 1 ? NXS : u == VFLAG ? 1 : u == VPOLL ? 1 : u == VPOLL + 1 ? NXS : 0; }
constexpr int vmc(int v) { return nx(v - 3) + nd(v + 2) + nx(v - 2) + nd(v + 3) + nx(v - 1); }
template <int SL, int T, int R> DI void rd(Ctx& c, Fr& f) {
  constexpr int j = R >> 1, hi = R & 1, off = SL * STAGE + T * 8192 + hi * 4096;
  if constexpr (R < 4) {
    if constexpr (hi) f.wh[T][j] = ldsT(at<off>(c.bW[j])); else f.wl[T][j] = ldsT(at<off>(c.bW[j]));
  } else {
    const bf16x4 v = lds_ld<bf16x4>(at<off>(c.bA[j - 2]));
    if constexpr (hi) f.ah[T][j - 2] = v; else f.al[T][j - 2] = v;
  }
}
template <int SL, bool FULL, int Dm> DI void dma(Ctx& c) {
  if constexpr (Dm == 0) m0_add<SL * STAGE>(c.mB);
  else if constexpr (Dm == 1) glds<0>(c.voffB0[SL], c.sB);
  else if constexpr (Dm == 2) { glds<1024>(c.voffB1[SL], c.sB); m0_add<SL * STAGE>(c.mA); if constexpr (SL == 3) c.sB += 4 * 64 * D_ * 2; }
  else {
    glds<(Dm - 3) * 1024, true>(c.voffA[SL], c.sA);
    if constexpr (SL == 3 && (Dm == 4 || !FULL)) c.sA += 8 * KBS;
  }
}
template <int SL, bool CF, bool NXF, int ND, int VM, bool RD, int M> DI void slot(Ctx& c, Fr& cur, Fr& nxt) {
  constexpr int NM = CF ? 16 : 8, T = M / (NM / 2), i = (M % (NM / 2)) >> 1, j = M & 1;
  if constexpr (M == 1 && RD) { wait_vm<VM>(); bar(); GAP(); }
  mfma_ip(c.acc[i][j], cat(cur.wl[T][j], cur.wh[T][j]), cat(cur.al[T][i], cur.ah[T][i]));
  GAP();
  if constexpr (RD && M >= 1) {
    constexpr int NR = NXF ? 24 : 16, per = CF ? 2 : 3, r0 = per * (M - 1);
    auto one = [&](auto rc) {
      constexpr int r = decltype(rc)::value;
      if constexpr (r < NR) { rd<(SL + 1) % NST, r / (NR / 2), r % (NR / 2)>(c, nxt); GAP(); }
    };
    one(std::integral_constant<int, r0>{}); one(std::integral_constant<int, r0 + 1>{});
    if constexpr (per == 3) one(std::integral_constant<int, r0 + 2>{});
  }
  if constexpr (ND > 0 && M == 0) dma<SL, ND == 4, 0>(c);
  if constexpr (CF && M >= 2 && (M - 2) % 4 == 0 && (M - 2) / 4 < ND) dma<SL, ND == 4, (M - 2) / 4 + 1>(c);
  if constexpr (!CF && M >= 1 && (M - 1) % 2 == 0 && (M - 1) / 2 < ND) dma<SL, ND == 4, (M - 1) / 2 + 1>(c);
  GAP();
}
template <int SL, bool CF, bool NXF, int ND, int VM, bool RD, int... Ms> DI void step_(Ctx& c, Fr& cur, Fr& nxt, iseq<Ms...>) {
  wait_lgkm0();
  GAP();
  (slot<SL, CF, NXF, ND, VM, RD, Ms>(c, cur, nxt), ...);
}
template <int V, int VM = vmc(V)> DI void step(Ctx& c, Fr& f0, Fr& f1) {
  step_<V % NST, (V < NF), (V + 1 < NF), nd(V + 4), VM, (V + 1 < NV)>(c, (V & 1) ? f1 : f0, (V & 1) ? f0 : f1, make_iseq<(V < NF ? 16 : 8)>{});
}
template <int V0, int... Vs> DI void steps(Ctx& c, Fr& f0, Fr& f1, iseq<Vs...>) { (step<V0 + Vs>(c, f0, f1), ...); }
template <int... Rs> DI void rd_all(Ctx& c, Fr& f, iseq<Rs...>) { ((rd<0, Rs / 12, Rs % 12>(c, f), GAP()), ...); }
template <int... Vs> DI void preB(Ctx& c, iseq<Vs...>) { ((dma<Vs, true, 0>(c), set_nop(), dma<Vs, true, 1>(c), dma<Vs, true, 2>(c)), ...); }
template <int... Vs> DI void preA(Ctx& c, iseq<Vs...>) { ((m0_add<Vs * STAGE>(c.mA), set_nop(), dma<Vs, true, 3>(c), dma<Vs, true, 4>(c)), ...); }
}

DI void phase4(Par& P) {
  using namespace g4;
  const int tid = opaque_tid(), lane = tid & 63, b = opaque_bid();
  const int wave = __builtin_amdgcn_readfirstlane(tid >> 6);
  const unsigned lds0 = P.lds0;
  const int wm = wave >> 2, wn = wave & 3;
  int tm, tn, kh; map_g4(b, tm, tn, kh);
  const int fr = lane & 15, g = lane >> 4, q = (lane >> 2) & 3, p = lane & 3;
  Ctx c;
#pragma unroll
  for (int s = 0; s < 4; ++s) {
    c.voffB0[s] = (unsigned)(((64 * s + 8 * wave + q) * D_ + tn * 128 + g * 32 + 8 * p) * 2 + 1024);
    c.voffB1[s] = (unsigned)(((64 * s + 8 * wave + 4 + q) * D_ + tn * 128 + g * 32 + 8 * (p ^ 2)) * 2);
    c.voffA[s] = lane * 16 + s * 2 * KBS;
    pin(c.voffB0[s]); pin(c.voffB1[s]); pin(c.voffA[s]);
  }
  c.sB = (const char*)P.w2 + (size_t)kh * (64 * NV) * D_ * 2 - 1024;
  c.mB = lds0 + OFF_B + wave * 2048;
  c.mA = lds0 + wave * 2048;
  const char* sA0 = P.ws + W_HH + (size_t)(wave >> 1) * 65536 + (tm * 4 + 2 * (wave & 1)) * 1024;
  c.sA = sA0 + (size_t)kh * NV * 2 * KBS;
#pragma unroll
  for (int w = 0; w < 2; ++w) {
#pragma unroll
    for (int i = 0; i < 4; ++i) { c.bA[i][w] = lds0 + w * 65536 + (2 * wm + (kh ^ (i >> 1))) * 1024 + lane * 16 + ((i ^ g) & 1) * 8; pin(c.bA[i][w]); }
    const unsigned bw = lds0 + w * 65536 + OFF_B + g * 1024 + wn * 256 + q * 64 + p * 8;
    c.bW[0][w] = bw + (g & 1) * 32; c.bW[1][w] = bw + ((g & 1) ^ 1) * 32;
    pin(c.bW[0][w]); pin(c.bW[1][w]);
  }
#pragma unroll
  for (int i = 0; i < 4; ++i)
#pragma unroll
    for (int j = 0; j < 2; ++j) { c.acc[i][j] = (f32x4){0.f, 0.f, 0.f, 0.f}; pin(c.acc[i][j]); }
  publish(P, P.pend);
  STAMP();
  preB(c, make_iseq<NST>{});
  await(P, wave, lane, FL_G3 + (tm >> 1) * 32, FL_G3 + (tm >> 1) * 32 + 31);
  STAMP();
  preA(c, make_iseq<NST>{});
  const unsigned eb = lds0 + NST * STAGE + wave * 2048;
  const int mb0 = tm * 8 + 4 * wm + 2 * kh;
  set_m0(eb);
  {
    const char* rp = P.ws + W_X1 + (size_t)(tn * 4 + wn) * KBS + mb0 * 1024;
    glds<0, true>(c.voffA[0], rp); glds<1024, true>(c.voffA[0], rp);
  }
  Fr f0, f1;
  wait_vm<8>(); bar();
  rd_all(c, f0, make_iseq<24>{});
  STAMP();
  step<0, 6>(c, f0, f1); step<1, 8>(c, f0, f1); step<2, 10>(c, f0, f1); step<3>(c, f0, f1);
  static_assert(NF % 4 == 0, "loop below");
#pragma unroll 1
  for (int s = 4; s < NF - 4; s += 4) steps<4>(c, f0, f1, make_iseq<4>{});
  c.sB = (const char*)P.w2 + (size_t)(64 * NF) * D_ * 2 - 1024;
  c.sA = sA0 + (size_t)NF * 2 * KBS + kh * 1024; c.mA += kh * 1024;
  steps<NF - 4>(c, f0, f1, make_iseq<4>{});
  mfma_fence();
  const unsigned xo = W_XCH + (tm * 8 + tn) * 65536 + wave * 4096 + lane * 16;
#pragma unroll
  for (int u = 0; u < NXS; ++u) st16(P, xo + (kh ^ 1) * 32768 + u * 1024, __builtin_bit_cast(u32x4, c.acc[2 + (u >> 1)][u & 1]));
  GAP();
  STAMP();
  steps<NF>(c, f0, f1, make_iseq<VFLAG - NF + 1>{});
  unsigned* fl = (unsigned*)(P.ws + W_FLAGS) + FL_G4 + ((tm * 8 + tn) * 16 + wave) * 32;
  static_assert(VFLAG == NF + 1, "count below");
  wait_vm<nd(NF + 4) + nd(NF + 5)>();
  __hip_atomic_store(fl + (kh ^ 1) * 256, 1u, __ATOMIC_RELAXED, __HIP_MEMORY_SCOPE_AGENT);
  GAP();
  steps<VFLAG + 1>(c, f0, f1, make_iseq<VPOLL - VFLAG>{});
  unsigned fv;
  asm volatile("global_load_dword %0, %1, off sc1" : "=v"(fv) : "v"(fl + kh * 256) : "memory");
  step<VPOLL + 1>(c, f0, f1);
  wait_vm<nd(VPOLL + 5)>();
  asm volatile("" : "+v"(fv));
  STAMP();
  if (__builtin_expect(__builtin_amdgcn_readfirstlane(fv) != 1u, 0)) {
#pragma unroll 1
    for (unsigned spins = 0; spins < SPIN_CAP; ++spins)
      if (__builtin_amdgcn_readfirstlane(__hip_atomic_load(fl + kh * 256, __ATOMIC_RELAXED, __HIP_MEMORY_SCOPE_AGENT)) == 1u) break;
  }
  asm volatile("" ::: "memory");
  STAMP();
  u32x4 pv[4];
#pragma unroll
  for (int u = 0; u < NXS; ++u) { asm volatile("buffer_load_dwordx4 %0, %1, %2, 0 offen sc1" : "=v"(pv[u]) : "v"(xo + kh * 32768 + u * 1024), "s"(P.rs) : "memory"); }
  unsigned oo[2];
  oo[0] = ((mb0 * 16 + fr) * D_ + tn * 128 + wn * 32 + (g & 1) * 16 + (g >> 1) * 8) * 2; oo[1] = oo[0] + 16 * D_ * 2;
  pin(oo[0]); pin(oo[1]);
  GAP();
  steps<VPOLL + 2>(c, f0, f1, make_iseq<NV - VPOLL - 3>{});
  f32x2 eg[4], er[2][4];
#pragma unroll
  for (int j = 0; j < 2; ++j) {
    const u32x2 gv = lds_ld<u32x2>(lds0 + PERS + 384 + wn * 64 + 32 * j + 8 * g);
    const u32x4 rv = lds_ld<u32x4>(eb + j * 1024 + lane * 16);
    eg[2 * j] = unpk(gv[0]); eg[2 * j + 1] = unpk(gv[1]);
#pragma unroll
    for (int e = 0; e < 4; ++e) er[j][e] = unpk(rv[e]);
  }
#pragma unroll
  for (int j = 0; j < 4; ++j) { pin(eg[j]); pin(er[0][j]); pin(er[1][j]); }
  step<NV - 1>(c, f0, f1);
  mfma_fence();
  STAMP();
  wait_vm<0>();
#pragma unroll
  for (int u = 0; u < NXS; ++u) pin(pv[u]);
#pragma unroll
  for (int i = 0; i < 2; ++i) {
    const u32x4 o = gated_resid(c.acc[i][0] + __builtin_bit_cast(f32x4, pv[2 * i]), c.acc[i][1] + __builtin_bit_cast(f32x4, pv[2 * i + 1]), eg, er[i]);
    auto r0 = __builtin_amdgcn_permlane16_swap(o[0], o[2], false, false);
    auto r1 = __builtin_amdgcn_permlane16_swap(o[1], o[3], false, false);
    const u32x4 w = {r0[0], r1[0], r0[1], r1[1]};
    *(u32x4*)((char*)P.out + oo[i]) = w;
  }
  STAMP();
}

__global__ __launch_bounds__(512, 2) void block_kernel(const u16* x, const u16* wqkv, const u16* wo, const u16* w1, const u16* w3, const u16* w2,
                                                       const u16* c, const u16* wmod, u16* out, char* ws) {
  extern __shared__ __attribute__((aligned(16))) char smem[];
  Par P;
  P.x = x; P.wqkv = wqkv; P.wo = wo; P.w1 = w1; P.w3 = w3; P.w2 = w2; P.c = c; P.wmod = wmod; P.out = out; P.ws = ws;
  P.lds0 = (unsigned)(uintptr_t)(LDS_PTR(char))smem;
  P.rs = __builtin_amdgcn_make_buffer_rsrc((void*)ws, 0, W_END, 0x00020000);
  const unsigned long long t0_ = __builtin_amdgcn_s_memrealtime();
  phase0(P);
  phase1(P);
  phase2(P);
  phase3(P);
  phase4(P);
  wait_vm<0>();
  while (__builtin_amdgcn_s_memrealtime() - t0_ < 10000) __builtin_amdgcn_s_sleep(1);
}

extern "C" void kernel_launch(void* const* d_in, const int* in_sizes, int n_in, void* d_out, int out_size, void* d_ws,
                              size_t ws_size, hipStream_t stream) {
  (void)hipFuncSetAttribute((const void*)block_kernel, hipFuncAttributeMaxDynamicSharedMemorySize, LDS_TOTAL);
  block_kernel<<<256, 512, LDS_TOTAL, stream>>>((const u16*)d_in[0], (const u16*)d_in[1], (const u16*)d_in[2], (const u16*)d_in[4],
                                                (const u16*)d_in[5], (const u16*)d_in[6], (const u16*)d_in[7], (const u16*)d_in[8], (u16*)d_out, (char*)d_ws);
}
```

```cpp
#include <hip/hip_runtime.h>
#include <hip/hip_bf16.h>
#include <stdint.h>
#include <utility>

typedef unsigned short u16;
typedef short bf16x8 __attribute__((ext_vector_type(8)));
typedef short bf16x4 __attribute__((ext_vector_type(4)));
typedef float f32x4 __attribute__((ext_vector_type(4)));
typedef float f32x2 __attribute__((ext_vector_type(2)));
typedef unsigned int u32x4 __attribute__((ext_vector_type(4)));
typedef unsigned int u32x2 __attribute__((ext_vector_type(2)));
typedef __amdgpu_buffer_rsrc_t rsrc_t;
#define LDS_PTR(T) __attribute__((address_space(3))) T*
#define DI __device__ __forceinline__
#define GAP() __builtin_amdgcn_sched_barrier(0)
template <int... Is> using iseq = std::integer_sequence<int, Is...>;
template <int N> using make_iseq = std::make_integer_sequence<int, N>;

constexpr int S_ = 2048, D_ = 1024, F_ = 3584;
constexpr int KBS = (S_ / 16) * 1024;

DI float bf2f(u16 v) { return __uint_as_float(((unsigned)v) << 16); }
typedef __bf16 bf16x2_t __attribute__((ext_vector_type(2)));
DI unsigned pk2(float lo, float hi) { const f32x2 v = {lo, hi}; return __builtin_bit_cast(unsigned, __builtin_convertvector(v, bf16x2_t)); }
DI float rbf(float v) { return __uint_as_float(pk2(0.f, v)); }
DI float lo2f(unsigned u) { return __uint_as_float(u << 16); }
DI float hi2f(unsigned u) { return __uint_as_float(u & 0xffff0000u); }
template <int N> DI void wait_vm() { asm volatile("s_waitcnt vmcnt(%0)" ::"n"(N) : "memory"); }
DI void drain_vm() { wait_vm<0>(); __builtin_amdgcn_s_waitcnt(0x0F70); asm volatile("" ::: "memory"); }
template <int N> DI void wait_lgkm() { __builtin_amdgcn_s_waitcnt(0xC07F | (N << 8)); asm volatile("" ::: "memory"); }
DI void bar() { asm volatile("" ::: "memory"); __builtin_amdgcn_s_barrier(); asm volatile("" ::: "memory"); }
template <int IMM> DI void m0_add(unsigned base) { asm volatile("s_add_u32 m0, %0, %1" ::"s"(base), "n"(IMM) : "scc", "memory"); }
template <int N> DI void nops() { asm volatile("s_nop %0" ::"n"(N - 1)); }
template <class T> DI void pin(T& v) { asm volatile("" : "+v"(v)); }
template <class T> DI void pins(T& v) { asm volatile("" : "+s"(v)); }
DI unsigned sel(bool c, unsigned a, unsigned b) { pin(a); pin(b); return c ? a : b; }
constexpr unsigned OOB = 0x7fffffff;
DI rsrc_t mk_rsrc(const void* p, unsigned bytes) { return __builtin_amdgcn_make_buffer_rsrc((void*)p, 0, bytes, 0x00020000); }
template <int OFF> DI void bdma(unsigned voff, const rsrc_t& rs, unsigned soff) {
  asm volatile("buffer_load_dwordx4 %0, %1, %2 offen offset:%3 lds" ::"v"(voff), "s"(rs), "s"(soff), "n"(OFF) : "memory");
}
DI void touch_line(const void* p, unsigned lds_scratch) {
  asm volatile("s_mov_b32 m0, %1\n\ts_nop 0\n\tglobal_load_lds_dword %0, off" ::"v"(p), "s"(lds_scratch) : "memory");
}
#define ACLOB "a0","a1","a2","a3","a4","a5","a6","a7","a8","a9","a10","a11","a12","a13","a14","a15","a16","a17","a18","a19","a20","a21","a22","a23","a24","a25","a26","a27","a28","a29","a30","a31","a32","a33","a34","a35","a36","a37","a38","a39","a40","a41","a42","a43","a44","a45","a46","a47","a48","a49","a50","a51","a52","a53","a54","a55","a56","a57","a58","a59","a60","a61","a62","a63","a64","a65","a66","a67","a68","a69","a70","a71","a72","a73","a74","a75","a76","a77","a78","a79","a80","a81","a82","a83","a84","a85","a86","a87","a88","a89","a90","a91","a92","a93","a94","a95","a96","a97","a98","a99","a100","a101","a102","a103","a104","a105","a106","a107","a108","a109","a110","a111","a112","a113","a114","a115","a116","a117","a118","a119","a120","a121","a122","a123","a124","a125","a126","a127","a128","a129","a130","a131","a132","a133","a134","a135","a136","a137","a138","a139","a140","a141","a142","a143","a144","a145","a146","a147","a148","a149","a150","a151","a152","a153","a154","a155","a156","a157","a158","a159","a160","a161","a162","a163","a164","a165","a166","a167","a168","a169","a170","a171","a172","a173","a174","a175","a176","a177","a178","a179","a180","a181","a182","a183","a184","a185","a186","a187","a188","a189","a190","a191","a192","a193","a194","a195","a196","a197","a198","a199","a200","a201","a202","a203","a204","a205","a206","a207","a208","a209","a210","a211","a212","a213","a214","a215","a216","a217","a218","a219","a220","a221","a222","a223","a224","a225","a226","a227","a228","a229","a230","a231","a232","a233","a234","a235","a236","a237","a238","a239","a240","a241","a242","a243","a244","a245","a246","a247","a248","a249","a250","a251","a252","a253","a254","a255"
template <int R> DI void ld_act(unsigned voff, const rsrc_t& rs, unsigned soff) {
  asm volatile("buffer_load_dwordx4 a[%c3:%c4], %0, %1, %2 offen offset:%5 sc1" ::"v"(voff), "s"(rs), "s"(soff), "i"(4 * R), "i"(4 * R + 3), "n"((R & 1) * 1024) : "memory");
}
template <int R, bool SW, bool FIRST> DI void mfa(f32x4& c, bf16x8 w) {
  if constexpr (FIRST && SW) asm volatile("v_mfma_f32_16x16x32_bf16 %0, a[%c2:%c3], %1, 0" : "=&v"(c) : "v"(w), "i"(4 * R), "i"(4 * R + 3));
  else if constexpr (FIRST) asm volatile("v_mfma_f32_16x16x32_bf16 %0, %1, a[%c2:%c3], 0" : "=&v"(c) : "v"(w), "i"(4 * R), "i"(4 * R + 3));
  else if constexpr (SW) asm volatile("v_mfma_f32_16x16x32_bf16 %0, a[%c2:%c3], %1, %0" : "+v"(c) : "v"(w), "i"(4 * R), "i"(4 * R + 3));
  else asm volatile("v_mfma_f32_16x16x32_bf16 %0, %1, a[%c2:%c3], %0" : "+v"(c) : "v"(w), "i"(4 * R), "i"(4 * R + 3));
}
DI void mfv(f32x4& d, const u32x4& a, const u32x4& b) { asm volatile("v_mfma_f32_16x16x32_bf16 %0, %1, %2, 0" : "=&v"(d) : "v"(a), "v"(b)); }
DI void mfma_fence() { GAP(); asm volatile("s_nop 7\n\ts_nop 1" ::: "memory"); GAP(); }
DI bf16x4 ldsT(unsigned addr) { return __builtin_amdgcn_ds_read_tr16_b64_v4i16((LDS_PTR(bf16x4))(uintptr_t)addr); }
DI bf16x8 cat(bf16x4 lo, bf16x4 hi) { return __builtin_shufflevector(lo, hi, 0, 1, 2, 3, 4, 5, 6, 7); }
DI u32x4 pack8(const f32x4& a, const f32x4& b) { return (u32x4){pk2(a[0], a[1]), pk2(a[2], a[3]), pk2(b[0], b[1]), pk2(b[2], b[3])}; }
DI void v_mul(float& d, float a, float b) { asm volatile("v_mul_f32 %0, %1, %2" : "=v"(d) : "v"(a), "v"(b)); }
DI void v_mulc(float& d, float a) { asm volatile("v_mul_f32 %0, 0xbfb8aa3b, %1" : "=v"(d) : "v"(a)); }
DI void v_exp(float& d, float a) { asm volatile("v_exp_f32 %0, %1" : "=v"(d) : "v"(a)); }
DI void v_fmac(float& d) { asm volatile("v_fma_f32 %0, %0, %1, %1" : "+v"(d) : "s"(-1.4426950408889634f)); }
DI void v_rcp(float& d) { asm volatile("v_rcp_f32 %0, %0" : "+v"(d)); }
DI unsigned v_cvt(float a, float b) { unsigned d; asm volatile("v_cvt_pk_bf16_f32 %0, %1, %2" : "=v"(d) : "v"(a), "v"(b)); return d; }

DI float fmax1(float a, float b) { return __builtin_amdgcn_fmed3f(a, b, __builtin_inff()); }
DI float xadd16(float v) { auto r = __builtin_amdgcn_permlane16_swap(__float_as_uint(v), __float_as_uint(v), false, false); return __uint_as_float(r[0]) + __uint_as_float(r[1]); }
DI float xadd32(float v) { auto r = __builtin_amdgcn_permlane32_swap(__float_as_uint(v), __float_as_uint(v), false, false); return __uint_as_float(r[0]) + __uint_as_float(r[1]); }
DI float add2(float u, float v) {
  auto r = __builtin_amdgcn_permlane16_swap(__float_as_uint(u), __float_as_uint(v), false, false);
  return xadd32(__uint_as_float(r[0]) + __uint_as_float(r[1]));
}
DI f32x2 spread2(float t) {
  auto r = __builtin_amdgcn_permlane16_swap(__float_as_uint(t), __float_as_uint(t), false, false);
  return (f32x2){__uint_as_float(r[0]), __uint_as_float(r[1])};
}
template <int CTRL> DI float dpp_add(float v) { return v + __uint_as_float(__builtin_amdgcn_update_dpp(0, __float_as_uint(v), CTRL, 0xf, 0xf, true)); }
DI f32x2 red8(const f32x2 (&a)[4]) {
  f32x2 o;
#pragma unroll
  for (int e = 0; e < 2; ++e) {
    auto r0 = __builtin_amdgcn_permlane16_swap(__float_as_uint(a[0][e]), __float_as_uint(a[1][e]), false, false);
    auto r1 = __builtin_amdgcn_permlane16_swap(__float_as_uint(a[2][e]), __float_as_uint(a[3][e]), false, false);
    const float u = __uint_as_float(r0[0]) + __uint_as_float(r0[1]), v = __uint_as_float(r1[0]) + __uint_as_float(r1[1]);
    auto r2 = __builtin_amdgcn_permlane32_swap(__float_as_uint(u), __float_as_uint(v), false, false);
    o[e] = dpp_add<0x128>(__uint_as_float(r2[0]) + __uint_as_float(r2[1]));
  }
  return o;
}
DI float row16_sum(float v) {
  v = dpp_add<0xB1>(v);
  v = dpp_add<0x4E>(v);
  v = dpp_add<0x141>(v);
  v = dpp_add<0x140>(v);
  return v;
}
DI f32x2 unpk(unsigned u) { return (f32x2){lo2f(u), hi2f(u)}; }
DI f32x2 rbf2(f32x2 v) { return (f32x2){rbf(v[0]), rbf(v[1])}; }
DI float ln_rstd(float var) { return __builtin_amdgcn_rcpf(rbf(__builtin_amdgcn_sqrtf(rbf(rbf(var) + 1e-5f)))); }
DI float silu_bf(u16 c) { const float x = bf2f(c); return rbf(x * __builtin_amdgcn_rcpf(1.f + __builtin_amdgcn_exp2f(x * -1.4426950408889634f))); }
DI unsigned umax4(const u32x4& v) { return max(max(v[0], v[1]), max(v[2], v[3])); }
DI unsigned umax16(const u32x4 (&v)[4]) { return max(max(umax4(v[0]), umax4(v[1])), max(umax4(v[2]), umax4(v[3]))); }
template <class T> DI T lds_ld(unsigned a) { return *(const LDS_PTR(T))(uintptr_t)a; }
template <class T> DI void lds_st(unsigned a, const T& v) { *(LDS_PTR(T))(uintptr_t)a = v; }
DI u32x4 swz_rows(const u32x4& o) {
  auto r0 = __builtin_amdgcn_permlane16_swap(o[0], o[2], false, false);
  auto r1 = __builtin_amdgcn_permlane16_swap(o[1], o[3], false, false);
  return (u32x4){r0[0], r1[0], r0[1], r1[1]};
}

constexpr unsigned W_H1 = 1u << 16, W_O = W_H1 + (4u << 20), W_X1 = W_O + (4u << 20), W_H2 = W_X1 + (4u << 20), W_HH = W_H2 + (4u << 20);
constexpr unsigned W_PART = 32u << 20, W_LATE = W_PART + (128u << 10), W_LN1 = W_LATE + (128u << 10), W_FLAGS = W_LN1 + (32u << 10), W_STATS = W_FLAGS + (128u << 10), W_HALO = W_STATS + (256u << 10);
constexpr unsigned W_XCH = 34u << 20, W_END = 42u << 20;
enum { PH_LN1 = 0, PH_G1 = 2, PH_G2, PH_G3, PH_G4 };
constexpr unsigned POISON = 0xffffffffu;
constexpr int AUX_SC1 = 16;
constexpr unsigned SPIN_CAP = 1u << 17;
constexpr int SLOT = 16384, L_KV = 8 * SLOT, L_WV = L_KV + 20480, L_RED = L_WV + 4096, L_TCH = L_RED + 1024, LDS_TOTAL = 163840;

#define STAMP()
#define STAMP0()
#define FST()
DI int opaque_tid() { int t = threadIdx.x; asm volatile("" : "+v"(t)); return t; }
DI int opaque_bid() { int b = blockIdx.x; asm volatile("" : "+s"(b)); return b; }

DI void map_g1(int b, int& tm, int& tn) { const int xcd = b & 7, idx = b >> 3; tm = (xcd & 1) * 8 + (idx & 7); tn = (xcd >> 1) * 4 + (idx >> 3); }
DI void map_g2(int b, int& tm, int& tn) { const int xcd = b & 7, idx = b >> 3; tm = (xcd & 3) * 4 + (idx >> 3); tn = (xcd >> 2) * 8 + (idx & 7); }
DI void map_g3(int b, int& tm, int& tn) { const int xcd = b & 7, idx = b >> 3; tm = (xcd >> 2) * 8 + (idx >> 2); tn = (xcd & 3) * 4 + ((idx + 2) & 3); }

struct Ctx {
  const u16 *x, *wqkv, *wo, *w1, *w3, *w2, *cc, *wmod; u16* out; char* ws;
  rsrc_t rs, rC, rQ, rO, r1, r3, r2, rM;
  f32x4 acc[13][2][2][2];
  bf16x4 wl[8][2], wh[8][2];
  float t[8]; u32x4 o;
  unsigned bW[2][2], bM[2][2], vA, vK[4], v1[4], v2[4], v3[4], vM[4];
  unsigned lds0, sB, sA, mB, so[2], sB4[2][2], sA4[2], fl3, fl4, xs4;
  int wave; unsigned long long pc0;
  u32x4 kf[2][2], vf[2][2], hl[4]; unsigned kvw, hso;
  u32x4 xr[2][2], lw[8]; float ls[8]; f32x2 ga[4];
  u32x4 mp[2][4]; unsigned mo, tag[2]; f32x2 eg[2][4], er[2][2][4], sc1[8], sh[8];
  unsigned pollv, pollo, flv, tch, vT1, vT2, vT3, vT4, vT5, vC, vG0; u32x4 xl[8];
};
DI void st16(const Ctx& c, unsigned off, const u32x4& v) { __builtin_amdgcn_raw_buffer_store_b128(v, c.rs, off, 0, AUX_SC1); }
DI void ld16x4(const Ctx& c, u32x4 (&v)[4], unsigned off, unsigned step) {
  asm volatile("buffer_load_dwordx4 %0, %4, %5, 0 offen sc1\n\tbuffer_load_dwordx4 %1, %4, %5, %6 offen sc1\n\tbuffer_load_dwordx4 %2, %4, %5, %7 offen sc1\n\tbuffer_load_dwordx4 %3, %4, %5, %8 offen sc1\n\ts_waitcnt vmcnt(0)"
               : "=&v"(v[0]), "=&v"(v[1]), "=&v"(v[2]), "=&v"(v[3]) : "v"(off), "s"(c.rs), "s"(step), "s"(2 * step), "s"(3 * step) : "memory");
}
DI void retry(const Ctx& c, u32x4 (&v)[4], unsigned off, unsigned step) {
  if (__builtin_expect(__any(umax16(v) == POISON), 0)) {
#pragma unroll 1
    for (unsigned spins = 0; spins < SPIN_CAP; ++spins) { ld16x4(c, v, off, step); if (!__any(umax16(v) == POISON)) break; __builtin_amdgcn_s_sleep(1); }
  }
}
DI bool tagged(const Ctx& c, const u32x4 (&v)[4]) {
  const unsigned d = ((v[0][2] ^ c.tag[0]) | (v[1][2] ^ c.tag[0]) | (v[2][2] ^ c.tag[0]) | (v[3][2] ^ c.tag[0])) | ((v[0][3] ^ c.tag[1]) | (v[1][3] ^ c.tag[1]) | (v[2][3] ^ c.tag[1]) | (v[3][3] ^ c.tag[1]));
  return __all(d == 0);
}
DI unsigned flag_off(int tm, int wave, int col, int ph) { return W_FLAGS + ((tm * 4 + wave) * 16 + col) * 128 + ph * 4; }
DI void set_flag(const Ctx& c, unsigned off, unsigned val) {
  asm volatile("s_mov_b64 exec, 1\n\tbuffer_store_dword %0, off, %1, %2 sc1\n\ts_mov_b64 exec, -1" ::"v"(val), "s"(c.rs), "s"(off) : "memory");
}
DI void spin(const Ctx& c, unsigned voff, unsigned need) {
#pragma unroll 1
  for (unsigned spins = 0; spins < SPIN_CAP; ++spins) {
    if (__all(__builtin_amdgcn_raw_buffer_load_b32(c.rs, voff, 0, AUX_SC1) >= need)) break;
    __builtin_amdgcn_s_sleep(1);
    asm volatile("" ::: "memory");
  }
  asm volatile("" ::: "memory");
}
template <int PH> DI void await(const Ctx& c, unsigned line) {
#pragma unroll 1
  for (unsigned spins = 0; spins < SPIN_CAP; ++spins) {
    const u32x4 v = __builtin_amdgcn_raw_buffer_load_b128(c.rs, line, 0, AUX_SC1);
    if (__all(v[0] == c.tag[0] && v[1] == c.tag[1] && (PH == PH_LN1 || v[PH] != 0))) break;
    __builtin_amdgcn_s_sleep(1);
    asm volatile("" ::: "memory");
  }
  asm volatile("" ::: "memory");
}

#define L4 7
#define NP1 8
#define NP3 8
#define PRE3 4
#define BS 4
#define QW 16
#define HP4 11
#define LG 5
#define AW 1000
#define DENSE 20
#define VQ 2
#define GNT " nt"
#define CAP 7
#define NFD 12
#define NF1 0
#define NF2 4
#define XD1 12
#define XD2 8
#define XD3 10
namespace en {
constexpr int RD = 4, NT = 116, NH = 8 * NT, T3 = 32, T4 = 88, NB4 = 14 - L4;
constexpr int seg_t0(int s) { return s == 1 ? 0 : s == 2 ? 24 : s == 3 ? T3 : NT; }
constexpr int seg_of(int H) { return H < 192 ? 1 : H < 256 ? 2 : 3; }
constexpr int seg_np(int s) { return s == 1 ? NP1 : s == 2 ? 8 : NP3; }
constexpr int seg_pre(int s) { return s == 3 ? PRE3 : 8; }
enum { U_K, U_V, U_Q, U_O, U_G0, U_U0, U_G1, U_U1, U_M, U_G2, U_U2, U_S, U_P };
constexpr int unit(int T) {
  if (T >= T4) { const int s = T - T4; return U_S + (s < 2 * NB4 ? s & 1 : s >= 2 * NB4 + L4); }
  return T < 32 ? T >> 3 : T < 64 ? U_G0 + ((T - 32) >> 4) * 2 + (T & 1) : T < 72 ? U_M : T - 72 < LG || (T - 72 < 16 - LG && ((T - 72 - LG) & 1)) ? U_G2 : U_U2;
}
constexpr int grp(int T) {
  if (T >= T4) { const int s = T - T4; return s < 2 * NB4 ? s / 2 : s < 2 * NB4 + L4 ? s - NB4 : s - NB4 - L4; }
  return T < 32 ? T & 7 : T < 64 ? (T >> 1) & 7 : T < 72 ? T & 7 : T - 72 < LG ? T - 72 : T - 72 >= 16 - LG ? T - 80 : (T - 72 - LG) / 2 + (unit(T) == U_G2 ? LG : 0);
}
constexpr int u2stage(int j) { int T = 72; while (unit(T) != U_U2 || grp(T) != j) ++T; return T; }
constexpr int g4first(int j) { return T4 + (j < NB4 ? 2 * j : j + NB4); }
constexpr int g4last(int j) { return T4 + (j < NB4 ? 2 * j + 1 : j + NB4 + L4); }
constexpr int g4kb(int j) { return j < 8 ? 32 * (j / 4) + 4 * (j % 4) : j < 10 ? 96 + 4 * (j - 8) : 64 + 4 * (j - 10); }
constexpr bool g4mix(int j) { return j == 8 || j == 9; }
constexpr bool barst(int T) { return T < NT - 1 && (BS == 1 || (T & 1) || (BS == 2 && (T - seg_t0(seg_of(8 * T)) < 16 || T >= 72)) || (BS == 4 && ((T >= 8 && T < 24) || (T >= 46 && T < 76) || (T > 103 && T < 110))) || T == 6 || T == 8); }
constexpr bool isbar(int H) { return (H & 7) == 8 - RD && barst(H >> 3); }
constexpr int cert(int T) { int b = T - 1; while (!barst(b)) --b; return 8 * b + 8 - RD; }
enum { K_A = 1, K_D, K_A4, K_P, K_XS, K_XL, K_M, K_T, K_C, K_G };
enum { A_CHK1 = 1, A_CHK2, A_CHK3, A_CHK4, A_FLAG, A_FLAG4, A_CHKX };
enum { P_K, P_GA, P_V, P_GB, P_GC, P_PREV, P_GD, P_GR, P_GO, PR_HH };
constexpr int NPR = PR_HH + 28, HH_ST = 13;
constexpr bool pr_up(int i) { return i - PR_HH < 20 ? (i - PR_HH) & 1 : i - PR_HH >= 24; }
constexpr int pr_blk(int i) { return i - PR_HH < 20 ? (i - PR_HH) / 2 : 10 + (i - PR_HH) % 4; }
constexpr int pr_avail(int i) {
  const int a[PR_HH] = {64, 106, 128, 0, 146, 165, 0, 0, 187}, n = i - PR_HH;
  return n < 0 ? a[i] : 8 * T3 + (n < 8 ? 128 : n < 16 ? 256 : n < 20 ? 320 : n < 24 ? 448 - 8 * LG : 448) - (n < 16 && !pr_up(i) ? 8 : 0);
}
constexpr int pr_len(int i) { return i == P_K || i == P_V ? 24 : i == P_PREV || i == P_GR || i == P_GO ? 1 : i < PR_HH ? 32 : pr_up(i) ? HH_ST + 1 : 40; }
struct Plan { short op[NH + 1], e[4 * NH], act[NH + 1], vw[NH + 1], d0[5], d1[5], slack, halo; };
constexpr Plan mk() {
  Plan p{};
  int atA[64] = {}, atD[4 * NT] = {}, atA4[112] = {}, req[NH + 1] = {}, nvm[NH + 1] = {};
  bool quiet[NH + 64] = {};
  for (int H = 0; H <= NH; ++H) req[H] = -1000;
  auto need = [&](int H, int x) { if (x > req[H]) req[H] = x; };
  auto window = [&](int H, int o0, int o1 = 0, int o2 = 0, int o3 = 0, int o4 = 0, int o5 = 0, int o6 = 0) {
    const int o[7] = {o0, o1, o2, o3, o4, o5, o6};
    for (int i = 0; i < 7 && o[i]; ++i) p.op[H++] = o[i];
    for (int i = 0; i < QW && seg_of(H + i) == seg_of(H - 1); ++i) quiet[H + i] = true;
  };
  auto poll = [&](int H, int a) { p.op[H - 16] = K_P * 1024; p.act[H] = a; need(H, H - 16); };
  constexpr int T = K_T * 1024, G = K_G * 1024, C = K_C * 1024, H3 = 8 * T3, H4 = 8 * T4;
  window(84, G, G + 1, G + 2, G + 3); window(120, G + 4, G + 5, G + 6, G + 7); window(153, C, T, T + 1, T + 2); window(172, C + 1, T + 3, T + 4);
  need(108, 87); need(148, 123);
  window(H3 + 130, C, T + 5, T + 6); window(H3 + 168, T + 7, T + 8); window(H3 + 270, C, T + 9, T + 10); window(H3 + 310, T + 11, T + 12);
  p.op[58] = T + 23; p.op[210] = T + 17; p.op[211] = T + 18; p.op[H3 + 67] = T + 19; p.op[H3 + 75] = T + 20; p.op[H3 + 235] = T + 21; p.op[H3 + 243] = T + 22;
  p.op[H3 + 366] = T + 13; p.op[H3 + 430] = T + 14; p.op[H3 + 496] = T + 16; p.op[H3 + 460] = C; p.op[H3 + 506] = T + 15; p.op[H3 + 546] = T + 16; p.op[H3 + 548] = T + 15;
  const int HA0 = H4 - 8 * LG;
  const int chk[4] = {H3 + 354, H3 + 410, H4 + 18, H4 + 8 * HP4 + 16}, hx0 = 8 * g4first(13) + 10;
  for (int i = 0; i < 4; ++i) poll(chk[i], A_CHK1 + i);
  int nd = 0, n4 = 0, nxs = 0, nxl = 0, hchk = 100000;
  p.slack = 1000;
  for (int s = 1; s <= 3; ++s) {
    const int T0 = seg_t0(s), T1 = seg_t0(s + 1), dbl = s == 3;
    int na = 2 * seg_pre(s), lim = T0 + 7;
    p.d0[s] = nd; if (nd < 4 * (T0 + seg_np(s))) nd = 4 * (T0 + seg_np(s)); p.d1[s] = nd;
    for (int n = 0; n < na; ++n) atA[n] = 8 * T0 - na + n;
    auto useA = [&](int n) { return 8 * T0 + (n >> 3) * (8 << dbl) + (n & 6); };
    for (int H = 8 * T0; H < 8 * T1; ++H) {
      if (isbar(H)) lim = (H >> 3) + 7;
      if (p.op[H] || quiet[H]) continue;
      if (H >= hx0 && nxs < 8) {
        p.op[H] = K_XS * 1024 + nxs++;
        if (nxs == 8) {
          const int hf = (H + XD1 + 1) & ~1; hchk = (hf + XD2 + XD3 + 1) & ~1;
          p.act[hf] = A_FLAG4; need(hf, H); p.op[hf + XD2] = K_P * 1024 + 1; p.act[hchk] = A_CHKX; need(hchk, hf + XD2);
        }
        continue;
      }
      if (H >= hchk && nxl < 8) { p.op[H] = K_XL * 1024 + nxl++; continue; }
      if (CAP < 8 && s == 3 && (H & 7) == 3 && H - 8 * T0 > 8 && H < HA0 - 40) continue;
      const int Td = nd >> 2, j = n4 >> 3;
      const bool dok = Td < NT && Td <= lim && Td < T1 + 8;
      const int dd = !dok ? 100000 : Td < T1 ? cert(Td) : 8 * T1 + 2 * (Td - T1);
      int da = 100000;
      if (na < 64) da = useA(na);
      else if (n4 < 112 && H >= 8 * (j < 8 ? u2stage(j) : g4last(j - 8)) + (n4 & 6) + 2 && H >= chk[j < 8 ? j >> 2 : j < 10 ? 2 : 3] && (H < HA0 || H >= cert(g4first(j)) - AW)) da = cert(g4first(j));
      if (da <= dd && da < 100000) { if (na < 64) { atA[na] = H; p.op[H] = K_A * 1024 + na++; } else { atA4[n4] = H; p.op[H] = K_A4 * 1024 + n4++; } }
      else if (dok && (H >= 8 * (Td - (barst(Td - 7) && barst(Td - 8) ? 7 : 6)) + 4 + 2 * (nd & 3) || da < 100000)) { atD[nd] = H; p.op[H] = K_D * 1024 + nd++; }
    }
    if (nd < 4 * T1 || na < 64) p.slack = -5;
    for (int n = 1; n < 64; n += 2) need(useA(n), atA[n]);
    for (int t = p.d1[s] >> 2; t < T1; ++t) need(cert(t), atD[4 * t + 3]);
  }
  for (int j = 0; j < 14; ++j) need(cert(g4first(j)), atA4[8 * j + 7]);
  if (n4 < 112 || nxl < 8 || nd < 4 * NT) p.slack = -1;
  int pr = 0, ix = 0;
  for (int q = 0; q < 4 * NH; ++q) {
    const int H = q >> 2, M = q & 3, k = p.op[H] / 1024, i = p.op[H] % 1024;
    const bool prep = (k == K_D && !(i & 3)) || ((k == K_A || k == K_A4) && !(i & 7)) || k == K_T || k == K_C || k == K_G;
    const bool busy = (M == 2 && k) || (M == 1 && prep) || (M == 0 && k == K_D && !(i & 3)) || (M == 3 && (H & 1));
    if (M == 2 && k) nvm[H]++;
    p.e[q] = -1;
    if (pr == NPR || q < 4 * pr_avail(pr) + 8) continue;
    if (busy && (pr < PR_HH + DENSE || (M == 2 && k) || (M == 3 && (isbar(H + 1) || (pr_up(pr) && ix == HH_ST))))) continue;
    p.e[q] = pr * 64 + ix;
    if (pr == P_PREV) p.halo = H;
    if ((pr == P_GR && H > 182) || (pr == P_GO && H > 191) || (pr == P_V && H > 149)) p.slack = -6;
    if (pr >= PR_HH && pr_up(pr) && ix == HH_ST) {
      nvm[H]++;
      const int n = pr - PR_HH;
      if (n == 7 || n == 15 || n == 19 || n == 27) {
        int hb = H + 22; while (!isbar(hb) || p.act[hb]) ++hb;
        p.act[hb] = A_FLAG; need(hb, H);
        if (hb + 16 > chk[n == 7 ? 0 : n == 15 ? 1 : n == 19 ? 2 : 3] - 16) p.slack = -3;
      }
    }
    if (++ix == pr_len(pr)) { ix = 0; ++pr; }
  }
  if (pr < NPR) p.slack = -2;
  for (int H = 0; H < NH; ++H) {
    int n = -1;
    if (req[H] != -1000) {
      const int h0 = 8 * seg_t0(seg_of(H));
      n = req[H] < h0 ? h0 - 1 - req[H] : 0;
      for (int h = req[H] < h0 ? h0 : req[H] + 1; h < H; ++h) n += nvm[h];
      if (req[H] >= h0 && H - req[H] < p.slack) p.slack = H - req[H];
    }
    p.vw[H] = n > 63 ? 63 : n;
  }
  { int n = 0; for (int h = p.halo + 1; h < 192; ++h) n += nvm[h]; p.halo = n; }
  return p;
}
constexpr Plan PL = mk();
static_assert(PL.slack >= 8, "every load is issued well before it is needed");
constexpr int seg_end(int H) { return 8 * seg_t0(seg_of(H) + 1); }

#define PGTAB 59, 272, 475, 883, 1149, 1364, 1570, 1757, 1962, 2160, 2346, 2534, 2731, 2906, 3107, 3323, 3535
constexpr int PGS[] = {PGTAB}, NPG = sizeof(PGS) / sizeof(int);
constexpr int pg_of(int q) { for (int i = 0; i < NPG; ++i) if (PGS[i] == q) return i; return -1; }
constexpr int pg_grp(int i) { return i < NPG && PGS[i] >= 0 ? PGS[i] >= 32 * T3 : -1; }
constexpr int pg_first(int g) { int i = 0; while (i < NPG && pg_grp(i) != g) ++i; return i; }
constexpr int pg_lab(int i, int g) { return pg_grp(i) == g ? i : 1000 + g; }
template <int I> DI void pg_mark() { asm volatile(".Lpg%c0:\n\ts_cbranch_execz .Lpg%c1" ::"i"(I), "i"(pg_lab(I + 4, pg_grp(I)))); }
template <int G> DI void pg_hop(int wave) {
  constexpr int f = pg_first(G);
  asm volatile("s_mov_b64 exec, 0\n\ts_cmp_eq_u32 %0, 0\n\ts_cbranch_scc1 .Lpg%c1\n\ts_cmp_eq_u32 %0, 1\n\ts_cbranch_scc1 .Lpg%c2\n\ts_cmp_eq_u32 %0, 2\n\ts_cbranch_scc1 .Lpg%c3\n\ts_branch .Lpg%c4\n"
               ".Lpg%c5:\n\ts_mov_b64 exec, -1" ::"s"(wave), "i"(pg_lab(f, G)), "i"(pg_lab(f + 1, G)), "i"(pg_lab(f + 2, G)), "i"(pg_lab(f + 3, G)), "i"(1000 + G) : "scc", "memory");
}
template <int H, int R> DI void rd(Ctx& c) {
  constexpr bool mx = unit(H >> 3) == U_M;
  constexpr int cb = R >> 1, hi = R & 1, off = ((H >> 3) & 7) * SLOT + ((H >> 1) & 3) * 4096 + (H & 1) * (mx ? 1024 : 256) + hi * 2048;
  const unsigned ad = (mx ? c.bM[cb][off >> 16] : c.bW[cb][off >> 16]) + (off & 65535);
  if constexpr (hi) c.wh[H & 7][cb] = ldsT(ad); else c.wl[H & 7][cb] = ldsT(ad);
}
template <int T> DI void dma_off(Ctx& c) {
  constexpr int u = unit(T), j = grp(T);
  if constexpr (u <= U_Q) c.sB = j * 128 * 6144 + (u + 1) % 3 * 2048;
  else if constexpr (u == U_O) c.sB = c.sB4[0][0] + j * 128 * 2048;
  else if constexpr (u < U_S) c.sB = j * 128 * F_ * 2 + (u == U_M ? 0 : u >= U_G2 ? 4096 : u >= U_G1 ? 2048 : 0);
  else c.sB = c.sB4[g4mix(j)][u - U_S] + g4kb(j) * 32 * 2048;
  pins(c.sB);
}
template <int D> DI void dma(Ctx& c) {
  constexpr int u = unit(D >> 2), i = D & 3;
  if constexpr (u <= U_Q) bdma<i * 1024>(c.v1[i], c.rQ, c.sB);
  else if constexpr (u == U_O) bdma<i * 1024>(c.v2[i], c.rO, c.sB);
  else if constexpr (u == U_M) bdma<i * 1024>(c.vM[i], (i & 1) ? c.r3 : c.r1, c.sB);
  else if constexpr (u < U_S) bdma<i * 1024>(c.v3[i], u == U_U0 || u == U_U1 || u == U_U2 ? c.r3 : c.r1, c.sB);
  else bdma<i * 1024>(c.v2[i], c.r2, c.sB);
}
template <int D> DI void dma1(Ctx& c) {
  if constexpr (!(D & 3)) { dma_off<(D >> 2)>(c); m0_add<((D >> 2) & 7) * SLOT>(c.mB); nops<1>(); }
  dma<D>(c);
}
template <int B, int... Ds> DI void dmas_(Ctx& c, iseq<Ds...>) { (dma1<B + Ds>(c), ...); }
template <int S, int A, int B> DI void dmas(Ctx& c) {
  constexpr int n = PL.d1[S] - PL.d0[S], a = A < n ? A : n, b = B < n ? B : n;
  GAP(); dmas_<PL.d0[S] + a>(c, make_iseq<b - a>{}); GAP();
}
template <int S, int B, int E, int... Is> DI void dma_at_(Ctx& c, int n, iseq<Is...>) { ((n == Is && B + Is < E ? dmas<S, B + Is, B + Is + 1>(c) : void()), ...); }
template <int S, int B, int E> DI void dma_at(Ctx& c, int n) { dma_at_<S, B, E>(c, n, make_iseq<16>{}); }
constexpr int NFREE2 = 4 * 31 - PL.d0[2];
constexpr int NFREE3 = 4 * 39 - PL.d0[3];

template <int N, bool UP, int I> DI void e_hh(Ctx& c) {
  constexpr bool mx = N == 8 || N == 9;
  constexpr int n = N < 8 ? N : mx ? N - 8 : N - 2, S = (n >> 1) & 1, B = n & 1, ug = mx ? U_M : N < 8 ? U_G0 + (N >> 2) * 2 : U_G2, e = I & 7, ph = I >> 3;
  f32x4& g = c.acc[ug][S][B][e >> 2];
  float r;
  if constexpr (UP) {
    if constexpr (I < 8) v_mul(c.t[e], g[e & 3], c.acc[mx ? U_M : ug + 1][mx ? 1 : S][B][e >> 2][e & 3]);
    else if constexpr (I < 12) c.o[e] = v_cvt(c.t[2 * e], c.t[2 * e + 1]);
    else if constexpr (I == HH_ST) asm volatile("buffer_store_dwordx4 %0, %1, %2, %3 offen offset:%4 sc1" ::"v"(c.o), "v"(c.vA), "s"(c.rs), "s"(c.so[mx] + (mx ? 96 : (n >> 2) * 32 + S) * KBS), "n"(B * 1024) : "memory");
  } else if constexpr (ph == 0) { v_mulc(r, g[e & 3]); g[e & 3] = r; }
  else if constexpr (ph == 1) v_exp(c.t[e], g[e & 3]);
  else if constexpr (ph == 2) v_fmac(c.t[e]);
  else if constexpr (ph == 3) v_rcp(c.t[e]);
  else { v_mul(r, g[e & 3], c.t[e]); g[e & 3] = r; }
}
template <int U, int F> DI void e_kv(Ctx& c) {
  u32x4 (&d)[2][2] = U ? c.vf : c.kf;
  if constexpr (F < 16) { const f32x4& a = c.acc[U][F >> 3][(F >> 2) & 1][(F >> 1) & 1]; d[F >> 3][(F >> 2) & 1][F & 3] = v_cvt(a[2 * (F & 1)], a[2 * (F & 1) + 1]); }
  else if constexpr (F == 18 || F == 22) lds_st(c.kvw + 4096 + U * 2048 + (F == 22) * 1024, d[F == 22][1]);
  else if constexpr (F == 17) {
    if (c.wave == 3) {
#pragma unroll
      for (int h = 0; h < 2; ++h) {
        const u32x4 v = d[h][1];
        st16(c, c.vA + c.hso + 65536 + U * 2048 + h * 1024, (u32x4){min(v[0], POISON - 1), min(v[1], POISON - 1), min(v[2], POISON - 1), min(v[3], POISON - 1)});
      }
    }
  }
}
DI void e_prev(Ctx& c) {
  asm volatile("s_cmp_eq_u32 %4, 0\n\ts_cbranch_scc1 1f\n\tds_read_b128 %0, %5\n\tds_read_b128 %1, %5 offset:1024\n\tds_read_b128 %2, %5 offset:2048\n\tds_read_b128 %3, %5 offset:3072\n\ts_branch 2f\n"
               "1:\n\tbuffer_load_dwordx4 %0, %6, %7, %8 offen sc1\n\tbuffer_load_dwordx4 %1, %6, %7, %8 offen offset:1024 sc1\n\tbuffer_load_dwordx4 %2, %6, %7, %8 offen offset:2048 sc1\n\tbuffer_load_dwordx4 %3, %6, %7, %8 offen offset:3072 sc1\n2:"
               : "=&v"(c.hl[0]), "=&v"(c.hl[1]), "=&v"(c.hl[2]), "=&v"(c.hl[3]) : "s"(c.wave), "v"(c.kvw), "v"(c.vA), "s"(c.rs), "s"(c.hso < W_HALO ? W_END : c.hso) : "scc", "memory");
}
DI void gemv_out(const Ctx& c, unsigned off, int lane) {
  const unsigned rb = c.lds0 + L_RED + (lane & 31) * 8;
  const f32x2 sum = (lds_ld<f32x2>(rb) + lds_ld<f32x2>(rb + 256)) + (lds_ld<f32x2>(rb + 512) + lds_ld<f32x2>(rb + 768));
  if (lane < 32) st16(c, off + lane * 16, (u32x4){__float_as_uint(sum[0]), __float_as_uint(sum[1]), c.tag[0], c.tag[1]});
}
template <int P, int I> DI void e_gv(Ctx& c) {
  if constexpr (P == P_GR) lds_st(c.lds0 + L_RED + c.wave * 256 + ((c.vA >> 4) & 7) * 32 + (c.vA >> 8) * 8, red8(c.ga));
  else if constexpr (P == P_GO) { if (c.wave == 3) gemv_out(c, W_LATE + blockIdx.x * 512, c.vA >> 4); }
  else {
    constexpr int r = 2 * (P == P_GA ? 0 : P == P_GB ? 1 : P == P_GC ? 2 : 3) + (I >> 4), e = I & 7;
    if constexpr (!(I & 8) && (e & 1)) asm volatile("v_and_b32 %0, 0xffff0000, %1" : "=v"(c.t[e]) : "v"(c.lw[r][e >> 1]));
    else if constexpr (!(I & 8)) asm volatile("v_lshlrev_b32 %0, 16, %1" : "=v"(c.t[e]) : "v"(c.lw[r][e >> 1]));
    else {
      float a = r ? c.ga[e >> 1][e & 1] : 0.f;
      if constexpr (r == 0) v_mul(a, c.t[e], c.ls[r]); else asm volatile("v_fmac_f32 %0, %1, %2" : "+v"(a) : "v"(c.t[e]), "v"(c.ls[r]));
      c.ga[e >> 1][e & 1] = a;
    }
  }
}
DI void touch(Ctx& c, unsigned v, const rsrc_t& r, unsigned o) { asm volatile("buffer_load_dword %0, %1, %2, %3 offen" : "+v"(c.tch) : "v"(v), "s"(r), "s"(o) : "memory"); }
template <int W> DI void warm(Ctx& c) {
  if constexpr (W == 0) touch(c, c.vT2, c.rO, 0);
  else if constexpr (W < 7) touch(c, c.vT3, (W & 1) ? c.r1 : c.r3, (W - 1) / 2 * 2048);
  else if constexpr (W < 9) touch(c, c.vT4, (W & 1) ? c.r1 : c.r3, 0);
  else if constexpr (W < 13) touch(c, c.vT5, c.r2, (W - 9) * (2 << 20));
  else if constexpr (W < 17) touch(c, c.vT2, c.r2, (W - 13) * (2 << 20));
  else if constexpr (W == 23) touch(c, c.vT1, c.rQ, 0);
  else touch(c, c.vT3 | (c.vA < 256 ? 0x40000000u : 0u), (W & 1) ? c.r1 : c.r3, (W - 17) / 2 * 2048);
}
DI unsigned slow_col(const void* base, int first, int n) {
  const unsigned k = (3 - (unsigned)((uintptr_t)base >> 7) - first) & 7;
  return k < n ? (first + k) * 128 : 0x40000000u;
}
DI void warm_code(Ctx& c) {
  unsigned o;
  asm volatile("s_mov_b32 %0, .-.Lblk_begin" : "=s"(o));
  touch(c, c.vC, c.rC, o);
}
template <int A> DI void action(Ctx& c) {
  if constexpr (A == A_FLAG) { c.flv += 1; set_flag(c, c.fl3, c.flv); }
  else if constexpr (A == A_FLAG4) set_flag(c, c.fl4, c.flv);
  else {
    constexpr unsigned need = A == A_CHKX ? 1 : A;
    pin(c.pollv);
    if (__builtin_expect(!__all(c.pollv >= need), 0)) spin(c, c.pollo + (A == A_CHKX ? (c.fl4 ^ 128) : 0), need);
    if constexpr (A == A_CHK4) { c.pollo = 0; pin(c.pollo); }
  }
}
template <int H> DI void waits(Ctx& c) {
  if constexpr (!(H & 1)) {
    constexpr int v = PL.vw[H] < 0 ? 63 : PL.vw[H];
    __builtin_amdgcn_s_waitcnt((v & 15) | ((v >> 4) << 14) | 0x70 | ((H + 4 <= seg_end(H) ? 8 : 0) << 8));
    asm volatile("" ::: "memory");
  }
  static_assert(!(H & 1) || (PL.vw[H] < 0 && !PL.act[H]), "waits sit in front of even half-steps");
  if constexpr (isbar(H)) bar();
  GAP();
  if constexpr (PL.act[H] != 0) { action<PL.act[H]>(c); GAP(); }
}
template <int H, int M> DI void slot(Ctx& c) {
  constexpr int cb = M >> 1, rb = M & 1, u = unit(H >> 3), j = grp(H >> 3), k = PL.op[H] / 1024, i = PL.op[H] % 1024, e = PL.e[4 * H + M];
  mfa<(j & 7) * 8 + (H & 6) + rb, u == U_V, j == 0 && (H & 6) == 0>(c.acc[u][H & 1][rb][cb], cat(c.wl[H & 7][cb], c.wh[H & 7][cb]));
  if constexpr (pg_of(4 * H + M) >= 0) pg_mark<pg_of(4 * H + M)>();
  GAP();
  if constexpr (H + RD < seg_end(H)) rd<H + RD, M>(c);
  GAP();
  if constexpr (M == 0 && k == K_D && !(i & 3)) dma_off<(i >> 2)>(c);
  if constexpr (M == 1) {
    if constexpr (k == K_D && !(i & 3)) m0_add<((i >> 2) & 7) * SLOT>(c.mB);
    if constexpr (k == K_A && !(i & 7)) { c.sA += 4 * KBS; pins(c.sA); }
    if constexpr (k == K_A4 && !(i & 7)) { c.sA = c.sA4[g4mix(i >> 3)] + g4kb(i >> 3) * KBS; pins(c.sA); }
  }
  if constexpr (M == 2) {
    if constexpr (k == K_D) dma<i>(c);
    if constexpr (k == K_T) warm<i>(c);
    if constexpr (k == K_C && i == 0) warm_code(c);
    if constexpr (k == K_C && i == 1) touch(c, c.vA << 20, c.rC, (((blockIdx.x >> 3) & 7) * 4 + c.wave) << 12);
    if constexpr (k == K_G && i < 8) asm volatile("buffer_load_dwordx4 %0, %1, %2, %3 offen" GNT : "=v"(c.lw[i & 7]) : "v"(c.vG0), "s"(c.rM), "s"(i * 6144 * 2) : "memory");
    if constexpr (k == K_A || k == K_A4) ld_act<i & 63>(c.vK[(i >> 1) & 3], c.rs, c.sA);
    if constexpr (k == K_P) asm volatile("buffer_load_dword %0, %1, %2, %3 offen sc1" : "+v"(c.pollv) : "v"(c.pollo), "s"(c.rs), "s"(i ? c.fl4 ^ 128 : 0) : "memory");
    if constexpr (k == K_XS) asm volatile("buffer_store_dwordx4 %0, %1, %2, %3 offen offset:%4 sc1" ::"v"(c.acc[U_S][i >> 2][(i >> 1) & 1][i & 1]), "v"(c.vA), "s"(c.rs), "s"(c.xs4 + (i >> 2) * 4096), "n"((i & 3) * 1024) : "memory");
    if constexpr (k == K_XL) asm volatile("buffer_load_dwordx4 %0, %1, %2, %3 offen offset:%4 sc1" : "=v"(c.xl[i]) : "v"(c.vA), "s"(c.rs), "s"((c.xs4 ^ 32768) + (i >> 2) * 4096), "n"((i & 3) * 1024) : "memory");
  }
  GAP();
  if constexpr (M == 3 && H + 1 < seg_end(H)) waits<H + 1>(c);
  if constexpr (e >= 64 * PR_HH) e_hh<pr_blk(e / 64), pr_up(e / 64), e % 64>(c);
  else if constexpr (e == 64 * P_PREV) e_prev(c);
  else if constexpr (e >= 0 && (e / 64 == P_K || e / 64 == P_V)) e_kv<e / 64 == P_V, e % 64>(c);
  else if constexpr (e >= 0) e_gv<e / 64, e % 64>(c);
  GAP();
}
template <int H0, int... Hs> DI void hsteps_(Ctx& c, iseq<Hs...>) { ((slot<H0 + Hs, 0>(c), slot<H0 + Hs, 1>(c), slot<H0 + Hs, 2>(c), slot<H0 + Hs, 3>(c)), ...); }
template <int Ta, int Tb> DI void stages(Ctx& c) { hsteps_<8 * Ta>(c, make_iseq<8 * (Tb - Ta)>{}); }
template <int H0, int... Is> DI void rd0(Ctx& c, iseq<Is...>) { (rd<H0 + Is / 4, Is % 4>(c), ...); }
template <int... Is> DI void ld0(Ctx& c, iseq<Is...>) { (((Is & 7) ? void() : (c.sA += 4 * KBS, pins(c.sA)), ld_act<Is>(c.vK[(Is >> 1) & 3], c.rs, c.sA)), ...); }
template <int S> DI void start(Ctx& c, unsigned base) {
  c.sA = base - 4 * KBS;
  ld0(c, make_iseq<2 * seg_pre(S)>{});
  bar();
  rd0<8 * seg_t0(S)>(c, make_iseq<4 * RD>{});
  GAP();
  waits<8 * seg_t0(S)>(c);
}
DI void lane_off(unsigned (&v)[4], int lane, int wave, int pitch, int col) {
  const int g = lane >> 4, q = (lane >> 2) & 3, p = lane & 3;
#pragma unroll
  for (int i = 0; i < 4; ++i) { v[i] = (32 * wave + 8 * i + 4 * (g >> 1) + q) * pitch + col + (g & 1) * 64 + (p ^ (g & 2)) * 16 - 1024 * i; pin(v[i]); }
}
DI void mod_load(Ctx& c) {
  asm volatile("buffer_load_dwordx4 %0, %8, %9, 0 offen sc1\n\tbuffer_load_dwordx4 %4, %8, %9, 0 offen offset:16 sc1\n\tbuffer_load_dwordx4 %1, %8, %9, 0 offen offset:512 sc1\n\tbuffer_load_dwordx4 %5, %8, %9, 0 offen offset:528 sc1\n\t"
               "buffer_load_dwordx4 %2, %8, %9, 0 offen offset:1024 sc1\n\tbuffer_load_dwordx4 %6, %8, %9, 0 offen offset:1040 sc1\n\tbuffer_load_dwordx4 %3, %8, %9, 0 offen offset:1536 sc1\n\tbuffer_load_dwordx4 %7, %8, %9, 0 offen offset:1552 sc1"
               : "=&v"(c.mp[0][0]), "=&v"(c.mp[0][1]), "=&v"(c.mp[0][2]), "=&v"(c.mp[0][3]), "=&v"(c.mp[1][0]), "=&v"(c.mp[1][1]), "=&v"(c.mp[1][2]), "=&v"(c.mp[1][3]) : "v"(c.mo), "s"(c.rs) : "memory");
}
template <int N> DI void mod_wait(Ctx& c) {
  asm volatile("s_waitcnt vmcnt(%8)" : "+v"(c.mp[0][0]), "+v"(c.mp[0][1]), "+v"(c.mp[0][2]), "+v"(c.mp[0][3]), "+v"(c.mp[1][0]), "+v"(c.mp[1][1]), "+v"(c.mp[1][2]), "+v"(c.mp[1][3]) : "n"(N) : "memory");
}
DI void mod_vectors(Ctx& c, int lane) {
  const int g = lane >> 4;
  if (__builtin_expect(!(tagged(c, c.mp[0]) && tagged(c, c.mp[1])), 0)) {
#pragma unroll 1
    for (unsigned spins = 0; spins < SPIN_CAP; ++spins) { mod_load(c); mod_wait<0>(c); if (tagged(c, c.mp[0]) && tagged(c, c.mp[1])) break; }
  }
  dmas<2, 15, 17>(c);
  f32x4 a;
#pragma unroll
  for (int e = 0; e < 4; ++e) { const u32x4 (&m)[4] = c.mp[e >> 1]; a[e] = (__uint_as_float(m[0][e & 1]) + __uint_as_float(m[1][e & 1])) + (__uint_as_float(m[2][e & 1]) + __uint_as_float(m[3][e & 1])); }
  const unsigned pb = c.lds0 + L_WV + c.wave * 1024;
  lds_st(pb + lane * 8, (u32x2){pk2(a[0], a[1]), pk2(a[2], a[3])});
#pragma unroll
  for (int i = 0; i < 4; ++i) {
    dma_at<2, 17, 25>(c, 2 * i);
    const u32x2 gv = lds_ld<u32x2>(pb + i * 32 + g * 8);
    c.eg[i >> 1][2 * (i & 1)] = unpk(gv[0]); c.eg[i >> 1][2 * (i & 1) + 1] = unpk(gv[1]);
    const u32x2 av = lds_ld<u32x2>(pb + 128 + i * 32 + g * 8), sv = lds_ld<u32x2>(pb + 256 + i * 32 + g * 8);
#pragma unroll
    for (int e = 0; e < 2; ++e) { c.sh[2 * i + e] = unpk(av[e]); c.sc1[2 * i + e] = rbf2(unpk(sv[e]) + 1.f); pin(c.sh[2 * i + e]); pin(c.sc1[2 * i + e]); }
    dma_at<2, 17, 25>(c, 2 * i + 1);
    const u32x4 r = c.xr[i >> 1][i & 1];
#pragma unroll
    for (int e = 0; e < 4; ++e) { c.er[i >> 1][i & 1][e] = unpk(r[e]); pin(c.er[i >> 1][i & 1][e]); }
  }
#pragma unroll
  for (int i = 0; i < 8; ++i) pin(c.eg[i >> 2][i & 3]);
}
}

DI u32x4 gated_resid(const f32x4& a0, const f32x4& a1, const f32x2 (&eg)[4], const f32x2 (&er)[4]) {
  u32x4 o;
#pragma unroll
  for (int j = 0; j < 4; ++j) {
    const f32x4& v = j < 2 ? a0 : a1;
    const f32x2 r = (f32x2){v[2 * (j & 1)], v[2 * (j & 1) + 1]} * eg[j] + er[j];
    o[j] = pk2(r[0], r[1]);
  }
  return o;
}

DI void phase1(Ctx& c) {
  using namespace en;
  const int tid = opaque_tid(), lane = tid & 63, b = opaque_bid(), wave = c.wave;
  const int fr = lane & 15, g = lane >> 4, q = (lane >> 2) & 3, p = lane & 3;
  c.tch = 0;
  asm volatile("global_load_dword %0, %1, %2" : "+v"(c.tch) : "v"((b >> 3) * 256 + (tid & 1) * 128), "s"(c.pc0) : "memory");
  GAP();
  int tm, tn; map_g1(b, tm, tn);
  int tm2, tn2; map_g2(b, tm2, tn2);
  u32x4 gw[4], xs[4], lc; u32x2 gc, ep;
  c.rM = mk_rsrc(c.wmod, D_ * 6 * D_ * 2);
  {
    const int k0 = (b & 7) * 128 + (tid >> 3) * 4;
    asm volatile("global_load_dwordx4 %0, %1, %2" : "=v"(lc) : "v"((tid >> 3) * 16), "s"(c.cc + (b & 3) * 256) : "memory");
    asm volatile("global_load_dwordx2 %4, %10, %11\n\tbuffer_load_dwordx4 %0, %5, %6, 0 offen" GNT "\n\tbuffer_load_dwordx4 %1, %5, %6, %7 offen" GNT "\n\tbuffer_load_dwordx4 %2, %5, %6, %8 offen" GNT "\n\tbuffer_load_dwordx4 %3, %5, %6, %9 offen" GNT
                 : "=&v"(gw[0]), "=&v"(gw[1]), "=&v"(gw[2]), "=&v"(gw[3]), "=&v"(gc)
                 : "v"((k0 * 6144 + (b >> 3) * 64 + (tid & 7) * 8) * 2), "s"(c.rM), "s"(12288), "s"(24576), "s"(36864), "v"(k0 * 2), "s"(c.cc) : "memory");
    asm volatile("global_load_dwordx2 %0, %1, %2 sc1" : "=v"(ep) : "v"(0), "s"(c.ws) : "memory");
    asm volatile("global_load_dwordx4 %0, %4, %5\n\tglobal_load_dwordx4 %1, %4, %5 offset:512\n\tglobal_load_dwordx4 %2, %4, %5 offset:1024\n\tglobal_load_dwordx4 %3, %4, %5 offset:1536"
                 : "=&v"(xs[0]), "=&v"(xs[1]), "=&v"(xs[2]), "=&v"(xs[3]) : "v"((tid >> 5) * 2048 + (tid & 31) * 16), "s"(c.x + (tm2 * 128 + tn2 * 8) * D_) : "memory");
  }
  GAP();
  c.rs = mk_rsrc(c.ws, W_END); c.rQ = mk_rsrc(c.wqkv, D_ * 3 * D_ * 2); c.rO = mk_rsrc(c.wo, D_ * D_ * 2);
  c.r1 = mk_rsrc(c.w1, D_ * F_ * 2); c.r3 = mk_rsrc(c.w3, D_ * F_ * 2); c.r2 = mk_rsrc(c.w2, D_ * F_ * 2);
  { unsigned n; asm volatile("s_mov_b32 %0, .Lblk_end-.Lblk_begin" : "=s"(n)); c.rC = mk_rsrc((const void*)c.pc0, n); }
  c.mB = c.lds0 + wave * 4096;
  pins(c.rs); pins(c.rQ); pins(c.rO); pins(c.rM); pins(c.r1); pins(c.r3); pins(c.r2); pins(c.rC); pins(c.mB);
  const int j8 = ((b >> 3) * 4 + wave) * 8;
  const unsigned slow1 = (j8 + (lane & 7)) * 6144 + slow_col(c.wqkv, ((b >> 1) & 3) * 4, 4), row1 = (b * 4 + wave) * 6144 + fr * 128;
  c.vT1 = sel(lane < 8, slow1, OOB); pin(c.vT1);
  c.vC = sel(lane == 2, (b * 4 + wave) * 128, j8 * 32 + (lane & 1) * 128);
  FST();
  warm_code(c);
  unsigned tk = sel(lane < 16, row1, sel(lane < 24, slow1, OOB)), tvq = sel(lane < 32, row1 + (lane >> 4) * 4096, sel(lane < 40, slow1 + 4096, OOB));
  unsigned i0 = W_HALO + (((tm2 - 1) & 15) * 16 + tn2) * 4096 + wave * 1024 + lane * 16, iz = lane < 16 ? POISON : 0u;
  unsigned i1 = sel(lane < 16, W_STATS + (tm2 * 4 + wave) * 4096 + tn2 * 256 + lane * 16, sel(lane == 16, flag_off(tm2, wave, tn2, PH_G1), OOB));
  unsigned xo = (fr * D_ + (g & 1) * 16 + (g >> 1) * 8) * 2, xo1 = xo + 16 * D_ * 2;
  unsigned o0 = W_PART + ((wave >> 1) * 16 + tn2) * 4096 + (wave & 1) * 2048 + lane * 16, o1 = W_LN1 + (tm2 * 128 + wave * 32 + fr) * 16;
  unsigned fl1 = sel(lane < 16, flag_off(tm, wave, fr, 0), flag_off(min(tm + 1, 15), wave, tn, 0));
  pin(tk); pin(tvq); pin(i0); pin(i1); pin(iz); pin(xo); pin(xo1); pin(o0); pin(o1); pin(fl1);
  lane_off(c.v1, lane, wave, 6144, tn * 128);
  c.vA = lane * 16; pin(c.vA); c.vK[0] = c.vA;
#pragma unroll
  for (int i = 1; i < 4; ++i) { c.vK[i] = c.vA + i * KBS; pin(c.vK[i]); }
  c.vT2 = sel(lane < 16, (b * 4 + wave) * 2048 + lane * 128, sel(lane < 24, (j8 + lane - 16) * 2048 + slow_col(c.wo, ((b >> 2) & 1) * 8, 8), OOB));
  c.vT3 = sel(lane < 16, (b * 4 + wave) * (F_ * 2) + lane * 128, sel(lane < 24, (j8 + lane - 16) * (F_ * 2) + slow_col(c.w1, (b & 3) * 4, 4), OOB));
  c.vG0 = (((b & 3) * 256 + (tid >> 3) * 8) * 6144 + 2048 + (b >> 2) * 64 + (tid & 7) * 8) * 2;
  pin(c.vG0);
  pin(c.vT2); pin(c.vT3);
#pragma unroll
  for (int w = 0; w < 2; ++w) {
    const unsigned bw = c.lds0 + w * 65536 + g * 512 + q * 64 + p * 8;
    c.bW[0][w] = bw + (g & 1) * 32; c.bW[1][w] = bw + ((g & 1) ^ 1) * 32;
    pin(c.bW[0][w]); pin(c.bW[1][w]);
  }
  lane_off(c.v2, lane, wave, 2048, 0);
  c.sB4[0][0] = tn2 * 128;
  c.mo = W_LATE + (g * 16 + tn2) * 2048 + fr * 32; pin(c.mo);
  c.kvw = c.lds0 + L_KV + wave * 4096 + lane * 16; pin(c.kvw);
  c.hso = W_HALO + ((tm - 1) * 16 + tn) * 4096;
  unsigned e_sel[4]; int e_base; f32x2 e_bias;
  {
    e_base = (fr & ~1) - 4 * g;
    const bool first = tm == 0 && wave == 0;
    e_bias = (f32x2){first && e_base < 0 ? -3.0e38f : 0.f, first && e_base < 2 ? -3.0e38f : 0.f};
    const unsigned s0 = (fr & 1) ? 0x01000c0cu : 0x0c0c0100u, s1 = (fr & 1) ? 0x03020c0cu : 0x0c0c0302u, z = 0x0c0c0c0cu;
    e_sel[0] = e_base >= 0 ? z : s0; e_sel[1] = e_base >= 2 ? z : s1; e_sel[2] = e_base >= 0 ? s0 : z; e_sel[3] = e_base >= 2 ? s1 : z;
    pin(e_base); pin(e_bias); pin(e_sel[0]); pin(e_sel[1]); pin(e_sel[2]); pin(e_sel[3]);
  }
  GAP();
  float sv[4];
  asm volatile("s_waitcnt vmcnt(10)" : "+v"(gc), "+v"(lc)::"memory");
#pragma unroll
  for (int i = 0; i < 4; ++i) { sv[i] = silu_bf((u16)(gc[i >> 1] >> (16 * (i & 1)))); pin(sv[i]); }
#pragma unroll
  for (int i = 0; i < 8; ++i) { c.ls[i] = silu_bf((u16)(lc[i >> 1] >> (16 * (i & 1)))); pin(c.ls[i]); }
  GAP();
  if constexpr (VQ == 3) { touch(c, tk, c.rQ, 2048); touch(c, tvq, c.rQ, 0); }
  FST();
  asm volatile("s_waitcnt vmcnt(%5)" : "+v"(gw[0]), "+v"(gw[1]), "+v"(gw[2]), "+v"(gw[3]), "+v"(ep) : "n"(VQ == 3 ? 7 : 5) : "memory");
  c.tag[0] = __builtin_amdgcn_readfirstlane(ep[0]) + 1; c.tag[1] = __builtin_amdgcn_readfirstlane(ep[1]);
  if constexpr (VQ != 3) touch(c, tk, c.rQ, 2048);
  GAP();
  FST();
  {
    f32x2 acc[4];
#pragma unroll
    for (int j = 0; j < 4; ++j) acc[j] = (f32x2){0.f, 0.f};
#pragma unroll
    for (int i = 0; i < 4; ++i) {
#pragma unroll
      for (int j = 0; j < 4; ++j) acc[j] += unpk(gw[i][j]) * sv[i];
    }
    lds_st(c.lds0 + L_RED + wave * 256 + (lane & 7) * 32 + g * 8, red8(acc));
    wait_lgkm<0>(); bar();
    if (wave == 3) gemv_out(c, W_PART + b * 512, lane);
    GAP();
    asm volatile("global_load_dwordx4 %0, %4, %6\n\tglobal_load_dwordx4 %1, %4, %6 offset:64\n\tglobal_load_dwordx4 %2, %5, %6\n\tglobal_load_dwordx4 %3, %5, %6 offset:64"
                 : "=&v"(c.xr[0][0]), "=&v"(c.xr[0][1]), "=&v"(c.xr[1][0]), "=&v"(c.xr[1][1]) : "v"(xo), "v"(xo1), "s"(c.x + (tm2 * 128 + wave * 32) * D_ + tn2 * 64) : "memory");
    if constexpr (VQ == 0) touch(c, tvq, c.rQ, 0);
    asm volatile("s_waitcnt vmcnt(%4)" : "+v"(xs[0]), "+v"(xs[1]), "+v"(xs[2]), "+v"(xs[3]) : "n"(6 + (VQ == 0 || VQ == 3)) : "memory");
    f32x2 f[16], s2 = {0.f, 0.f};
#pragma unroll
    for (int i = 0; i < 16; ++i) { f[i] = unpk(xs[i >> 2][i & 3]); s2 += f[i]; }
    const float mean = xadd16(row16_sum(s2[0] + s2[1])) * (1.f / 1024.f);
    f32x2 q2 = {0.f, 0.f};
#pragma unroll
    for (int i = 0; i < 16; ++i) { const f32x2 d = f[i] - mean; q2 += d * d; }
    const float rstd = ln_rstd(xadd16(row16_sum(q2[0] + q2[1])) * (1.f / 1024.f));
    if (!(lane & 31)) st16(c, W_LN1 + (tm2 * 128 + tn2 * 8 + (tid >> 5)) * 16, (u32x4){__float_as_uint(rbf(mean)), __float_as_uint(rstd), c.tag[0], c.tag[1]});
  }
  st16(c, i0, (u32x4){POISON, POISON, POISON, POISON}); st16(c, i1, (u32x4){iz, iz, iz, iz});
  GAP();
  FST();
  bar();
  dmas<1, 0, NFD>(c);
  if constexpr (VQ == 1) touch(c, tvq, c.rQ, 0);
  FST();
  f32x4 ln;
  {
    u32x4 v[4];
#pragma unroll 1
    for (unsigned spins = 0; spins < SPIN_CAP; ++spins) {
      asm volatile("buffer_load_dwordx4 %0, %4, %6, 0 offen sc1\n\tbuffer_load_dwordx4 %1, %4, %6, 0 offen offset:1024 sc1\n\tbuffer_load_dwordx4 %2, %5, %6, 0 offen sc1\n\tbuffer_load_dwordx4 %3, %5, %6, 0 offen offset:256 sc1\n\ts_waitcnt vmcnt(0)"
                   : "=&v"(v[0]), "=&v"(v[1]), "=&v"(v[2]), "=&v"(v[3]) : "v"(o0), "v"(o1), "s"(c.rs) : "memory");
      if (tagged(c, v)) break;
      __builtin_amdgcn_s_sleep(1);
    }
    pin(c.xr[0][0]); pin(c.xr[0][1]); pin(c.xr[1][0]); pin(c.xr[1][1]);
    FST();
    ln = (f32x4){__uint_as_float(v[2][0]), __uint_as_float(v[2][1]), __uint_as_float(v[3][0]), __uint_as_float(v[3][1])};
    lds_st(c.lds0 + L_RED + wave * 256 + (lane & 31) * 8, (f32x2){xadd32(__uint_as_float(v[0][0]) + __uint_as_float(v[1][0])), xadd32(__uint_as_float(v[0][1]) + __uint_as_float(v[1][1]))});
    wait_lgkm<0>(); bar();
    if constexpr (VQ == 2) touch(c, tvq, c.rQ, 0);
  }
  FST();
  {
    const f32x4 r = lds_ld<f32x4>(c.lds0 + L_RED + (g & 1) * 512 + fr * 16) + lds_ld<f32x4>(c.lds0 + L_RED + (g & 1) * 512 + 256 + fr * 16);
    const f32x2 one = (g & 1) ? (f32x2){1.f, 1.f} : (f32x2){0.f, 0.f};
    const f32x2 m0 = rbf2(rbf2((f32x2){r[0], r[1]}) + one), m1 = rbf2(rbf2((f32x2){r[2], r[3]}) + one);
    const unsigned Mt = c.lds0 + L_WV + wave * 1024, off = W_H1 + 2 * tn2 * KBS + (tm2 * 8 + 2 * wave) * 1024 + lane * 16;
    lds_st(Mt + (lane & 31) * 16, (f32x4){m0[0], m0[1], m1[0], m1[1]});
#pragma unroll
    for (int s = 0; s < 2; ++s) {
      const unsigned a = Mt + s * 128 + g * 16;
      const f32x4 sh0 = lds_ld<f32x4>(a), sh1 = lds_ld<f32x4>(a + 64), sc0 = lds_ld<f32x4>(a + 256), sc1 = lds_ld<f32x4>(a + 320);
      const f32x2 sc[4] = {{sc0[0], sc0[1]}, {sc0[2], sc0[3]}, {sc1[0], sc1[1]}, {sc1[2], sc1[3]}}, sh[4] = {{sh0[0], sh0[1]}, {sh0[2], sh0[3]}, {sh1[0], sh1[1]}, {sh1[2], sh1[3]}};
#pragma unroll
      for (int i = 0; i < 2; ++i) {
        c.xr[i][s] = swz_rows(c.xr[i][s]);
        u32x4 o;
#pragma unroll
        for (int j = 0; j < 4; ++j) {
          dma_at<1, NFD, NFD + NF1>(c, 8 * s + 4 * i + j - (16 - NF1));
          const f32x2 y = rbf2(rbf2(rbf2(unpk(c.xr[i][s][j]) - ln[2 * i]) * ln[2 * i + 1]) * sc[j]) + sh[j];
          o[j] = pk2(y[0], y[1]);
        }
        st16(c, off + s * KBS + i * 1024, o);
      }
    }
  }
  GAP();
  STAMP();
  dmas<1, NFD + NF1, NFD + NF1 + NF2>(c);
  wait_vm<NF2>();
  asm volatile("s_mov_b64 exec, 1\n\tbuffer_store_dwordx2 %0, off, %1, %2 sc1\n\ts_mov_b64 exec, -1" ::"v"((u32x2){c.tag[0], c.tag[1]}), "s"(c.rs), "s"(flag_off(tm2, wave, tn2, 0)) : "memory");
  STAMP();
  bar();
  FST();
  dmas<1, NFD + NF1 + NF2, 64>(c);
  FST();
  pg_hop<0>(wave);
  await<PH_LN1>(c, fl1);
  if (b + wave == 0) asm volatile("s_mov_b64 exec, 1\n\tbuffer_store_dwordx2 %0, off, %1, 0 sc1\n\ts_mov_b64 exec, -1" ::"v"((u32x2){c.tag[0], c.tag[1]}), "s"(c.rs) : "memory");
  STAMP();
  start<1>(c, W_H1 + (tm * 8 + 2 * wave) * 1024);
  STAMP();
  stages<0, 8>(c);
  STAMP();
  stages<8, 16>(c);
  STAMP();
  stages<16, 24>(c);
  mfma_fence();
  STAMP();
  {
    u32x4 qf[2][2];
#pragma unroll
    for (int i = 0; i < 4; ++i) qf[i >> 1][i & 1] = pack8(c.acc[U_Q][i >> 1][i & 1][0], c.acc[U_Q][i >> 1][i & 1][1]);
    asm volatile("s_waitcnt vmcnt(%4) lgkmcnt(0)" : "+v"(c.hl[0]), "+v"(c.hl[1]), "+v"(c.hl[2]), "+v"(c.hl[3]) : "n"(PL.halo) : "memory");
    if (wave == 0) retry(c, c.hl, c.vA + c.hso, 1024);
    dmas<2, 0, 1>(c);
    u32x4 vo[2][2][2];
#pragma unroll
    for (int h = 0; h < 2; ++h)
#pragma unroll
      for (int j = 0; j < 2; ++j) {
        vo[h][0][j] = (u32x4){c.hl[2 + h][2 * j], c.hl[2 + h][2 * j + 1], c.vf[h][0][2 * j], c.vf[h][0][2 * j + 1]};
        vo[h][1][j] = (u32x4){c.vf[h][0][2 * j], c.vf[h][0][2 * j + 1], c.vf[h][1][2 * j], c.vf[h][1][2 * j + 1]};
      }
#pragma unroll
    for (int i = 0; i < 4; ++i) { pin(qf[i >> 1][i & 1]); pin(c.hl[i]); }
    GAP(); nops<2>();
    f32x4 sp[2][2], sc[2][2];
#pragma unroll
    for (int h = 0; h < 2; ++h) { mfv(sp[h][0], c.hl[h], qf[h][0]); mfv(sc[h][0], c.kf[h][0], qf[h][0]); mfv(sp[h][1], c.kf[h][0], qf[h][1]); mfv(sc[h][1], c.kf[h][1], qf[h][1]); }
    GAP();
#pragma unroll
    for (int i = 0; i < 8; ++i) pin(vo[i >> 2][(i >> 1) & 1][i & 1]);
    dmas<2, 1, 2>(c);
    mfma_fence();
    const bool par = fr & 1, cur0 = e_base >= 0, cur1 = e_base >= 2;
    auto red2 = [](float& u, float& v, auto op) {
      auto r = __builtin_amdgcn_permlane16_swap(__float_as_uint(u), __float_as_uint(v), false, false);
      const float m = op(__uint_as_float(r[0]), __uint_as_float(r[1]));
      auto r2 = __builtin_amdgcn_permlane32_swap(__float_as_uint(m), __float_as_uint(m), false, false);
      const float t = op(__uint_as_float(r2[0]), __uint_as_float(r2[1]));
      auto r3 = __builtin_amdgcn_permlane16_swap(__float_as_uint(t), __float_as_uint(t), false, false);
      u = __uint_as_float(r3[0]); v = __uint_as_float(r3[1]);
    };
    f32x2 l[2][2]; float mx[2][2], sm[2][2]; u32x4 pw[2][2];
#pragma unroll
    for (int h = 0; h < 2; ++h)
#pragma unroll
      for (int i = 0; i < 2; ++i) {
        dma_at<2, 2, 6>(c, 2 * h + i);
        const float a0 = sp[h][i][0], a1 = sp[h][i][1], a2 = sp[h][i][2], a3 = sp[h][i][3], b0 = sc[h][i][0], b1 = sc[h][i][1], b2 = sc[h][i][2], b3 = sc[h][i][3];
        const float x0 = cur0 ? b0 : a0, x1 = cur0 ? b1 : a1, x2 = cur1 ? b2 : a2, x3 = cur1 ? b3 : a3;
        const float y0 = par ? x1 : x0, y1 = par ? x3 : x2;
        l[h][i] = rbf2(rbf2((f32x2){y0, y1}) * 0.17677669529663687f);
        if (i == 0) l[h][i] += e_bias;
        mx[h][i] = fmax1(l[h][i][0], l[h][i][1]);
      }
    dmas<2, 6, 7>(c);
#pragma unroll
    for (int h = 0; h < 2; ++h) red2(mx[h][0], mx[h][1], [](float x, float y) { return fmax1(x, y); });
#pragma unroll
    for (int h = 0; h < 2; ++h)
#pragma unroll
      for (int i = 0; i < 2; ++i) {
        if (i == 0) dma_at<2, 7, 9>(c, h);
        const f32x2 t = (l[h][i] - mx[h][i]) * 1.4426950408889634f;
        l[h][i] = (f32x2){__builtin_amdgcn_exp2f(t[0]), __builtin_amdgcn_exp2f(t[1])};
        sm[h][i] = l[h][i][0] + l[h][i][1];
      }
    dmas<2, 9, 10>(c);
#pragma unroll
    for (int h = 0; h < 2; ++h) red2(sm[h][0], sm[h][1], [](float x, float y) { return x + y; });
#pragma unroll
    for (int h = 0; h < 2; ++h)
#pragma unroll
      for (int i = 0; i < 2; ++i) {
        if (i == 0) dma_at<2, 10, 12>(c, h);
        const f32x2 pr = l[h][i] * __builtin_amdgcn_rcpf(sm[h][i]);
        const unsigned pp = pk2(pr[0], pr[1]);
        pw[h][i] = (u32x4){__builtin_amdgcn_perm(pp, pp, e_sel[0]), __builtin_amdgcn_perm(pp, pp, e_sel[1]), __builtin_amdgcn_perm(pp, pp, e_sel[2]), __builtin_amdgcn_perm(pp, pp, e_sel[3])};
        pin(pw[h][i]);
      }
    GAP(); nops<2>();
    f32x4 o[2][2][2];
#pragma unroll
    for (int i = 0; i < 8; ++i) mfv(o[i >> 2][(i >> 1) & 1][i & 1], vo[i >> 2][(i >> 1) & 1][i & 1], pw[i >> 2][(i >> 1) & 1]);
    dmas<2, 12, 13>(c);
    static_assert(NFREE2 >= 13, "slots of these pieces are free");
    mfma_fence();
    const unsigned oo = W_O + 2 * tn * KBS + (tm * 8 + 2 * wave) * 1024 + c.vA;
#pragma unroll
    for (int i = 0; i < 4; ++i) st16(c, oo + (i >> 1) * KBS + (i & 1) * 1024, pack8(o[i >> 1][i & 1][0], o[i >> 1][i & 1][1]));
  }
  GAP();
  mod_load(c);
  STAMP();
  wait_vm<8>();
  set_flag(c, flag_off(tm, wave, tn, PH_G1), 1);
  STAMP();
}

DI void phase2(Ctx& c) {
  using namespace en;
  const int tid = opaque_tid(), lane = tid & 63, b = opaque_bid(), wave = c.wave;
  const int fr = lane & 15, g = lane >> 4;
  int tm, tn; map_g2(b, tm, tn);
  bar();
  dmas<2, 13, 15>(c);
  mod_wait<3>(c);
  mod_vectors(c, lane);
  dmas<2, 25, 64>(c);
  { int tm3, tn3; map_g3(b, tm3, tn3); lane_off(c.v3, lane, wave, F_ * 2, tn3 * 128); }
  STAMP();
  await<PH_G1>(c, flag_off(tm, wave, fr, 0));
  STAMP();
  start<2>(c, W_O + (tm * 8 + 2 * wave) * 1024);
  STAMP();
  stages<24, 32>(c);
  mfma_fence();
  STAMP();
  f32x2 d[2][8]; float s1[2];

#pragma unroll
  for (int i = 0; i < 2; ++i) {
#pragma unroll
    for (int s = 0; s < 2; ++s) {
      dma_at<3, 0, NFREE3>(c, 4 * i + 2 * s);
      c.xr[i][s] = gated_resid(c.acc[U_O][s][i][0], c.acc[U_O][s][i][1], c.eg[s], c.er[i][s]);
      dma_at<3, 0, NFREE3>(c, 4 * i + 2 * s + 1);
#pragma unroll
      for (int j = 0; j < 4; ++j) d[i][4 * s + j] = unpk(c.xr[i][s][j]);
    }
    const f32x2 s2 = ((d[i][0] + d[i][1]) + (d[i][2] + d[i][3])) + ((d[i][4] + d[i][5]) + (d[i][6] + d[i][7]));
    s1[i] = s2[0] + s2[1];
  }
  const f32x2 mu = spread2(add2(s1[0], s1[1]) * (1.f / 64.f));
#pragma unroll
  for (int i = 0; i < 2; ++i) {
    f32x2 q2 = {0.f, 0.f};
#pragma unroll
    for (int j = 0; j < 8; ++j) { if (!(j & 3)) dma_at<3, 8, NFREE3>(c, 2 * i + (j >> 2)); d[i][j] -= mu[i]; q2 += d[i][j] * d[i][j]; }
    s1[i] = q2[0] + q2[1];
  }
  const f32x2 m2 = spread2(add2(s1[0], s1[1]));
  const unsigned sb = W_STATS + (tm * 4 + wave) * 4096;
  if (g == 0) st16(c, sb + tn * 256 + fr * 16, (u32x4){min(__float_as_uint(mu[0]), POISON - 1), min(__float_as_uint(m2[0]), POISON - 1), min(__float_as_uint(mu[1]), POISON - 1), min(__float_as_uint(m2[1]), POISON - 1)});
  GAP();
  dmas<3, 12, (NFREE3 < 16 ? NFREE3 : 16)>(c);
  STAMP();
  u32x4 pv[4];
  pg_hop<1>(wave);
  ld16x4(c, pv, sb + c.vA, 1024);
  retry(c, pv, sb + c.vA, 1024);
  STAMP();
  {
    f32x2 um[4], s2 = {0.f, 0.f}, q2 = {0.f, 0.f};
#pragma unroll
    for (int t = 0; t < 4; ++t) { um[t] = (f32x2){__uint_as_float(pv[t][0]), __uint_as_float(pv[t][2])}; s2 += um[t]; q2 += (f32x2){__uint_as_float(pv[t][1]), __uint_as_float(pv[t][3])}; }
    const float mg = add2(s2[0], s2[1]) * (1.f / 16.f);
    const f32x2 mean = spread2(mg);
    f32x2 e2 = {0.f, 0.f};
#pragma unroll
    for (int t = 0; t < 4; ++t) { const f32x2 dd = um[t] - mean; e2 += dd * dd; }
    q2 += e2 * 64.f;
    const f32x2 rs = spread2(ln_rstd(add2(q2[0], q2[1]) * (1.f / 1024.f))), mb = rbf2(mean);
    const unsigned ob = W_H2 + 2 * tn * KBS + (tm * 8 + 2 * wave) * 1024 + c.vA;
#pragma unroll
    for (int i = 0; i < 2; ++i) {
      const float dm = (mu[i] - mb[i]) * rs[i];
#pragma unroll
      for (int s = 0; s < 2; ++s) {
        u32x4 o;
#pragma unroll
        for (int j = 0; j < 4; ++j) {
          const f32x2 y = (d[i][4 * s + j] * rs[i] + dm) * c.sc1[4 * s + j] + c.sh[4 * s + j];
          o[j] = pk2(y[0], y[1]);
        }
        st16(c, ob + s * KBS + i * 1024, o);
      }
    }
  }
  GAP();
  STAMP();
  drain_vm();
  set_flag(c, flag_off(tm, wave, tn, PH_G2), 1);
  STAMP();
}

DI void phase3(Ctx& c) {
  using namespace en;
  int tid = opaque_tid(), lane = tid & 63; const int b = opaque_bid(), wave = c.wave;
  int tm, tn, tm4, tn4; map_g3(b, tm, tn); map_g2(b, tm4, tn4);
  bar();
  dmas<3, (NFREE3 < 16 ? NFREE3 : 16), 64>(c);
  {
    const int g = lane >> 4, q = (lane >> 2) & 3, p = lane & 3;
#pragma unroll
    for (int i = 0; i < 4; ++i) {
      c.bM[i >> 1][i & 1] = c.bW[i >> 1][i & 1] - g * 256; pin(c.bM[i >> 1][i & 1]);
      c.vM[i] = (32 * wave + 16 * (i >> 1) + 4 * g + q) * (F_ * 2) + (96 + tn) * 64 + (p ^ (2 * (g & 1))) * 16 - 1024 * i; pin(c.vM[i]);
    }
    const int j8 = ((b >> 3) * 4 + wave) * 8;
    c.vT4 = sel(lane < 16, (j8 + (lane >> 1)) * (F_ * 2) + 6144 + (b & 3) * 256 + (lane & 1) * 128, OOB);
    c.vT5 = sel(lane < 16, c.vT2, OOB);
    c.vT2 = sel((lane >> 3) == 2, (j8 + lane - 16) * 2048 + slow_col(c.w2, ((b >> 2) & 1) * 8, 8), OOB);
    pin(c.vT2); pin(c.vT4); pin(c.vT5);
    const int kh = tn4 & 1;
#pragma unroll
    for (int i = 0; i < 4; ++i) { c.sB4[i >> 1][i & 1] = (tn4 ^ 1 ^ (i & 1)) * 128 + kh * (16 >> (i >> 1)) * 32 * 2048; pins(c.sB4[i >> 1][i & 1]); }
#pragma unroll
    for (int i = 0; i < 2; ++i) { c.sA4[i] = W_HH + kh * (16 >> i) * KBS + (tm4 * 8 + 2 * wave) * 1024; pins(c.sA4[i]); }
    c.so[1] = W_HH + tn * KBS + (tm * 8 + 2 * wave) * 1024; c.so[0] = c.so[1] + tn * KBS;
    c.fl3 = flag_off(tm, wave, tn, PH_G3); c.fl4 = flag_off(tm4, wave, tn4, PH_G4);
    c.xs4 = W_XCH + (tm4 * 16 + tn4) * 32768 + wave * 8192;
    c.pollo = flag_off(tm4, wave, kh * 8 + (lane & 7), PH_G3); c.flv = 0; c.pollv = 0;
    pin(c.pollo); pin(c.flv); pins(c.so[0]); pins(c.so[1]); pins(c.fl3); pins(c.fl4); pins(c.xs4);
  }
  STAMP();
  await<PH_G2>(c, flag_off(tm, wave, lane & 15, 0));
  STAMP();
  start<3>(c, W_H2 + (tm * 8 + 2 * wave) * 1024);
  STAMP();
  stages<32, 40>(c);
  STAMP();
  stages<40, 48>(c);
  STAMP();
  stages<48, 56>(c);
  STAMP();
  stages<56, 64>(c);
  STAMP();
  stages<64, 72>(c);
  STAMP();
  stages<72, 80>(c);
  STAMP();
  stages<80, 88>(c);
  STAMP();
  stages<88, 96>(c);
  STAMP();
  stages<96, 104>(c);
  STAMP();
  stages<104, 110>(c);
  STAMP();
  stages<110, NT - 1>(c);
  tid = opaque_tid(); lane = tid & 63;
  const int g = lane >> 4;
  {
    const unsigned pb = c.lds0 + L_WV + wave * 1024 + 384 + g * 8;
#pragma unroll
    for (int i = 0; i < 4; ++i) {
      const u32x2 gv = lds_ld<u32x2>(pb + i * 32);
      c.eg[i >> 1][2 * (i & 1)] = unpk(gv[0]); c.eg[i >> 1][2 * (i & 1) + 1] = unpk(gv[1]);
#pragma unroll
      for (int e = 0; e < 4; ++e) { c.er[i >> 1][i & 1][e] = unpk(c.xr[i >> 1][i & 1][e]); pin(c.er[i >> 1][i & 1][e]); }
    }
#pragma unroll
    for (int i = 0; i < 8; ++i) pin(c.eg[i >> 2][i & 3]);
  }
  unsigned oo = ((tm4 * 128 + wave * 32 + (lane & 15)) * D_ + tn4 * 64 + (g & 1) * 16 + (g >> 1) * 8) * 2;
  pin(oo);
  GAP();
  stages<NT - 1, NT>(c);
  mfma_fence();
  STAMP();
  wait_vm<0>();
  pin(c.tch);
#pragma unroll
  for (int u = 0; u < 8; ++u) pin(c.xl[u]);
#pragma unroll
  for (int i = 0; i < 4; ++i) {
    const int s = i & 1, rb = i >> 1;
    const u32x4 o = gated_resid(c.acc[U_P][s][rb][0] + __builtin_bit_cast(f32x4, c.xl[4 * s + 2 * rb]), c.acc[U_P][s][rb][1] + __builtin_bit_cast(f32x4, c.xl[4 * s + 2 * rb + 1]), c.eg[s], c.er[rb][s]);
    *(u32x4*)((char*)c.out + oo + rb * 16 * D_ * 2 + s * 64) = swz_rows(o);
  }
  STAMP();
}

__global__ __launch_bounds__(256, 1) __attribute__((aligned(4096))) void block_kernel(const u16* x, const u16* wqkv, const u16* wo, const u16* w1, const u16* w3, const u16* w2,
                                                       const u16* cc, const u16* wmod, u16* out, char* ws) {
  extern __shared__ __attribute__((aligned(16))) char smem[];
  asm volatile("" ::: ACLOB);
  Ctx c;
  c.x = x; c.wqkv = wqkv; c.wo = wo; c.w1 = w1; c.w3 = w3; c.w2 = w2; c.cc = cc; c.wmod = wmod; c.out = out; c.ws = ws;
  c.lds0 = (unsigned)(uintptr_t)(LDS_PTR(char))smem;
  asm volatile("s_getpc_b64 %0\n.Lblk_begin:" : "=s"(c.pc0));
  c.wave = __builtin_amdgcn_readfirstlane(threadIdx.x >> 6);
  STAMP0();
  phase1(c);
  phase2(c);
  phase3(c);
  asm volatile(".Lblk_end:");
}

extern "C" void kernel_launch(void* const* d_in, const int* in_sizes, int n_in, void* d_out, int out_size, void* d_ws,
                              size_t ws_size, hipStream_t stream) {
  (void)hipFuncSetAttribute((const void*)block_kernel, hipFuncAttributeMaxDynamicSharedMemorySize, LDS_TOTAL);
  block_kernel<<<256, 256, LDS_TOTAL, stream>>>((const u16*)d_in[0], (const u16*)d_in[1], (const u16*)d_in[2], (const u16*)d_in[4],
                                                (const u16*)d_in[5], (const u16*)d_in[6], (const u16*)d_in[7], (const u16*)d_in[8], (u16*)d_out, (char*)d_ws);
}
```

```cpp
#include <hip/hip_runtime.h>
#include <hip/hip_bf16.h>
#include <stdint.h>
#include <utility>

typedef unsigned short u16;
typedef short bf16x8 __attribute__((ext_vector_type(8)));
typedef short bf16x4 __attribute__((ext_vector_type(4)));
typedef float f32x4 __attribute__((ext_vector_type(4)));
typedef float f32x2 __attribute__((ext_vector_type(2)));
typedef unsigned int u32x4 __attribute__((ext_vector_type(4)));
typedef unsigned int u32x2 __attribute__((ext_vector_type(2)));
typedef __amdgpu_buffer_rsrc_t rsrc_t;
#define LDS_PTR(T) __attribute__((address_space(3))) T*
#define DI __device__ __forceinline__
#define GAP() __builtin_amdgcn_sched_barrier(0)
template <int... Is> using iseq = std::integer_sequence<int, Is...>;
template <int N> using make_iseq = std::make_integer_sequence<int, N>;

constexpr int S_ = 2048, D_ = 1024, F_ = 3584;
constexpr int KBS = (S_ / 16) * 1024;

DI float bf2f(u16 v) { return __uint_as_float(((unsigned)v) << 16); }
typedef __bf16 bf16x2_t __attribute__((ext_vector_type(2)));
DI unsigned pk2(float lo, float hi) { const f32x2 v = {lo, hi}; return __builtin_bit_cast(unsigned, __builtin_convertvector(v, bf16x2_t)); }
DI float rbf(float v) { return __uint_as_float(pk2(0.f, v)); }
DI float lo2f(unsigned u) { return __uint_as_float(u << 16); }
DI float hi2f(unsigned u) { return __uint_as_float(u & 0xffff0000u); }
template <int N> DI void wait_vm() { asm volatile("s_waitcnt vmcnt(%0)" ::"n"(N) : "memory"); }
DI void drain_vm() { wait_vm<0>(); __builtin_amdgcn_s_waitcnt(0x0F70); asm volatile("" ::: "memory"); }
template <int N> DI void wait_lgkm() { __builtin_amdgcn_s_waitcnt(0xC07F | (N << 8)); asm volatile("" ::: "memory"); }
DI void bar() { asm volatile("" ::: "memory"); __builtin_amdgcn_s_barrier(); asm volatile("" ::: "memory"); }
template <int IMM> DI void m0_add(unsigned base) { asm volatile("s_add_u32 m0, %0, %1" ::"s"(base), "n"(IMM) : "scc", "memory"); }
template <int N> DI void nops() { asm volatile("s_nop %0" ::"n"(N - 1)); }
template <class T> DI void pin(T& v) { asm volatile("" : "+v"(v)); }
template <class T> DI void pins(T& v) { asm volatile("" : "+s"(v)); }
DI unsigned sel(bool c, unsigned a, unsigned b) { pin(a); pin(b); return c ? a : b; }
constexpr unsigned OOB = 0x7fffffff;
DI rsrc_t mk_rsrc(const void* p, unsigned bytes) { return __builtin_amdgcn_make_buffer_rsrc((void*)p, 0, bytes, 0x00020000); }
template <int OFF> DI void bdma(unsigned voff, const rsrc_t& rs, unsigned soff) {
  asm volatile("buffer_load_dwordx4 %0, %1, %2 offen offset:%3 lds" ::"v"(voff), "s"(rs), "s"(soff), "n"(OFF) : "memory");
}
DI void touch_line(const void* p, unsigned lds_scratch) {
  asm volatile("s_mov_b32 m0, %1\n\ts_nop 0\n\tglobal_load_lds_dword %0, off" ::"v"(p), "s"(lds_scratch) : "memory");
}
#define ACLOB "a0","a1","a2","a3","a4","a5","a6","a7","a8","a9","a10","a11","a12","a13","a14","a15","a16","a17","a18","a19","a20","a21","a22","a23","a24","a25","a26","a27","a28","a29","a30","a31","a32","a33","a34","a35","a36","a37","a38","a39","a40","a41","a42","a43","a44","a45","a46","a47","a48","a49","a50","a51","a52","a53","a54","a55","a56","a57","a58","a59","a60","a61","a62","a63","a64","a65","a66","a67","a68","a69","a70","a71","a72","a73","a74","a75","a76","a77","a78","a79","a80","a81","a82","a83","a84","a85","a86","a87","a88","a89","a90","a91","a92","a93","a94","a95","a96","a97","a98","a99","a100","a101","a102","a103","a104","a105","a106","a107","a108","a109","a110","a111","a112","a113","a114","a115","a116","a117","a118","a119","a120","a121","a122","a123","a124","a125","a126","a127","a128","a129","a130","a131","a132","a133","a134","a135","a136","a137","a138","a139","a140","a141","a142","a143","a144","a145","a146","a147","a148","a149","a150","a151","a152","a153","a154","a155","a156","a157","a158","a159","a160","a161","a162","a163","a164","a165","a166","a167","a168","a169","a170","a171","a172","a173","a174","a175","a176","a177","a178","a179","a180","a181","a182","a183","a184","a185","a186","a187","a188","a189","a190","a191","a192","a193","a194","a195","a196","a197","a198","a199","a200","a201","a202","a203","a204","a205","a206","a207","a208","a209","a210","a211","a212","a213","a214","a215","a216","a217","a218","a219","a220","a221","a222","a223","a224","a225","a226","a227","a228","a229","a230","a231","a232","a233","a234","a235","a236","a237","a238","a239","a240","a241","a242","a243","a244","a245","a246","a247","a248","a249","a250","a251","a252","a253","a254","a255"
template <int R> DI void ld_act(unsigned voff, const rsrc_t& rs, unsigned soff) {
  asm volatile("buffer_load_dwordx4 a[%c3:%c4], %0, %1, %2 offen offset:%5 sc1" ::"v"(voff), "s"(rs), "s"(soff), "i"(4 * R), "i"(4 * R + 3), "n"((R & 1) * 1024) : "memory");
}
template <int R, bool SW, bool FIRST> DI void mfa(f32x4& c, bf16x8 w) {
  if constexpr (FIRST && SW) asm volatile("v_mfma_f32_16x16x32_bf16 %0, a[%c2:%c3], %1, 0" : "=&v"(c) : "v"(w), "i"(4 * R), "i"(4 * R + 3));
  else if constexpr (FIRST) asm volatile("v_mfma_f32_16x16x32_bf16 %0, %1, a[%c2:%c3], 0" : "=&v"(c) : "v"(w), "i"(4 * R), "i"(4 * R + 3));
  else if constexpr (SW) asm volatile("v_mfma_f32_16x16x32_bf16 %0, a[%c2:%c3], %1, %0" : "+v"(c) : "v"(w), "i"(4 * R), "i"(4 * R + 3));
  else asm volatile("v_mfma_f32_16x16x32_bf16 %0, %1, a[%c2:%c3], %0" : "+v"(c) : "v"(w), "i"(4 * R), "i"(4 * R + 3));
}
DI void mfv(f32x4& d, const u32x4& a, const u32x4& b) { asm volatile("v_mfma_f32_16x16x32_bf16 %0, %1, %2, 0" : "=&v"(d) : "v"(a), "v"(b)); }
DI void mfma_fence() { GAP(); asm volatile("s_nop 7\n\ts_nop 1" ::: "memory"); GAP(); }
DI bf16x4 ldsT(unsigned addr) { return __builtin_amdgcn_ds_read_tr16_b64_v4i16((LDS_PTR(bf16x4))(uintptr_t)addr); }
DI bf16x8 cat(bf16x4 lo, bf16x4 hi) { return __builtin_shufflevector(lo, hi, 0, 1, 2, 3, 4, 5, 6, 7); }
DI u32x4 pack8(const f32x4& a, const f32x4& b) { return (u32x4){pk2(a[0], a[1]), pk2(a[2], a[3]), pk2(b[0], b[1]), pk2(b[2], b[3])}; }
DI void v_mul(float& d, float a, float b) { asm volatile("v_mul_f32 %0, %1, %2" : "=v"(d) : "v"(a), "v"(b)); }
DI void v_mulc(float& d, float a) { asm volatile("v_mul_f32 %0, 0xbfb8aa3b, %1" : "=v"(d) : "v"(a)); }
DI void v_exp(float& d, float a) { asm volatile("v_exp_f32 %0, %1" : "=v"(d) : "v"(a)); }
DI void v_fmac(float& d) { asm volatile("v_fma_f32 %0, %0, %1, %1" : "+v"(d) : "s"(-1.4426950408889634f)); }
DI void v_rcp(float& d) { asm volatile("v_rcp_f32 %0, %0" : "+v"(d)); }
DI unsigned v_cvt(float a, float b) { unsigned d; asm volatile("v_cvt_pk_bf16_f32 %0, %1, %2" : "=v"(d) : "v"(a), "v"(b)); return d; }

DI float fmax1(float a, float b) { return __builtin_amdgcn_fmed3f(a, b, __builtin_inff()); }
DI float xadd16(float v) { auto r = __builtin_amdgcn_permlane16_swap(__float_as_uint(v), __float_as_uint(v), false, false); return __uint_as_float(r[0]) + __uint_as_float(r[1]); }
DI float xadd32(float v) { auto r = __builtin_amdgcn_permlane32_swap(__float_as_uint(v), __float_as_uint(v), false, false); return __uint_as_float(r[0]) + __uint_as_float(r[1]); }
DI float add2(float u, float v) {
  auto r = __builtin_amdgcn_permlane16_swap(__float_as_uint(u), __float_as_uint(v), false, false);
  return xadd32(__uint_as_float(r[0]) + __uint_as_float(r[1]));
}
DI f32x2 spread2(float t) {
  auto r = __builtin_amdgcn_permlane16_swap(__float_as_uint(t), __float_as_uint(t), false, false);
  return (f32x2){__uint_as_float(r[0]), __uint_as_float(r[1])};
}
template <int CTRL> DI float dpp_add(float v) { return v + __uint_as_float(__builtin_amdgcn_update_dpp(0, __float_as_uint(v), CTRL, 0xf, 0xf, true)); }
DI f32x2 red8(const f32x2 (&a)[4]) {
  f32x2 o;
#pragma unroll
  for (int e = 0; e < 2; ++e) {
    auto r0 = __builtin_amdgcn_permlane16_swap(__float_as_uint(a[0][e]), __float_as_uint(a[1][e]), false, false);
    auto r1 = __builtin_amdgcn_permlane16_swap(__float_as_uint(a[2][e]), __float_as_uint(a[3][e]), false, false);
    const float u = __uint_as_float(r0[0]) + __uint_as_float(r0[1]), v = __uint_as_float(r1[0]) + __uint_as_float(r1[1]);
    auto r2 = __builtin_amdgcn_permlane32_swap(__float_as_uint(u), __float_as_uint(v), false, false);
    o[e] = dpp_add<0x128>(__uint_as_float(r2[0]) + __uint_as_float(r2[1]));
  }
  return o;
}
DI float row16_sum(float v) {
  v = dpp_add<0xB1>(v);
  v = dpp_add<0x4E>(v);
  v = dpp_add<0x141>(v);
  v = dpp_add<0x140>(v);
  return v;
}
DI f32x2 unpk(unsigned u) { return (f32x2){lo2f(u), hi2f(u)}; }
DI f32x2 rbf2(f32x2 v) { return (f32x2){rbf(v[0]), rbf(v[1])}; }
DI float ln_rstd(float var) { return __builtin_amdgcn_rcpf(rbf(__builtin_amdgcn_sqrtf(rbf(rbf(var) + 1e-5f)))); }
DI float silu_bf(u16 c) { const float x = bf2f(c); return rbf(x * __builtin_amdgcn_rcpf(1.f + __builtin_amdgcn_exp2f(x * -1.4426950408889634f))); }
DI unsigned umax4(const u32x4& v) { return max(max(v[0], v[1]), max(v[2], v[3])); }
DI unsigned umax16(const u32x4 (&v)[4]) { return max(max(umax4(v[0]), umax4(v[1])), max(umax4(v[2]), umax4(v[3]))); }
template <class T> DI T lds_ld(unsigned a) { return *(const LDS_PTR(T))(uintptr_t)a; }
template <class T> DI void lds_st(unsigned a, const T& v) { *(LDS_PTR(T))(uintptr_t)a = v; }
DI u32x4 swz_rows(const u32x4& o) {
  auto r0 = __builtin_amdgcn_permlane16_swap(o[0], o[2], false, false);
  auto r1 = __builtin_amdgcn_permlane16_swap(o[1], o[3], false, false);
  return (u32x4){r0[0], r1[0], r0[1], r1[1]};
}

constexpr unsigned W_H1 = 1u << 16, W_O = W_H1 + (4u << 20), W_X1 = W_O + (4u << 20), W_H2 = W_X1 + (4u << 20), W_HH = W_H2 + (4u << 20);
constexpr unsigned W_PART = 32u << 20, W_LATE = W_PART + (128u << 10), W_LN1 = W_LATE + (128u << 10), W_FLAGS = W_LN1 + (32u << 10), W_STATS = W_FLAGS + (128u << 10), W_HALO = W_STATS + (256u << 10);
constexpr unsigned W_XCH = 34u << 20, W_END = 42u << 20;
enum { PH_LN1 = 0, PH_G1 = 2, PH_G2, PH_G3, PH_G4 };
constexpr unsigned POISON = 0xffffffffu;
constexpr int AUX_SC1 = 16;
constexpr unsigned SPIN_CAP = 1u << 17;
constexpr int SLOT = 16384, L_KV = 8 * SLOT, L_WV = L_KV + 20480, L_RED = L_WV + 4096, L_TCH = L_RED + 1024, LDS_TOTAL = 163840;

#define STAMP()
#define STAMP0()
#define FST()
DI int opaque_tid() { int t = threadIdx.x; asm volatile("" : "+v"(t)); return t; }
DI int opaque_bid() { int b = blockIdx.x; asm volatile("" : "+s"(b)); return b; }

DI void map_g1(int b, int& tm, int& tn) { const int xcd = b & 7, idx = b >> 3; tm = (xcd & 1) * 8 + (idx & 7); tn = (xcd >> 1) * 4 + (idx >> 3); }
DI void map_g2(int b, int& tm, int& tn) { const int xcd = b & 7, idx = b >> 3; tm = (xcd & 3) * 4 + (idx >> 3); tn = (xcd >> 2) * 8 + (idx & 7); }
DI void map_g3(int b, int& tm, int& tn) { const int xcd = b & 7, idx = b >> 3; tm = (xcd >> 2) * 8 + (idx >> 2); tn = (xcd & 3) * 4 + ((idx + 2) & 3); }

struct Ctx {
  const u16 *x, *wqkv, *wo, *w1, *w3, *w2, *cc, *wmod; u16* out; char* ws;
  rsrc_t rs, rC, rQ, rO, r1, r3, r2, rM;
  f32x4 acc[13][2][2][2];
  bf16x4 wl[8][2], wh[8][2];
  float t[8]; u32x4 o;
  unsigned bW[2][2], bM[2][2], vA, vK[4], v1[4], v2[4], v3[4], vM[4];
  unsigned lds0, sB, sA, mB, so[2], sB4[2][2], sA4[2], fl3, fl4, xs4;
  int wave; unsigned long long pc0;
  u32x4 kf[2][2], vf[2][2], hl[4]; unsigned kvw, hso;
  u32x4 xr[2][2], lw[8]; float ls[8]; f32x2 ga[4];
  u32x4 mp[2][4]; unsigned mo, tag[2]; f32x2 eg[2][4], er[2][2][4], sc1[8], sh[8];
  unsigned pollv, pollo, flv, tch, vT1, vT2, vT3, vT4, vT5, vC, vG0; u32x4 xl[8];
};
DI void st16(const Ctx& c, unsigned off, const u32x4& v) { __builtin_amdgcn_raw_buffer_store_b128(v, c.rs, off, 0, AUX_SC1); }
DI void ld16x4(const Ctx& c, u32x4 (&v)[4], unsigned off, unsigned step) {
  asm volatile("buffer_load_dwordx4 %0, %4, %5, 0 offen sc1\n\tbuffer_load_dwordx4 %1, %4, %5, %6 offen sc1\n\tbuffer_load_dwordx4 %2, %4, %5, %7 offen sc1\n\tbuffer_load_dwordx4 %3, %4, %5, %8 offen sc1\n\ts_waitcnt vmcnt(0)"
               : "=&v"(v[0]), "=&v"(v[1]), "=&v"(v[2]), "=&v"(v[3]) : "v"(off), "s"(c.rs), "s"(step), "s"(2 * step), "s"(3 * step) : "memory");
}
DI void retry(const Ctx& c, u32x4 (&v)[4], unsigned off, unsigned step) {
  if (__builtin_expect(__any(umax16(v) == POISON), 0)) {
#pragma unroll 1
    for (unsigned spins = 0; spins < SPIN_CAP; ++spins) { ld16x4(c, v, off, step); if (!__any(umax16(v) == POISON)) break; __builtin_amdgcn_s_sleep(1); }
  }
}
DI bool tagged(const Ctx& c, const u32x4 (&v)[4]) {
  const unsigned d = ((v[0][2] ^ c.tag[0]) | (v[1][2] ^ c.tag[0]) | (v[2][2] ^ c.tag[0]) | (v[3][2] ^ c.tag[0])) | ((v[0][3] ^ c.tag[1]) | (v[1][3] ^ c.tag[1]) | (v[2][3] ^ c.tag[1]) | (v[3][3] ^ c.tag[1]));
  return __all(d == 0);
}
DI unsigned flag_off(int tm, int wave, int col, int ph) { return W_FLAGS + ((tm * 4 + wave) * 16 + col) * 128 + ph * 4; }
DI void set_flag(const Ctx& c, unsigned off, unsigned val) {
  asm volatile("s_mov_b64 exec, 1\n\tbuffer_store_dword %0, off, %1, %2 sc1\n\ts_mov_b64 exec, -1" ::"v"(val), "s"(c.rs), "s"(off) : "memory");
}
DI void spin(const Ctx& c, unsigned voff, unsigned need) {
#pragma unroll 1
  for (unsigned spins = 0; spins < SPIN_CAP; ++spins) {
    if (__all(__builtin_amdgcn_raw_buffer_load_b32(c.rs, voff, 0, AUX_SC1) >= need)) break;
    __builtin_amdgcn_s_sleep(1);
    asm volatile("" ::: "memory");
  }
  asm volatile("" ::: "memory");
}
template <int PH> DI void await(const Ctx& c, unsigned line) {
#pragma unroll 1
  for (unsigned spins = 0; spins < SPIN_CAP; ++spins) {
    const u32x4 v = __builtin_amdgcn_raw_buffer_load_b128(c.rs, line, 0, AUX_SC1);
    if (__all(v[0] == c.tag[0] && v[1] == c.tag[1] && (PH == PH_LN1 || v[PH] != 0))) break;
    __builtin_amdgcn_s_sleep(1);
    asm volatile("" ::: "memory");
  }
  asm volatile("" ::: "memory");
}

#define L4 7
#define NP1 8
#define NP3 8
#define PRE3 4
#define BS 4
#define QW 16
#define HP4 11
#define LG 5
#define AW 1000
#define DENSE 20
#define VQ 2
#define GNT " nt"
#define OUTM 2
#define OUTA 16
#define CAP 7
#define NFD 12
#define NF1 0
#define NF2 4
#define XD1 12
#define XD2 8
#define XD3 10
namespace en {
constexpr int RD = 4, NT = 116, NH = 8 * NT, T3 = 32, T4 = 88, NB4 = 14 - L4;
constexpr int seg_t0(int s) { return s == 1 ? 0 : s == 2 ? 24 : s == 3 ? T3 : NT; }
constexpr int seg_of(int H) { return H < 192 ? 1 : H < 256 ? 2 : 3; }
constexpr int seg_np(int s) { return s == 1 ? NP1 : s == 2 ? 8 : NP3; }
constexpr int seg_pre(int s) { return s == 3 ? PRE3 : 8; }
enum { U_K, U_V, U_Q, U_O, U_G0, U_U0, U_G1, U_U1, U_M, U_G2, U_U2, U_S, U_P };
constexpr int unit(int T) {
  if (T >= T4) { const int s = T - T4; return U_S + (s < 2 * NB4 ? s & 1 : s >= 2 * NB4 + L4); }
  return T < 32 ? T >> 3 : T < 64 ? U_G0 + ((T - 32) >> 4) * 2 + (T & 1) : T < 72 ? U_M : T - 72 < LG || (T - 72 < 16 - LG && ((T - 72 - LG) & 1)) ? U_G2 : U_U2;
}
constexpr int grp(int T) {
  if (T >= T4) { const int s = T - T4; return s < 2 * NB4 ? s / 2 : s < 2 * NB4 + L4 ? s - NB4 : s - NB4 - L4; }
  return T < 32 ? T & 7 : T < 64 ? (T >> 1) & 7 : T < 72 ? T & 7 : T - 72 < LG ? T - 72 : T - 72 >= 16 - LG ? T - 80 : (T - 72 - LG) / 2 + (unit(T) == U_G2 ? LG : 0);
}
constexpr int u2stage(int j) { int T = 72; while (unit(T) != U_U2 || grp(T) != j) ++T; return T; }
constexpr int g4first(int j) { return T4 + (j < NB4 ? 2 * j : j + NB4); }
constexpr int g4last(int j) { return T4 + (j < NB4 ? 2 * j + 1 : j + NB4 + L4); }
constexpr int g4kb(int j) { return j < 8 ? 32 * (j / 4) + 4 * (j % 4) : j < 10 ? 96 + 4 * (j - 8) : 64 + 4 * (j - 10); }
constexpr bool g4mix(int j) { return j == 8 || j == 9; }
constexpr bool barst(int T) { return T < NT - 1 && (BS == 1 || (T & 1) || (BS == 2 && (T - seg_t0(seg_of(8 * T)) < 16 || T >= 72)) || (BS == 4 && ((T >= 8 && T < 24) || (T >= 46 && T < 76) || (T > 103 && T < 110))) || T == 6 || T == 8); }
constexpr bool isbar(int H) { return (H & 7) == 8 - RD && barst(H >> 3); }
constexpr int cert(int T) { int b = T - 1; while (!barst(b)) --b; return 8 * b + 8 - RD; }
enum { K_A = 1, K_D, K_A4, K_P, K_XS, K_XL, K_M, K_T, K_C, K_G };
enum { A_CHK1 = 1, A_CHK2, A_CHK3, A_CHK4, A_FLAG, A_FLAG4, A_CHKX };
enum { P_K, P_GA, P_V, P_GB, P_GC, P_PREV, P_GD, P_GR, P_GO, PR_HH };
constexpr int NPR = PR_HH + 28, HH_ST = 13;
constexpr bool pr_up(int i) { return i - PR_HH < 20 ? (i - PR_HH) & 1 : i - PR_HH >= 24; }
constexpr int pr_blk(int i) { return i - PR_HH < 20 ? (i - PR_HH) / 2 : 10 + (i - PR_HH) % 4; }
constexpr int pr_avail(int i) {
  const int a[PR_HH] = {64, 106, 128, 0, 146, 165, 0, 0, 187}, n = i - PR_HH;
  return n < 0 ? a[i] : 8 * T3 + (n < 8 ? 128 : n < 16 ? 256 : n < 20 ? 320 : n < 24 ? 448 - 8 * LG : 448) - (n < 16 && !pr_up(i) ? 8 : 0);
}
constexpr int pr_len(int i) { return i == P_K || i == P_V ? 24 : i == P_PREV || i == P_GR || i == P_GO ? 1 : i < PR_HH ? 32 : pr_up(i) ? HH_ST + 1 : 40; }
struct Plan { short op[NH + 1], e[4 * NH], act[NH + 1], vw[NH + 1], d0[5], d1[5], slack, halo; };
constexpr Plan mk() {
  Plan p{};
  int atA[64] = {}, atD[4 * NT] = {}, atA4[112] = {}, req[NH + 1] = {}, nvm[NH + 1] = {};
  bool quiet[NH + 64] = {};
  for (int H = 0; H <= NH; ++H) req[H] = -1000;
  auto need = [&](int H, int x) { if (x > req[H]) req[H] = x; };
  auto window = [&](int H, int o0, int o1 = 0, int o2 = 0, int o3 = 0, int o4 = 0, int o5 = 0, int o6 = 0) {
    const int o[7] = {o0, o1, o2, o3, o4, o5, o6};
    for (int i = 0; i < 7 && o[i]; ++i) p.op[H++] = o[i];
    for (int i = 0; i < QW && seg_of(H + i) == seg_of(H - 1); ++i) quiet[H + i] = true;
  };
  auto poll = [&](int H, int a) { p.op[H - 16] = K_P * 1024; p.act[H] = a; need(H, H - 16); };
  constexpr int T = K_T * 1024, G = K_G * 1024, C = K_C * 1024, H3 = 8 * T3, H4 = 8 * T4;
  window(84, G, G + 1, G + 2, G + 3); window(120, G + 4, G + 5, G + 6, G + 7); window(153, C, T, T + 1, T + 2); window(172, C + 1, T + 3, T + 4);
  need(108, 87); need(148, 123);
  window(H3 + 130, C, T + 5, T + 6); window(H3 + 168, T + 7, T + 8); window(H3 + 270, C, T + 9, T + 10); window(H3 + 310, T + 11, T + 12);
  p.op[58] = T + 23; p.op[210] = T + 17; p.op[211] = T + 18; p.op[H3 + 67] = T + 19; p.op[H3 + 75] = T + 20; p.op[H3 + 235] = T + 21; p.op[H3 + 243] = T + 22;
  p.op[H3 + 366] = T + 13; p.op[H3 + 430] = T + 14; p.op[H3 + 496] = T + 16; p.op[H3 + 460] = C; p.op[H3 + 506] = T + 15; p.op[H3 + 546] = T + 16; p.op[H3 + 548] = T + 15;
  const int HA0 = H4 - 8 * LG;
  const int chk[4] = {H3 + 354, H3 + 410, H4 + 18, H4 + 8 * HP4 + 16}, hx0 = 8 * g4first(13) + 10;
  for (int i = 0; i < 4; ++i) poll(chk[i], A_CHK1 + i);
  int nd = 0, n4 = 0, nxs = 0, nxl = 0, hchk = 100000;
  p.slack = 1000;
  for (int s = 1; s <= 3; ++s) {
    const int T0 = seg_t0(s), T1 = seg_t0(s + 1), dbl = s == 3;
    int na = 2 * seg_pre(s), lim = T0 + 7;
    p.d0[s] = nd; if (nd < 4 * (T0 + seg_np(s))) nd = 4 * (T0 + seg_np(s)); p.d1[s] = nd;
    for (int n = 0; n < na; ++n) atA[n] = 8 * T0 - na + n;
    auto useA = [&](int n) { return 8 * T0 + (n >> 3) * (8 << dbl) + (n & 6); };
    for (int H = 8 * T0; H < 8 * T1; ++H) {
      if (isbar(H)) lim = (H >> 3) + 7;
      if (p.op[H] || quiet[H]) continue;
      if (H >= hx0 && nxs < 8) {
        p.op[H] = K_XS * 1024 + nxs++;
        if (nxs == 8) {
          const int hf = (H + XD1 + 1) & ~1; hchk = (hf + XD2 + XD3 + 1) & ~1;
          p.act[hf] = A_FLAG4; need(hf, H); p.op[hf + XD2] = K_P * 1024 + 1; p.act[hchk] = A_CHKX; need(hchk, hf + XD2);
        }
        continue;
      }
      if (H >= hchk && nxl < 8) { p.op[H] = K_XL * 1024 + nxl++; continue; }
      if (CAP < 8 && s == 3 && (H & 7) == 3 && H - 8 * T0 > 8 && H < HA0 - 40) continue;
      const int Td = nd >> 2, j = n4 >> 3;
      const bool dok = Td < NT && Td <= lim && Td < T1 + 8;
      const int dd = !dok ? 100000 : Td < T1 ? cert(Td) : 8 * T1 + 2 * (Td - T1);
      int da = 100000;
      if (na < 64) da = useA(na);
      else if (n4 < 112 && H >= 8 * (j < 8 ? u2stage(j) : g4last(j - 8)) + (n4 & 6) + 2 && H >= chk[j < 8 ? j >> 2 : j < 10 ? 2 : 3] && (H < HA0 || H >= cert(g4first(j)) - AW)) da = cert(g4first(j));
      if (da <= dd && da < 100000) { if (na < 64) { atA[na] = H; p.op[H] = K_A * 1024 + na++; } else { atA4[n4] = H; p.op[H] = K_A4 * 1024 + n4++; } }
      else if (dok && (H >= 8 * (Td - (barst(Td - 7) && barst(Td - 8) ? 7 : 6)) + 4 + 2 * (nd & 3) || da < 100000)) { atD[nd] = H; p.op[H] = K_D * 1024 + nd++; }
    }
    if (nd < 4 * T1 || na < 64) p.slack = -5;
    for (int n = 1; n < 64; n += 2) need(useA(n), atA[n]);
    for (int t = p.d1[s] >> 2; t < T1; ++t) need(cert(t), atD[4 * t + 3]);
  }
  for (int j = 0; j < 14; ++j) need(cert(g4first(j)), atA4[8 * j + 7]);
  if (n4 < 112 || nxl < 8 || nd < 4 * NT) p.slack = -1;
  int pr = 0, ix = 0;
  for (int q = 0; q < 4 * NH; ++q) {
    const int H = q >> 2, M = q & 3, k = p.op[H] / 1024, i = p.op[H] % 1024;
    const bool prep = (k == K_D && !(i & 3)) || ((k == K_A || k == K_A4) && !(i & 7)) || k == K_T || k == K_C || k == K_G;
    const bool busy = (M == 2 && k) || (M == 1 && prep) || (M == 0 && k == K_D && !(i & 3)) || (M == 3 && (H & 1));
    if (M == 2 && k) nvm[H]++;
    p.e[q] = -1;
    if (pr == NPR || q < 4 * pr_avail(pr) + 8) continue;
    if (busy && (pr < PR_HH + DENSE || (M == 2 && k) || (M == 3 && (isbar(H + 1) || (pr_up(pr) && ix == HH_ST))))) continue;
    p.e[q] = pr * 64 + ix;
    if (pr == P_PREV) p.halo = H;
    if ((pr == P_GR && H > 182) || (pr == P_GO && H > 191) || (pr == P_V && H > 149)) p.slack = -6;
    if (pr >= PR_HH && pr_up(pr) && ix == HH_ST) {
      nvm[H]++;
      const int n = pr - PR_HH;
      if (n == 7 || n == 15 || n == 19 || n == 27) {
        int hb = H + 22; while (!isbar(hb) || p.act[hb]) ++hb;
        p.act[hb] = A_FLAG; need(hb, H);
        if (hb + 16 > chk[n == 7 ? 0 : n == 15 ? 1 : n == 19 ? 2 : 3] - 16) p.slack = -3;
      }
    }
    if (++ix == pr_len(pr)) { ix = 0; ++pr; }
  }
  if (pr < NPR) p.slack = -2;
  for (int H = 0; H < NH; ++H) {
    int n = -1;
    if (req[H] != -1000) {
      const int h0 = 8 * seg_t0(seg_of(H));
      n = req[H] < h0 ? h0 - 1 - req[H] : 0;
      for (int h = req[H] < h0 ? h0 : req[H] + 1; h < H; ++h) n += nvm[h];
      if (req[H] >= h0 && H - req[H] < p.slack) p.slack = H - req[H];
    }
    p.vw[H] = n > 63 ? 63 : n;
  }
  { int n = 0; for (int h = p.halo + 1; h < 192; ++h) n += nvm[h]; p.halo = n; }
  return p;
}
constexpr Plan PL = mk();
static_assert(PL.slack >= 8, "every load is issued well before it is needed");
constexpr int seg_end(int H) { return 8 * seg_t0(seg_of(H) + 1); }

#define PGTAB 59, 271, 474, 883, 1149, 1364, 1569, 1757, 1962, 2160, 2345, 2533, 2731, 2906, 3107, 3323, 3534
constexpr int PGS[] = {PGTAB}, NPG = sizeof(PGS) / sizeof(int);
constexpr int pg_of(int q) { for (int i = 0; i < NPG; ++i) if (PGS[i] == q) return i; return -1; }
constexpr int pg_grp(int i) { return i < NPG && PGS[i] >= 0 ? PGS[i] >= 32 * T3 : -1; }
constexpr int pg_first(int g) { int i = 0; while (i < NPG && pg_grp(i) != g) ++i; return i; }
constexpr int pg_lab(int i, int g) { return pg_grp(i) == g ? i : 1000 + g; }
template <int I> DI void pg_mark() { asm volatile(".Lpg%c0:\n\ts_cbranch_execz .Lpg%c1" ::"i"(I), "i"(pg_lab(I + 4, pg_grp(I)))); }
template <int G> DI void pg_hop(int wave) {
  constexpr int f = pg_first(G);
  asm volatile("s_mov_b64 exec, 0\n\ts_cmp_eq_u32 %0, 0\n\ts_cbranch_scc1 .Lpg%c1\n\ts_cmp_eq_u32 %0, 1\n\ts_cbranch_scc1 .Lpg%c2\n\ts_cmp_eq_u32 %0, 2\n\ts_cbranch_scc1 .Lpg%c3\n\ts_branch .Lpg%c4\n"
               ".Lpg%c5:\n\ts_mov_b64 exec, -1" ::"s"(wave), "i"(pg_lab(f, G)), "i"(pg_lab(f + 1, G)), "i"(pg_lab(f + 2, G)), "i"(pg_lab(f + 3, G)), "i"(1000 + G) : "scc", "memory");
}
template <int H, int R> DI void rd(Ctx& c) {
  constexpr bool mx = unit(H >> 3) == U_M;
  constexpr int cb = R >> 1, hi = R & 1, off = ((H >> 3) & 7) * SLOT + ((H >> 1) & 3) * 4096 + (H & 1) * (mx ? 1024 : 256) + hi * 2048;
  const unsigned ad = (mx ? c.bM[cb][off >> 16] : c.bW[cb][off >> 16]) + (off & 65535);
  if constexpr (hi) c.wh[H & 7][cb] = ldsT(ad); else c.wl[H & 7][cb] = ldsT(ad);
}
template <int T> DI void dma_off(Ctx& c) {
  constexpr int u = unit(T), j = grp(T);
  if constexpr (u <= U_Q) c.sB = j * 128 * 6144 + (u + 1) % 3 * 2048;
  else if constexpr (u == U_O) c.sB = c.sB4[0][0] + j * 128 * 2048;
  else if constexpr (u < U_S) c.sB = j * 128 * F_ * 2 + (u == U_M ? 0 : u >= U_G2 ? 4096 : u >= U_G1 ? 2048 : 0);
  else c.sB = c.sB4[g4mix(j)][u - U_S] + g4kb(j) * 32 * 2048;
  pins(c.sB);
}
template <int D> DI void dma(Ctx& c) {
  constexpr int u = unit(D >> 2), i = D & 3;
  if constexpr (u <= U_Q) bdma<i * 1024>(c.v1[i], c.rQ, c.sB);
  else if constexpr (u == U_O) bdma<i * 1024>(c.v2[i], c.rO, c.sB);
  else if constexpr (u == U_M) bdma<i * 1024>(c.vM[i], (i & 1) ? c.r3 : c.r1, c.sB);
  else if constexpr (u < U_S) bdma<i * 1024>(c.v3[i], u == U_U0 || u == U_U1 || u == U_U2 ? c.r3 : c.r1, c.sB);
  else bdma<i * 1024>(c.v2[i], c.r2, c.sB);
}
template <int D> DI void dma1(Ctx& c) {
  if constexpr (!(D & 3)) { dma_off<(D >> 2)>(c); m0_add<((D >> 2) & 7) * SLOT>(c.mB); nops<1>(); }
  dma<D>(c);
}
template <int B, int... Ds> DI void dmas_(Ctx& c, iseq<Ds...>) { (dma1<B + Ds>(c), ...); }
template <int S, int A, int B> DI void dmas(Ctx& c) {
  constexpr int n = PL.d1[S] - PL.d0[S], a = A < n ? A : n, b = B < n ? B : n;
  GAP(); dmas_<PL.d0[S] + a>(c, make_iseq<b - a>{}); GAP();
}
template <int S, int B, int E, int... Is> DI void dma_at_(Ctx& c, int n, iseq<Is...>) { ((n == Is && B + Is < E ? dmas<S, B + Is, B + Is + 1>(c) : void()), ...); }
template <int S, int B, int E> DI void dma_at(Ctx& c, int n) { dma_at_<S, B, E>(c, n, make_iseq<16>{}); }
constexpr int NFREE2 = 4 * 31 - PL.d0[2];
constexpr int NFREE3 = 4 * 39 - PL.d0[3];

template <int N, bool UP, int I> DI void e_hh(Ctx& c) {
  constexpr bool mx = N == 8 || N == 9;
  constexpr int n = N < 8 ? N : mx ? N - 8 : N - 2, S = (n >> 1) & 1, B = n & 1, ug = mx ? U_M : N < 8 ? U_G0 + (N >> 2) * 2 : U_G2, e = I & 7, ph = I >> 3;
  f32x4& g = c.acc[ug][S][B][e >> 2];
  float r;
  if constexpr (UP) {
    if constexpr (I < 8) v_mul(c.t[e], g[e & 3], c.acc[mx ? U_M : ug + 1][mx ? 1 : S][B][e >> 2][e & 3]);
    else if constexpr (I < 12) c.o[e] = v_cvt(c.t[2 * e], c.t[2 * e + 1]);
    else if constexpr (I == HH_ST) asm volatile("buffer_store_dwordx4 %0, %1, %2, %3 offen offset:%4 sc1" ::"v"(c.o), "v"(c.vA), "s"(c.rs), "s"(c.so[mx] + (mx ? 96 : (n >> 2) * 32 + S) * KBS), "n"(B * 1024) : "memory");
  } else if constexpr (ph == 0) { v_mulc(r, g[e & 3]); g[e & 3] = r; }
  else if constexpr (ph == 1) v_exp(c.t[e], g[e & 3]);
  else if constexpr (ph == 2) v_fmac(c.t[e]);
  else if constexpr (ph == 3) v_rcp(c.t[e]);
  else { v_mul(r, g[e & 3], c.t[e]); g[e & 3] = r; }
}
template <int U, int F> DI void e_kv(Ctx& c) {
  u32x4 (&d)[2][2] = U ? c.vf : c.kf;
  if constexpr (F < 16) { const f32x4& a = c.acc[U][F >> 3][(F >> 2) & 1][(F >> 1) & 1]; d[F >> 3][(F >> 2) & 1][F & 3] = v_cvt(a[2 * (F & 1)], a[2 * (F & 1) + 1]); }
  else if constexpr (F == 18 || F == 22) lds_st(c.kvw + 4096 + U * 2048 + (F == 22) * 1024, d[F == 22][1]);
  else if constexpr (F == 17) {
    if (c.wave == 3) {
#pragma unroll
      for (int h = 0; h < 2; ++h) {
        const u32x4 v = d[h][1];
        st16(c, c.vA + c.hso + 65536 + U * 2048 + h * 1024, (u32x4){min(v[0], POISON - 1), min(v[1], POISON - 1), min(v[2], POISON - 1), min(v[3], POISON - 1)});
      }
    }
  }
}
DI void e_prev(Ctx& c) {
  asm volatile("s_cmp_eq_u32 %4, 0\n\ts_cbranch_scc1 1f\n\tds_read_b128 %0, %5\n\tds_read_b128 %1, %5 offset:1024\n\tds_read_b128 %2, %5 offset:2048\n\tds_read_b128 %3, %5 offset:3072\n\ts_branch 2f\n"
               "1:\n\tbuffer_load_dwordx4 %0, %6, %7, %8 offen sc1\n\tbuffer_load_dwordx4 %1, %6, %7, %8 offen offset:1024 sc1\n\tbuffer_load_dwordx4 %2, %6, %7, %8 offen offset:2048 sc1\n\tbuffer_load_dwordx4 %3, %6, %7, %8 offen offset:3072 sc1\n2:"
               : "=&v"(c.hl[0]), "=&v"(c.hl[1]), "=&v"(c.hl[2]), "=&v"(c.hl[3]) : "s"(c.wave), "v"(c.kvw), "v"(c.vA), "s"(c.rs), "s"(c.hso < W_HALO ? W_END : c.hso) : "scc", "memory");
}
DI void gemv_out(const Ctx& c, unsigned off, int lane) {
  const unsigned rb = c.lds0 + L_RED + (lane & 31) * 8;
  const f32x2 sum = (lds_ld<f32x2>(rb) + lds_ld<f32x2>(rb + 256)) + (lds_ld<f32x2>(rb + 512) + lds_ld<f32x2>(rb + 768));
  if (lane < 32) st16(c, off + lane * 16, (u32x4){__float_as_uint(sum[0]), __float_as_uint(sum[1]), c.tag[0], c.tag[1]});
}
template <int P, int I> DI void e_gv(Ctx& c) {
  if constexpr (P == P_GR) lds_st(c.lds0 + L_RED + c.wave * 256 + ((c.vA >> 4) & 7) * 32 + (c.vA >> 8) * 8, red8(c.ga));
  else if constexpr (P == P_GO) { if (c.wave == 3) gemv_out(c, W_LATE + blockIdx.x * 512, c.vA >> 4); }
  else {
    constexpr int r = 2 * (P == P_GA ? 0 : P == P_GB ? 1 : P == P_GC ? 2 : 3) + (I >> 4), e = I & 7;
    if constexpr (!(I & 8) && (e & 1)) asm volatile("v_and_b32 %0, 0xffff0000, %1" : "=v"(c.t[e]) : "v"(c.lw[r][e >> 1]));
    else if constexpr (!(I & 8)) asm volatile("v_lshlrev_b32 %0, 16, %1" : "=v"(c.t[e]) : "v"(c.lw[r][e >> 1]));
    else {
      float a = r ? c.ga[e >> 1][e & 1] : 0.f;
      if constexpr (r == 0) v_mul(a, c.t[e], c.ls[r]); else asm volatile("v_fmac_f32 %0, %1, %2" : "+v"(a) : "v"(c.t[e]), "v"(c.ls[r]));
      c.ga[e >> 1][e & 1] = a;
    }
  }
}
DI void touch(Ctx& c, unsigned v, const rsrc_t& r, unsigned o) { asm volatile("buffer_load_dword %0, %1, %2, %3 offen" : "+v"(c.tch) : "v"(v), "s"(r), "s"(o) : "memory"); }
template <int W> DI void warm(Ctx& c) {
  if constexpr (W == 0) touch(c, c.vT2, c.rO, 0);
  else if constexpr (W < 7) touch(c, c.vT3, (W & 1) ? c.r1 : c.r3, (W - 1) / 2 * 2048);
  else if constexpr (W < 9) touch(c, c.vT4, (W & 1) ? c.r1 : c.r3, 0);
  else if constexpr (W < 13) touch(c, c.vT5, c.r2, (W - 9) * (2 << 20));
  else if constexpr (W < 17) touch(c, c.vT2, c.r2, (W - 13) * (2 << 20));
  else if constexpr (W == 23) touch(c, c.vT1, c.rQ, 0);
  else touch(c, c.vT3 | (c.vA < 256 ? 0x40000000u : 0u), (W & 1) ? c.r1 : c.r3, (W - 17) / 2 * 2048);
}
DI unsigned slow_col(const void* base, int first, int n) {
  const unsigned k = (3 - (unsigned)((uintptr_t)base >> 7) - first) & 7;
  return k < n ? (first + k) * 128 : 0x40000000u;
}
DI void warm_code(Ctx& c) {
  unsigned o;
  asm volatile("s_mov_b32 %0, .-.Lblk_begin" : "=s"(o));
  touch(c, c.vC, c.rC, o);
}
template <int A> DI void action(Ctx& c) {
  if constexpr (A == A_FLAG) { c.flv += 1; set_flag(c, c.fl3, c.flv); }
  else if constexpr (A == A_FLAG4) set_flag(c, c.fl4, c.flv);
  else {
    constexpr unsigned need = A == A_CHKX ? 1 : A;
    pin(c.pollv);
    if (__builtin_expect(!__all(c.pollv >= need), 0)) spin(c, c.pollo + (A == A_CHKX ? (c.fl4 ^ 128) : 0), need);
    if constexpr (A == A_CHK4) { c.pollo = 0; pin(c.pollo); }
  }
}
template <int H> DI void waits(Ctx& c) {
  if constexpr (!(H & 1)) {
    constexpr int v = PL.vw[H] < 0 ? 63 : PL.vw[H];
    __builtin_amdgcn_s_waitcnt((v & 15) | ((v >> 4) << 14) | 0x70 | ((H + 4 <= seg_end(H) ? 8 : 0) << 8));
    asm volatile("" ::: "memory");
  }
  static_assert(!(H & 1) || (PL.vw[H] < 0 && !PL.act[H]), "waits sit in front of even half-steps");
  if constexpr (isbar(H)) bar();
  GAP();
  if constexpr (PL.act[H] != 0) { action<PL.act[H]>(c); GAP(); }
}
template <int H, int M> DI void slot(Ctx& c) {
  constexpr int cb = M >> 1, rb = M & 1, u = unit(H >> 3), j = grp(H >> 3), k = PL.op[H] / 1024, i = PL.op[H] % 1024, e = PL.e[4 * H + M];
  mfa<(j & 7) * 8 + (H & 6) + rb, u == U_V, j == 0 && (H & 6) == 0>(c.acc[u][H & 1][rb][cb], cat(c.wl[H & 7][cb], c.wh[H & 7][cb]));
  if constexpr (pg_of(4 * H + M) >= 0) pg_mark<pg_of(4 * H + M)>();
  GAP();
  if constexpr (H + RD < seg_end(H)) rd<H + RD, M>(c);
  GAP();
  if constexpr (M == 0 && k == K_D && !(i & 3)) dma_off<(i >> 2)>(c);
  if constexpr (M == 1) {
    if constexpr (k == K_D && !(i & 3)) m0_add<((i >> 2) & 7) * SLOT>(c.mB);
    if constexpr (k == K_A && !(i & 7)) { c.sA += 4 * KBS; pins(c.sA); }
    if constexpr (k == K_A4 && !(i & 7)) { c.sA = c.sA4[g4mix(i >> 3)] + g4kb(i >> 3) * KBS; pins(c.sA); }
  }
  if constexpr (M == 2) {
    if constexpr (k == K_D) dma<i>(c);
    if constexpr (k == K_T) warm<i>(c);
    if constexpr (k == K_C && i == 0) warm_code(c);
    if constexpr (k == K_C && i == 1) touch(c, c.vA << 20, c.rC, (((blockIdx.x >> 3) & 7) * 4 + c.wave) << 12);
    if constexpr (k == K_G && i < 8) asm volatile("buffer_load_dwordx4 %0, %1, %2, %3 offen" GNT : "=v"(c.lw[i & 7]) : "v"(c.vG0), "s"(c.rM), "s"(i * 6144 * 2) : "memory");
    if constexpr (k == K_A || k == K_A4) ld_act<i & 63>(c.vK[(i >> 1) & 3], c.rs, c.sA);
    if constexpr (k == K_P) asm volatile("buffer_load_dword %0, %1, %2, %3 offen sc1" : "+v"(c.pollv) : "v"(c.pollo), "s"(c.rs), "s"(i ? c.fl4 ^ 128 : 0) : "memory");
    if constexpr (k == K_XS) asm volatile("buffer_store_dwordx4 %0, %1, %2, %3 offen offset:%4 sc1" ::"v"(c.acc[U_S][i >> 2][(i >> 1) & 1][i & 1]), "v"(c.vA), "s"(c.rs), "s"(c.xs4 + (i >> 2) * 4096), "n"((i & 3) * 1024) : "memory");
    if constexpr (k == K_XL) asm volatile("buffer_load_dwordx4 %0, %1, %2, %3 offen offset:%4 sc1" : "=v"(c.xl[i]) : "v"(c.vA), "s"(c.rs), "s"((c.xs4 ^ 32768) + (i >> 2) * 4096), "n"((i & 3) * 1024) : "memory");
  }
  GAP();
  if constexpr (M == 3 && H + 1 < seg_end(H)) waits<H + 1>(c);
  if constexpr (e >= 64 * PR_HH) e_hh<pr_blk(e / 64), pr_up(e / 64), e % 64>(c);
  else if constexpr (e == 64 * P_PREV) e_prev(c);
  else if constexpr (e >= 0 && (e / 64 == P_K || e / 64 == P_V)) e_kv<e / 64 == P_V, e % 64>(c);
  else if constexpr (e >= 0) e_gv<e / 64, e % 64>(c);
  GAP();
}
template <int H0, int... Hs> DI void hsteps_(Ctx& c, iseq<Hs...>) { ((slot<H0 + Hs, 0>(c), slot<H0 + Hs, 1>(c), slot<H0 + Hs, 2>(c), slot<H0 + Hs, 3>(c)), ...); }
template <int Ta, int Tb> DI void stages(Ctx& c) { hsteps_<8 * Ta>(c, make_iseq<8 * (Tb - Ta)>{}); }
template <int H0, int... Is> DI void rd0(Ctx& c, iseq<Is...>) { (rd<H0 + Is / 4, Is % 4>(c), ...); }
template <int... Is> DI void ld0(Ctx& c, iseq<Is...>) { (((Is & 7) ? void() : (c.sA += 4 * KBS, pins(c.sA)), ld_act<Is>(c.vK[(Is >> 1) & 3], c.rs, c.sA)), ...); }
template <int S> DI void start(Ctx& c, unsigned base) {
  c.sA = base - 4 * KBS;
  ld0(c, make_iseq<2 * seg_pre(S)>{});
  bar();
  rd0<8 * seg_t0(S)>(c, make_iseq<4 * RD>{});
  GAP();
  waits<8 * seg_t0(S)>(c);
}
DI void lane_off(unsigned (&v)[4], int lane, int wave, int pitch, int col) {
  const int g = lane >> 4, q = (lane >> 2) & 3, p = lane & 3;
#pragma unroll
  for (int i = 0; i < 4; ++i) { v[i] = (32 * wave + 8 * i + 4 * (g >> 1) + q) * pitch + col + (g & 1) * 64 + (p ^ (g & 2)) * 16 - 1024 * i; pin(v[i]); }
}
DI void mod_load(Ctx& c) {
  asm volatile("buffer_load_dwordx4 %0, %8, %9, 0 offen sc1\n\tbuffer_load_dwordx4 %4, %8, %9, 0 offen offset:16 sc1\n\tbuffer_load_dwordx4 %1, %8, %9, 0 offen offset:512 sc1\n\tbuffer_load_dwordx4 %5, %8, %9, 0 offen offset:528 sc1\n\t"
               "buffer_load_dwordx4 %2, %8, %9, 0 offen offset:1024 sc1\n\tbuffer_load_dwordx4 %6, %8, %9, 0 offen offset:1040 sc1\n\tbuffer_load_dwordx4 %3, %8, %9, 0 offen offset:1536 sc1\n\tbuffer_load_dwordx4 %7, %8, %9, 0 offen offset:1552 sc1"
               : "=&v"(c.mp[0][0]), "=&v"(c.mp[0][1]), "=&v"(c.mp[0][2]), "=&v"(c.mp[0][3]), "=&v"(c.mp[1][0]), "=&v"(c.mp[1][1]), "=&v"(c.mp[1][2]), "=&v"(c.mp[1][3]) : "v"(c.mo), "s"(c.rs) : "memory");
}
template <int N> DI void mod_wait(Ctx& c) {
  asm volatile("s_waitcnt vmcnt(%8)" : "+v"(c.mp[0][0]), "+v"(c.mp[0][1]), "+v"(c.mp[0][2]), "+v"(c.mp[0][3]), "+v"(c.mp[1][0]), "+v"(c.mp[1][1]), "+v"(c.mp[1][2]), "+v"(c.mp[1][3]) : "n"(N) : "memory");
}
DI void mod_vectors(Ctx& c, int lane) {
  const int g = lane >> 4;
  if (__builtin_expect(!(tagged(c, c.mp[0]) && tagged(c, c.mp[1])), 0)) {
#pragma unroll 1
    for (unsigned spins = 0; spins < SPIN_CAP; ++spins) { mod_load(c); mod_wait<0>(c); if (tagged(c, c.mp[0]) && tagged(c, c.mp[1])) break; }
  }
  dmas<2, 15, 17>(c);
  f32x4 a;
#pragma unroll
  for (int e = 0; e < 4; ++e) { const u32x4 (&m)[4] = c.mp[e >> 1]; a[e] = (__uint_as_float(m[0][e & 1]) + __uint_as_float(m[1][e & 1])) + (__uint_as_float(m[2][e & 1]) + __uint_as_float(m[3][e & 1])); }
  const unsigned pb = c.lds0 + L_WV + c.wave * 1024;
  lds_st(pb + lane * 8, (u32x2){pk2(a[0], a[1]), pk2(a[2], a[3])});
#pragma unroll
  for (int i = 0; i < 4; ++i) {
    dma_at<2, 17, 25>(c, 2 * i);
    const u32x2 gv = lds_ld<u32x2>(pb + i * 32 + g * 8);
    c.eg[i >> 1][2 * (i & 1)] = unpk(gv[0]); c.eg[i >> 1][2 * (i & 1) + 1] = unpk(gv[1]);
    const u32x2 av = lds_ld<u32x2>(pb + 128 + i * 32 + g * 8), sv = lds_ld<u32x2>(pb + 256 + i * 32 + g * 8);
#pragma unroll
    for (int e = 0; e < 2; ++e) { c.sh[2 * i + e] = unpk(av[e]); c.sc1[2 * i + e] = rbf2(unpk(sv[e]) + 1.f); pin(c.sh[2 * i + e]); pin(c.sc1[2 * i + e]); }
    dma_at<2, 17, 25>(c, 2 * i + 1);
    const u32x4 r = c.xr[i >> 1][i & 1];
#pragma unroll
    for (int e = 0; e < 4; ++e) { c.er[i >> 1][i & 1][e] = unpk(r[e]); pin(c.er[i >> 1][i & 1][e]); }
  }
#pragma unroll
  for (int i = 0; i < 8; ++i) pin(c.eg[i >> 2][i & 3]);
}
}

DI u32x4 gated_resid(const f32x4& a0, const f32x4& a1, const f32x2 (&eg)[4], const f32x2 (&er)[4]) {
  u32x4 o;
#pragma unroll
  for (int j = 0; j < 4; ++j) {
    const f32x4& v = j < 2 ? a0 : a1;
    const f32x2 r = (f32x2){v[2 * (j & 1)], v[2 * (j & 1) + 1]} * eg[j] + er[j];
    o[j] = pk2(r[0], r[1]);
  }
  return o;
}

DI void phase1(Ctx& c) {
  using namespace en;
  const int tid = opaque_tid(), lane = tid & 63, b = opaque_bid(), wave = c.wave;
  const int fr = lane & 15, g = lane >> 4, q = (lane >> 2) & 3, p = lane & 3;
  c.tch = 0;
  asm volatile("global_load_dword %0, %1, %2" : "+v"(c.tch) : "v"((b >> 3) * 256 + (tid & 1) * 128), "s"(c.pc0) : "memory");
  GAP();
  int tm, tn; map_g1(b, tm, tn);
  int tm2, tn2; map_g2(b, tm2, tn2);
  u32x4 gw[4], xs[4], lc; u32x2 gc, ep;
  c.rM = mk_rsrc(c.wmod, D_ * 6 * D_ * 2);
  {
    const int k0 = (b & 7) * 128 + (tid >> 3) * 4;
    asm volatile("global_load_dwordx4 %0, %1, %2" : "=v"(lc) : "v"((tid >> 3) * 16), "s"(c.cc + (b & 3) * 256) : "memory");
    asm volatile("global_load_dwordx2 %4, %10, %11\n\tbuffer_load_dwordx4 %0, %5, %6, 0 offen" GNT "\n\tbuffer_load_dwordx4 %1, %5, %6, %7 offen" GNT "\n\tbuffer_load_dwordx4 %2, %5, %6, %8 offen" GNT "\n\tbuffer_load_dwordx4 %3, %5, %6, %9 offen" GNT
                 : "=&v"(gw[0]), "=&v"(gw[1]), "=&v"(gw[2]), "=&v"(gw[3]), "=&v"(gc)
                 : "v"((k0 * 6144 + (b >> 3) * 64 + (tid & 7) * 8) * 2), "s"(c.rM), "s"(12288), "s"(24576), "s"(36864), "v"(k0 * 2), "s"(c.cc) : "memory");
    asm volatile("global_load_dwordx2 %0, %1, %2 sc1" : "=v"(ep) : "v"(0), "s"(c.ws) : "memory");
    asm volatile("global_load_dwordx4 %0, %4, %5\n\tglobal_load_dwordx4 %1, %4, %5 offset:512\n\tglobal_load_dwordx4 %2, %4, %5 offset:1024\n\tglobal_load_dwordx4 %3, %4, %5 offset:1536"
                 : "=&v"(xs[0]), "=&v"(xs[1]), "=&v"(xs[2]), "=&v"(xs[3]) : "v"((tid >> 5) * 2048 + (tid & 31) * 16), "s"(c.x + (tm2 * 128 + tn2 * 8) * D_) : "memory");
  }
  GAP();
  c.rs = mk_rsrc(c.ws, W_END); c.rQ = mk_rsrc(c.wqkv, D_ * 3 * D_ * 2); c.rO = mk_rsrc(c.wo, D_ * D_ * 2);
  c.r1 = mk_rsrc(c.w1, D_ * F_ * 2); c.r3 = mk_rsrc(c.w3, D_ * F_ * 2); c.r2 = mk_rsrc(c.w2, D_ * F_ * 2);
  { unsigned n; asm volatile("s_mov_b32 %0, .Lblk_end-.Lblk_begin" : "=s"(n)); c.rC = mk_rsrc((const void*)c.pc0, n); }
  c.mB = c.lds0 + wave * 4096;
  pins(c.rs); pins(c.rQ); pins(c.rO); pins(c.rM); pins(c.r1); pins(c.r3); pins(c.r2); pins(c.rC); pins(c.mB);
  const int j8 = ((b >> 3) * 4 + wave) * 8;
  const unsigned slow1 = (j8 + (lane & 7)) * 6144 + slow_col(c.wqkv, ((b >> 1) & 3) * 4, 4), row1 = (b * 4 + wave) * 6144 + fr * 128;
  c.vT1 = sel(lane < 8, slow1, OOB); pin(c.vT1);
  c.vC = sel(lane == 2, (b * 4 + wave) * 128, j8 * 32 + (lane & 1) * 128);
  FST();
  warm_code(c);
  unsigned tk = sel(lane < 16, row1, sel(lane < 24, slow1, OOB)), tvq = sel(lane < 32, row1 + (lane >> 4) * 4096, sel(lane < 40, slow1 + 4096, OOB));
  unsigned i0 = W_HALO + (((tm2 - 1) & 15) * 16 + tn2) * 4096 + wave * 1024 + lane * 16, iz = lane < 16 ? POISON : 0u;
  unsigned i1 = sel(lane < 16, W_STATS + (tm2 * 4 + wave) * 4096 + tn2 * 256 + lane * 16, sel(lane == 16, flag_off(tm2, wave, tn2, PH_G1), OOB));
  unsigned xo = (fr * D_ + (g & 1) * 16 + (g >> 1) * 8) * 2, xo1 = xo + 16 * D_ * 2;
  unsigned o0 = W_PART + ((wave >> 1) * 16 + tn2) * 4096 + (wave & 1) * 2048 + lane * 16, o1 = W_LN1 + (tm2 * 128 + wave * 32 + fr) * 16;
  unsigned fl1 = sel(lane < 16, flag_off(tm, wave, fr, 0), flag_off(min(tm + 1, 15), wave, tn, 0));
  pin(tk); pin(tvq); pin(i0); pin(i1); pin(iz); pin(xo); pin(xo1); pin(o0); pin(o1); pin(fl1);
  lane_off(c.v1, lane, wave, 6144, tn * 128);
  c.vA = lane * 16; pin(c.vA); c.vK[0] = c.vA;
#pragma unroll
  for (int i = 1; i < 4; ++i) { c.vK[i] = c.vA + i * KBS; pin(c.vK[i]); }
  c.vT2 = sel(lane < 16, (b * 4 + wave) * 2048 + lane * 128, sel(lane < 24, (j8 + lane - 16) * 2048 + slow_col(c.wo, ((b >> 2) & 1) * 8, 8), OOB));
  c.vT3 = sel(lane < 16, (b * 4 + wave) * (F_ * 2) + lane * 128, sel(lane < 24, (j8 + lane - 16) * (F_ * 2) + slow_col(c.w1, (b & 3) * 4, 4), OOB));
  c.vG0 = (((b & 3) * 256 + (tid >> 3) * 8) * 6144 + 2048 + (b >> 2) * 64 + (tid & 7) * 8) * 2;
  pin(c.vG0);
  pin(c.vT2); pin(c.vT3);
#pragma unroll
  for (int w = 0; w < 2; ++w) {
    const unsigned bw = c.lds0 + w * 65536 + g * 512 + q * 64 + p * 8;
    c.bW[0][w] = bw + (g & 1) * 32; c.bW[1][w] = bw + ((g & 1) ^ 1) * 32;
    pin(c.bW[0][w]); pin(c.bW[1][w]);
  }
  lane_off(c.v2, lane, wave, 2048, 0);
  c.sB4[0][0] = tn2 * 128;
  c.mo = W_LATE + (g * 16 + tn2) * 2048 + fr * 32; pin(c.mo);
  c.kvw = c.lds0 + L_KV + wave * 4096 + lane * 16; pin(c.kvw);
  c.hso = W_HALO + ((tm - 1) * 16 + tn) * 4096;
  unsigned e_sel[4]; int e_base; f32x2 e_bias;
  {
    e_base = (fr & ~1) - 4 * g;
    const bool first = tm == 0 && wave == 0;
    e_bias = (f32x2){first && e_base < 0 ? -3.0e38f : 0.f, first && e_base < 2 ? -3.0e38f : 0.f};
    const unsigned s0 = (fr & 1) ? 0x01000c0cu : 0x0c0c0100u, s1 = (fr & 1) ? 0x03020c0cu : 0x0c0c0302u, z = 0x0c0c0c0cu;
    e_sel[0] = e_base >= 0 ? z : s0; e_sel[1] = e_base >= 2 ? z : s1; e_sel[2] = e_base >= 0 ? s0 : z; e_sel[3] = e_base >= 2 ? s1 : z;
    pin(e_base); pin(e_bias); pin(e_sel[0]); pin(e_sel[1]); pin(e_sel[2]); pin(e_sel[3]);
  }
  GAP();
  float sv[4];
  asm volatile("s_waitcnt vmcnt(10)" : "+v"(gc), "+v"(lc)::"memory");
#pragma unroll
  for (int i = 0; i < 4; ++i) { sv[i] = silu_bf((u16)(gc[i >> 1] >> (16 * (i & 1)))); pin(sv[i]); }
#pragma unroll
  for (int i = 0; i < 8; ++i) { c.ls[i] = silu_bf((u16)(lc[i >> 1] >> (16 * (i & 1)))); pin(c.ls[i]); }
  GAP();
  if constexpr (VQ == 3) { touch(c, tk, c.rQ, 2048); touch(c, tvq, c.rQ, 0); }
  FST();
  asm volatile("s_waitcnt vmcnt(%5)" : "+v"(gw[0]), "+v"(gw[1]), "+v"(gw[2]), "+v"(gw[3]), "+v"(ep) : "n"(VQ == 3 ? 7 : 5) : "memory");
  c.tag[0] = __builtin_amdgcn_readfirstlane(ep[0]) + 1; c.tag[1] = __builtin_amdgcn_readfirstlane(ep[1]);
  if constexpr (VQ != 3) touch(c, tk, c.rQ, 2048);
  GAP();
  FST();
  {
    f32x2 acc[4];
#pragma unroll
    for (int j = 0; j < 4; ++j) acc[j] = (f32x2){0.f, 0.f};
#pragma unroll
    for (int i = 0; i < 4; ++i) {
#pragma unroll
      for (int j = 0; j < 4; ++j) acc[j] += unpk(gw[i][j]) * sv[i];
    }
    lds_st(c.lds0 + L_RED + wave * 256 + (lane & 7) * 32 + g * 8, red8(acc));
    wait_lgkm<0>(); bar();
    if (wave == 3) gemv_out(c, W_PART + b * 512, lane);
    GAP();
    asm volatile("global_load_dwordx4 %0, %4, %6\n\tglobal_load_dwordx4 %1, %4, %6 offset:64\n\tglobal_load_dwordx4 %2, %5, %6\n\tglobal_load_dwordx4 %3, %5, %6 offset:64"
                 : "=&v"(c.xr[0][0]), "=&v"(c.xr[0][1]), "=&v"(c.xr[1][0]), "=&v"(c.xr[1][1]) : "v"(xo), "v"(xo1), "s"(c.x + (tm2 * 128 + wave * 32) * D_ + tn2 * 64) : "memory");
    if constexpr (VQ == 0) touch(c, tvq, c.rQ, 0);
    asm volatile("s_waitcnt vmcnt(%4)" : "+v"(xs[0]), "+v"(xs[1]), "+v"(xs[2]), "+v"(xs[3]) : "n"(6 + (VQ == 0 || VQ == 3)) : "memory");
    f32x2 f[16], s2 = {0.f, 0.f};
#pragma unroll
    for (int i = 0; i < 16; ++i) { f[i] = unpk(xs[i >> 2][i & 3]); s2 += f[i]; }
    const float mean = xadd16(row16_sum(s2[0] + s2[1])) * (1.f / 1024.f);
    f32x2 q2 = {0.f, 0.f};
#pragma unroll
    for (int i = 0; i < 16; ++i) { const f32x2 d = f[i] - mean; q2 += d * d; }
    const float rstd = ln_rstd(xadd16(row16_sum(q2[0] + q2[1])) * (1.f / 1024.f));
    if (!(lane & 31)) st16(c, W_LN1 + (tm2 * 128 + tn2 * 8 + (tid >> 5)) * 16, (u32x4){__float_as_uint(rbf(mean)), __float_as_uint(rstd), c.tag[0], c.tag[1]});
  }
  st16(c, i0, (u32x4){POISON, POISON, POISON, POISON}); st16(c, i1, (u32x4){iz, iz, iz, iz});
  GAP();
  FST();
  bar();
  dmas<1, 0, NFD>(c);
  if constexpr (VQ == 1) touch(c, tvq, c.rQ, 0);
  FST();
  f32x4 ln;
  {
    u32x4 v[4];
#pragma unroll 1
    for (unsigned spins = 0; spins < SPIN_CAP; ++spins) {
      asm volatile("buffer_load_dwordx4 %0, %4, %6, 0 offen sc1\n\tbuffer_load_dwordx4 %1, %4, %6, 0 offen offset:1024 sc1\n\tbuffer_load_dwordx4 %2, %5, %6, 0 offen sc1\n\tbuffer_load_dwordx4 %3, %5, %6, 0 offen offset:256 sc1\n\ts_waitcnt vmcnt(0)"
                   : "=&v"(v[0]), "=&v"(v[1]), "=&v"(v[2]), "=&v"(v[3]) : "v"(o0), "v"(o1), "s"(c.rs) : "memory");
      if (tagged(c, v)) break;
      __builtin_amdgcn_s_sleep(1);
    }
    pin(c.xr[0][0]); pin(c.xr[0][1]); pin(c.xr[1][0]); pin(c.xr[1][1]);
    FST();
    ln = (f32x4){__uint_as_float(v[2][0]), __uint_as_float(v[2][1]), __uint_as_float(v[3][0]), __uint_as_float(v[3][1])};
    lds_st(c.lds0 + L_RED + wave * 256 + (lane & 31) * 8, (f32x2){xadd32(__uint_as_float(v[0][0]) + __uint_as_float(v[1][0])), xadd32(__uint_as_float(v[0][1]) + __uint_as_float(v[1][1]))});
    wait_lgkm<0>(); bar();
    if constexpr (VQ == 2) touch(c, tvq, c.rQ, 0);
  }
  FST();
  {
    const f32x4 r = lds_ld<f32x4>(c.lds0 + L_RED + (g & 1) * 512 + fr * 16) + lds_ld<f32x4>(c.lds0 + L_RED + (g & 1) * 512 + 256 + fr * 16);
    const f32x2 one = (g & 1) ? (f32x2){1.f, 1.f} : (f32x2){0.f, 0.f};
    const f32x2 m0 = rbf2(rbf2((f32x2){r[0], r[1]}) + one), m1 = rbf2(rbf2((f32x2){r[2], r[3]}) + one);
    const unsigned Mt = c.lds0 + L_WV + wave * 1024, off = W_H1 + 2 * tn2 * KBS + (tm2 * 8 + 2 * wave) * 1024 + lane * 16;
    lds_st(Mt + (lane & 31) * 16, (f32x4){m0[0], m0[1], m1[0], m1[1]});
#pragma unroll
    for (int s = 0; s < 2; ++s) {
      const unsigned a = Mt + s * 128 + g * 16;
      const f32x4 sh0 = lds_ld<f32x4>(a), sh1 = lds_ld<f32x4>(a + 64), sc0 = lds_ld<f32x4>(a + 256), sc1 = lds_ld<f32x4>(a + 320);
      const f32x2 sc[4] = {{sc0[0], sc0[1]}, {sc0[2], sc0[3]}, {sc1[0], sc1[1]}, {sc1[2], sc1[3]}}, sh[4] = {{sh0[0], sh0[1]}, {sh0[2], sh0[3]}, {sh1[0], sh1[1]}, {sh1[2], sh1[3]}};
#pragma unroll
      for (int i = 0; i < 2; ++i) {
        c.xr[i][s] = swz_rows(c.xr[i][s]);
        u32x4 o;
#pragma unroll
        for (int j = 0; j < 4; ++j) {
          dma_at<1, NFD, NFD + NF1>(c, 8 * s + 4 * i + j - (16 - NF1));
          const f32x2 y = rbf2(rbf2(rbf2(unpk(c.xr[i][s][j]) - ln[2 * i]) * ln[2 * i + 1]) * sc[j]) + sh[j];
          o[j] = pk2(y[0], y[1]);
        }
        st16(c, off + s * KBS + i * 1024, o);
      }
    }
  }
  GAP();
  STAMP();
  dmas<1, NFD + NF1, NFD + NF1 + NF2>(c);
  wait_vm<NF2>();
  asm volatile("s_mov_b64 exec, 1\n\tbuffer_store_dwordx2 %0, off, %1, %2 sc1\n\ts_mov_b64 exec, -1" ::"v"((u32x2){c.tag[0], c.tag[1]}), "s"(c.rs), "s"(flag_off(tm2, wave, tn2, 0)) : "memory");
  STAMP();
  bar();
  FST();
  dmas<1, NFD + NF1 + NF2, 64>(c);
  FST();
  pg_hop<0>(wave);
  await<PH_LN1>(c, fl1);
  if (b + wave == 0) asm volatile("s_mov_b64 exec, 1\n\tbuffer_store_dwordx2 %0, off, %1, 0 sc1\n\ts_mov_b64 exec, -1" ::"v"((u32x2){c.tag[0], c.tag[1]}), "s"(c.rs) : "memory");
  STAMP();
  start<1>(c, W_H1 + (tm * 8 + 2 * wave) * 1024);
  STAMP();
  stages<0, 8>(c);
  STAMP();
  stages<8, 16>(c);
  STAMP();
  stages<16, 24>(c);
  mfma_fence();
  STAMP();
  {
    u32x4 qf[2][2];
#pragma unroll
    for (int i = 0; i < 4; ++i) qf[i >> 1][i & 1] = pack8(c.acc[U_Q][i >> 1][i & 1][0], c.acc[U_Q][i >> 1][i & 1][1]);
    asm volatile("s_waitcnt vmcnt(%4) lgkmcnt(0)" : "+v"(c.hl[0]), "+v"(c.hl[1]), "+v"(c.hl[2]), "+v"(c.hl[3]) : "n"(PL.halo) : "memory");
    if (wave == 0) retry(c, c.hl, c.vA + c.hso, 1024);
    dmas<2, 0, 1>(c);
    u32x4 vo[2][2][2];
#pragma unroll
    for (int h = 0; h < 2; ++h)
#pragma unroll
      for (int j = 0; j < 2; ++j) {
        vo[h][0][j] = (u32x4){c.hl[2 + h][2 * j], c.hl[2 + h][2 * j + 1], c.vf[h][0][2 * j], c.vf[h][0][2 * j + 1]};
        vo[h][1][j] = (u32x4){c.vf[h][0][2 * j], c.vf[h][0][2 * j + 1], c.vf[h][1][2 * j], c.vf[h][1][2 * j + 1]};
      }
#pragma unroll
    for (int i = 0; i < 4; ++i) { pin(qf[i >> 1][i & 1]); pin(c.hl[i]); }
    GAP(); nops<2>();
    f32x4 sp[2][2], sc[2][2];
#pragma unroll
    for (int h = 0; h < 2; ++h) { mfv(sp[h][0], c.hl[h], qf[h][0]); mfv(sc[h][0], c.kf[h][0], qf[h][0]); mfv(sp[h][1], c.kf[h][0], qf[h][1]); mfv(sc[h][1], c.kf[h][1], qf[h][1]); }
    GAP();
#pragma unroll
    for (int i = 0; i < 8; ++i) pin(vo[i >> 2][(i >> 1) & 1][i & 1]);
    dmas<2, 1, 2>(c);
    mfma_fence();
    const bool par = fr & 1, cur0 = e_base >= 0, cur1 = e_base >= 2;
    auto red2 = [](float& u, float& v, auto op) {
      auto r = __builtin_amdgcn_permlane16_swap(__float_as_uint(u), __float_as_uint(v), false, false);
      const float m = op(__uint_as_float(r[0]), __uint_as_float(r[1]));
      auto r2 = __builtin_amdgcn_permlane32_swap(__float_as_uint(m), __float_as_uint(m), false, false);
      const float t = op(__uint_as_float(r2[0]), __uint_as_float(r2[1]));
      auto r3 = __builtin_amdgcn_permlane16_swap(__float_as_uint(t), __float_as_uint(t), false, false);
      u = __uint_as_float(r3[0]); v = __uint_as_float(r3[1]);
    };
    f32x2 l[2][2]; float mx[2][2], sm[2][2]; u32x4 pw[2][2];
#pragma unroll
    for (int h = 0; h < 2; ++h)
#pragma unroll
      for (int i = 0; i < 2; ++i) {
        dma_at<2, 2, 6>(c, 2 * h + i);
        const float a0 = sp[h][i][0], a1 = sp[h][i][1], a2 = sp[h][i][2], a3 = sp[h][i][3], b0 = sc[h][i][0], b1 = sc[h][i][1], b2 = sc[h][i][2], b3 = sc[h][i][3];
        const float x0 = cur0 ? b0 : a0, x1 = cur0 ? b1 : a1, x2 = cur1 ? b2 : a2, x3 = cur1 ? b3 : a3;
        const float y0 = par ? x1 : x0, y1 = par ? x3 : x2;
        l[h][i] = rbf2(rbf2((f32x2){y0, y1}) * 0.17677669529663687f);
        if (i == 0) l[h][i] += e_bias;
        mx[h][i] = fmax1(l[h][i][0], l[h][i][1]);
      }
    dmas<2, 6, 7>(c);
#pragma unroll
    for (int h = 0; h < 2; ++h) red2(mx[h][0], mx[h][1], [](float x, float y) { return fmax1(x, y); });
#pragma unroll
    for (int h = 0; h < 2; ++h)
#pragma unroll
      for (int i = 0; i < 2; ++i) {
        if (i == 0) dma_at<2, 7, 9>(c, h);
        const f32x2 t = (l[h][i] - mx[h][i]) * 1.4426950408889634f;
        l[h][i] = (f32x2){__builtin_amdgcn_exp2f(t[0]), __builtin_amdgcn_exp2f(t[1])};
        sm[h][i] = l[h][i][0] + l[h][i][1];
      }
    dmas<2, 9, 10>(c);
#pragma unroll
    for (int h = 0; h < 2; ++h) red2(sm[h][0], sm[h][1], [](float x, float y) { return x + y; });
#pragma unroll
    for (int h = 0; h < 2; ++h)
#pragma unroll
      for (int i = 0; i < 2; ++i) {
        if (i == 0) dma_at<2, 10, 12>(c, h);
        const f32x2 pr = l[h][i] * __builtin_amdgcn_rcpf(sm[h][i]);
        const unsigned pp = pk2(pr[0], pr[1]);
        pw[h][i] = (u32x4){__builtin_amdgcn_perm(pp, pp, e_sel[0]), __builtin_amdgcn_perm(pp, pp, e_sel[1]), __builtin_amdgcn_perm(pp, pp, e_sel[2]), __builtin_amdgcn_perm(pp, pp, e_sel[3])};
        pin(pw[h][i]);
      }
    GAP(); nops<2>();
    f32x4 o[2][2][2];
#pragma unroll
    for (int i = 0; i < 8; ++i) mfv(o[i >> 2][(i >> 1) & 1][i & 1], vo[i >> 2][(i >> 1) & 1][i & 1], pw[i >> 2][(i >> 1) & 1]);
    dmas<2, 12, 13>(c);
    static_assert(NFREE2 >= 13, "slots of these pieces are free");
    mfma_fence();
    const unsigned oo = W_O + 2 * tn * KBS + (tm * 8 + 2 * wave) * 1024 + c.vA;
#pragma unroll
    for (int i = 0; i < 4; ++i) st16(c, oo + (i >> 1) * KBS + (i & 1) * 1024, pack8(o[i >> 1][i & 1][0], o[i >> 1][i & 1][1]));
  }
  GAP();
  mod_load(c);
  STAMP();
  wait_vm<8>();
  set_flag(c, flag_off(tm, wave, tn, PH_G1), 1);
  STAMP();
}

DI void phase2(Ctx& c) {
  using namespace en;
  const int tid = opaque_tid(), lane = tid & 63, b = opaque_bid(), wave = c.wave;
  const int fr = lane & 15, g = lane >> 4;
  int tm, tn; map_g2(b, tm, tn);
  bar();
  dmas<2, 13, 15>(c);
  mod_wait<3>(c);
  mod_vectors(c, lane);
  dmas<2, 25, 64>(c);
  { int tm3, tn3; map_g3(b, tm3, tn3); lane_off(c.v3, lane, wave, F_ * 2, tn3 * 128); }
  STAMP();
  await<PH_G1>(c, flag_off(tm, wave, fr, 0));
  STAMP();
  start<2>(c, W_O + (tm * 8 + 2 * wave) * 1024);
  STAMP();
  stages<24, 32>(c);
  mfma_fence();
  STAMP();
  f32x2 d[2][8]; float s1[2];

#pragma unroll
  for (int i = 0; i < 2; ++i) {
#pragma unroll
    for (int s = 0; s < 2; ++s) {
      dma_at<3, 0, NFREE3>(c, 4 * i + 2 * s);
      c.xr[i][s] = gated_resid(c.acc[U_O][s][i][0], c.acc[U_O][s][i][1], c.eg[s], c.er[i][s]);
      dma_at<3, 0, NFREE3>(c, 4 * i + 2 * s + 1);
#pragma unroll
      for (int j = 0; j < 4; ++j) d[i][4 * s + j] = unpk(c.xr[i][s][j]);
    }
    const f32x2 s2 = ((d[i][0] + d[i][1]) + (d[i][2] + d[i][3])) + ((d[i][4] + d[i][5]) + (d[i][6] + d[i][7]));
    s1[i] = s2[0] + s2[1];
  }
  const f32x2 mu = spread2(add2(s1[0], s1[1]) * (1.f / 64.f));
#pragma unroll
  for (int i = 0; i < 2; ++i) {
    f32x2 q2 = {0.f, 0.f};
#pragma unroll
    for (int j = 0; j < 8; ++j) { if (!(j & 3)) dma_at<3, 8, NFREE3>(c, 2 * i + (j >> 2)); d[i][j] -= mu[i]; q2 += d[i][j] * d[i][j]; }
    s1[i] = q2[0] + q2[1];
  }
  const f32x2 m2 = spread2(add2(s1[0], s1[1]));
  const unsigned sb = W_STATS + (tm * 4 + wave) * 4096;
  if (g == 0) st16(c, sb + tn * 256 + fr * 16, (u32x4){min(__float_as_uint(mu[0]), POISON - 1), min(__float_as_uint(m2[0]), POISON - 1), min(__float_as_uint(mu[1]), POISON - 1), min(__float_as_uint(m2[1]), POISON - 1)});
  GAP();
  dmas<3, 12, (NFREE3 < 16 ? NFREE3 : 16)>(c);
  STAMP();
  u32x4 pv[4];
  pg_hop<1>(wave);
  ld16x4(c, pv, sb + c.vA, 1024);
  retry(c, pv, sb + c.vA, 1024);
  STAMP();
  {
    f32x2 um[4], s2 = {0.f, 0.f}, q2 = {0.f, 0.f};
#pragma unroll
    for (int t = 0; t < 4; ++t) { um[t] = (f32x2){__uint_as_float(pv[t][0]), __uint_as_float(pv[t][2])}; s2 += um[t]; q2 += (f32x2){__uint_as_float(pv[t][1]), __uint_as_float(pv[t][3])}; }
    const float mg = add2(s2[0], s2[1]) * (1.f / 16.f);
    const f32x2 mean = spread2(mg);
    f32x2 e2 = {0.f, 0.f};
#pragma unroll
    for (int t = 0; t < 4; ++t) { const f32x2 dd = um[t] - mean; e2 += dd * dd; }
    q2 += e2 * 64.f;
    const f32x2 rs = spread2(ln_rstd(add2(q2[0], q2[1]) * (1.f / 1024.f))), mb = rbf2(mean);
    const unsigned ob = W_H2 + 2 * tn * KBS + (tm * 8 + 2 * wave) * 1024 + c.vA;
#pragma unroll
    for (int i = 0; i < 2; ++i) {
      const float dm = (mu[i] - mb[i]) * rs[i];
#pragma unroll
      for (int s = 0; s < 2; ++s) {
        u32x4 o;
#pragma unroll
        for (int j = 0; j < 4; ++j) {
          const f32x2 y = (d[i][4 * s + j] * rs[i] + dm) * c.sc1[4 * s + j] + c.sh[4 * s + j];
          o[j] = pk2(y[0], y[1]);
        }
        st16(c, ob + s * KBS + i * 1024, o);
      }
    }
  }
  GAP();
  STAMP();
  drain_vm();
  set_flag(c, flag_off(tm, wave, tn, PH_G2), 1);
  STAMP();
}

DI void phase3(Ctx& c) {
  using namespace en;
  int tid = opaque_tid(), lane = tid & 63; const int b = opaque_bid(), wave = c.wave;
  int tm, tn, tm4, tn4; map_g3(b, tm, tn); map_g2(b, tm4, tn4);
  bar();
  dmas<3, (NFREE3 < 16 ? NFREE3 : 16), 64>(c);
  {
    const int g = lane >> 4, q = (lane >> 2) & 3, p = lane & 3;
#pragma unroll
    for (int i = 0; i < 4; ++i) {
      c.bM[i >> 1][i & 1] = c.bW[i >> 1][i & 1] - g * 256; pin(c.bM[i >> 1][i & 1]);
      c.vM[i] = (32 * wave + 16 * (i >> 1) + 4 * g + q) * (F_ * 2) + (96 + tn) * 64 + (p ^ (2 * (g & 1))) * 16 - 1024 * i; pin(c.vM[i]);
    }
    const int j8 = ((b >> 3) * 4 + wave) * 8;
    c.vT4 = sel(lane < 16, (j8 + (lane >> 1)) * (F_ * 2) + 6144 + (b & 3) * 256 + (lane & 1) * 128, OOB);
    c.vT5 = sel(lane < 16, c.vT2, OOB);
    c.vT2 = sel((lane >> 3) == 2, (j8 + lane - 16) * 2048 + slow_col(c.w2, ((b >> 2) & 1) * 8, 8), OOB);
    pin(c.vT2); pin(c.vT4); pin(c.vT5);
    const int kh = tn4 & 1;
#pragma unroll
    for (int i = 0; i < 4; ++i) { c.sB4[i >> 1][i & 1] = (tn4 ^ 1 ^ (i & 1)) * 128 + kh * (16 >> (i >> 1)) * 32 * 2048; pins(c.sB4[i >> 1][i & 1]); }
#pragma unroll
    for (int i = 0; i < 2; ++i) { c.sA4[i] = W_HH + kh * (16 >> i) * KBS + (tm4 * 8 + 2 * wave) * 1024; pins(c.sA4[i]); }
    c.so[1] = W_HH + tn * KBS + (tm * 8 + 2 * wave) * 1024; c.so[0] = c.so[1] + tn * KBS;
    c.fl3 = flag_off(tm, wave, tn, PH_G3); c.fl4 = flag_off(tm4, wave, tn4, PH_G4);
    c.xs4 = W_XCH + (tm4 * 16 + tn4) * 32768 + wave * 8192;
    c.pollo = flag_off(tm4, wave, kh * 8 + (lane & 7), PH_G3); c.flv = 0; c.pollv = 0;
    pin(c.pollo); pin(c.flv); pins(c.so[0]); pins(c.so[1]); pins(c.fl3); pins(c.fl4); pins(c.xs4);
  }
  STAMP();
  await<PH_G2>(c, flag_off(tm, wave, lane & 15, 0));
  STAMP();
  start<3>(c, W_H2 + (tm * 8 + 2 * wave) * 1024);
  STAMP();
  stages<32, 40>(c);
  STAMP();
  stages<40, 48>(c);
  STAMP();
  stages<48, 56>(c);
  STAMP();
  stages<56, 64>(c);
  STAMP();
  stages<64, 72>(c);
  STAMP();
  stages<72, 80>(c);
  STAMP();
  stages<80, 88>(c);
  STAMP();
  stages<88, 96>(c);
  STAMP();
  stages<96, 104>(c);
  STAMP();
  stages<104, 110>(c);
  STAMP();
  stages<110, NT - 1>(c);
  tid = opaque_tid(); lane = tid & 63;
  const int g = lane >> 4;
  {
    const unsigned pb = c.lds0 + L_WV + wave * 1024 + 384 + g * 8;
#pragma unroll
    for (int i = 0; i < 4; ++i) {
      const u32x2 gv = lds_ld<u32x2>(pb + i * 32);
      c.eg[i >> 1][2 * (i & 1)] = unpk(gv[0]); c.eg[i >> 1][2 * (i & 1) + 1] = unpk(gv[1]);
#pragma unroll
      for (int e = 0; e < 4; ++e) { c.er[i >> 1][i & 1][e] = unpk(c.xr[i >> 1][i & 1][e]); pin(c.er[i >> 1][i & 1][e]); }
    }
#pragma unroll
    for (int i = 0; i < 8; ++i) pin(c.eg[i >> 2][i & 3]);
  }
  unsigned oo = ((tm4 * 128 + wave * 32 + (lane & 7)) * D_ + tn4 * 64 + (lane & 8) * 4 + (g & 1) * 16 + (g >> 1) * 8) * 2;
  pin(oo);
  GAP();
  stages<NT - 1, NT>(c);
  mfma_fence();
  STAMP();
  wait_vm<0>();
  pin(c.tch);
#pragma unroll
  for (int u = 0; u < 8; ++u) pin(c.xl[u]);
  const rsrc_t ro = mk_rsrc(c.out, S_ * D_ * 2);
#pragma unroll
  for (int rb = 0; rb < 2; ++rb) {
    u32x4 o[2];
#pragma unroll
    for (int s = 0; s < 2; ++s)
      o[s] = swz_rows(gated_resid(c.acc[U_P][s][rb][0] + __builtin_bit_cast(f32x4, c.xl[4 * s + 2 * rb]), c.acc[U_P][s][rb][1] + __builtin_bit_cast(f32x4, c.xl[4 * s + 2 * rb + 1]), c.eg[s], c.er[rb][s]));
    u32x4 w[2];
#pragma unroll
    for (int e = 0; e < 4; ++e) {
      w[0][e] = __builtin_amdgcn_update_dpp(o[0][e], o[1][e], 0x128, 0xf, 0xc, false);
      w[1][e] = __builtin_amdgcn_update_dpp(o[1][e], o[0][e], 0x128, 0xf, 0x3, false);
    }
#pragma unroll
    for (int s = 0; s < 2; ++s) __builtin_amdgcn_raw_buffer_store_b128(w[s], ro, oo + (rb * 16 + s * 8) * D_ * 2, 0, OUTA);
  }
  STAMP();
}

__global__ __launch_bounds__(256, 1) __attribute__((aligned(4096))) void block_kernel(const u16* x, const u16* wqkv, const u16* wo, const u16* w1, const u16* w3, const u16* w2,
                                                       const u16* cc, const u16* wmod, u16* out, char* ws) {
  extern __shared__ __attribute__((aligned(16))) char smem[];
  asm volatile("" ::: ACLOB);
  Ctx c;
  c.x = x; c.wqkv = wqkv; c.wo = wo; c.w1 = w1; c.w3 = w3; c.w2 = w2; c.cc = cc; c.wmod = wmod; c.out = out; c.ws = ws;
  c.lds0 = (unsigned)(uintptr_t)(LDS_PTR(char))smem;
  asm volatile("s_getpc_b64 %0\n.Lblk_begin:" : "=s"(c.pc0));
  c.wave = __builtin_amdgcn_readfirstlane(threadIdx.x >> 6);
  STAMP0();
  phase1(c);
  phase2(c);
  phase3(c);
  asm volatile(".Lblk_end:");
}

extern "C" void kernel_launch(void* const* d_in, const int* in_sizes, int n_in, void* d_out, int out_size, void* d_ws,
                              size_t ws_size, hipStream_t stream) {
  (void)hipFuncSetAttribute((const void*)block_kernel, hipFuncAttributeMaxDynamicSharedMemorySize, LDS_TOTAL);
  block_kernel<<<256, 256, LDS_TOTAL, stream>>>((const u16*)d_in[0], (const u16*)d_in[1], (const u16*)d_in[2], (const u16*)d_in[4],
                                                (const u16*)d_in[5], (const u16*)d_in[6], (const u16*)d_in[7], (const u16*)d_in[8], (u16*)d_out, (char*)d_ws);
}
```

```cpp
#include <hip/hip_runtime.h>
#include <hip/hip_bf16.h>
#include <stdint.h>
#include <utility>

typedef unsigned short u16;
typedef short bf16x8 __attribute__((ext_vector_type(8)));
typedef short bf16x4 __attribute__((ext_vector_type(4)));
typedef float f32x4 __attribute__((ext_vector_type(4)));
typedef float f32x2 __attribute__((ext_vector_type(2)));
typedef unsigned int u32x4 __attribute__((ext_vector_type(4)));
typedef unsigned int u32x2 __attribute__((ext_vector_type(2)));
typedef __amdgpu_buffer_rsrc_t rsrc_t;
#define LDS_PTR(T) __attribute__((address_space(3))) T*
#define DI __device__ __forceinline__
#define GAP() __builtin_amdgcn_sched_barrier(0)
template <int... Is> using iseq = std::integer_sequence<int, Is...>;
template <int N> using make_iseq = std::make_integer_sequence<int, N>;

constexpr int S_ = 2048, D_ = 1024, F_ = 3584;
constexpr int KBS = (S_ / 16) * 1024;

DI float bf2f(u16 v) { return __uint_as_float(((unsigned)v) << 16); }
typedef __bf16 bf16x2_t __attribute__((ext_vector_type(2)));
DI unsigned pk2(float lo, float hi) { const f32x2 v = {lo, hi}; return __builtin_bit_cast(unsigned, __builtin_convertvector(v, bf16x2_t)); }
DI float rbf(float v) { return __uint_as_float(pk2(0.f, v)); }
DI float lo2f(unsigned u) { return __uint_as_float(u << 16); }
DI float hi2f(unsigned u) { return __uint_as_float(u & 0xffff0000u); }
template <int N> DI void wait_vm() { asm volatile("s_waitcnt vmcnt(%0)" ::"n"(N) : "memory"); }
DI void drain_vm() { wait_vm<0>(); __builtin_amdgcn_s_waitcnt(0x0F70); asm volatile("" ::: "memory"); }
template <int N> DI void wait_lgkm() { __builtin_amdgcn_s_waitcnt(0xC07F | (N << 8)); asm volatile("" ::: "memory"); }
DI void bar() { asm volatile("" ::: "memory"); __builtin_amdgcn_s_barrier(); asm volatile("" ::: "memory"); }
template <int IMM> DI void m0_add(unsigned base) { asm volatile("s_add_u32 m0, %0, %1" ::"s"(base), "n"(IMM) : "scc", "memory"); }
template <int N> DI void nops() { asm volatile("s_nop %0" ::"n"(N - 1)); }
template <class T> DI void pin(T& v) { asm volatile("" : "+v"(v)); }
template <class T> DI void pins(T& v) { asm volatile("" : "+s"(v)); }
DI unsigned sel(bool c, unsigned a, unsigned b) { pin(a); pin(b); return c ? a : b; }
constexpr unsigned OOB = 0x7fffffff;
DI rsrc_t mk_rsrc(const void* p, unsigned bytes) { return __builtin_amdgcn_make_buffer_rsrc((void*)p, 0, bytes, 0x00020000); }
template <int OFF> DI void bdma(unsigned voff, const rsrc_t& rs, unsigned soff) {
  asm volatile("buffer_load_dwordx4 %0, %1, %2 offen offset:%3 lds" ::"v"(voff), "s"(rs), "s"(soff), "n"(OFF) : "memory");
}
DI void touch_line(const void* p, unsigned lds_scratch) {
  asm volatile("s_mov_b32 m0, %1\n\ts_nop 0\n\tglobal_load_lds_dword %0, off" ::"v"(p), "s"(lds_scratch) : "memory");
}
#define ACLOB "a0","a1","a2","a3","a4","a5","a6","a7","a8","a9","a10","a11","a12","a13","a14","a15","a16","a17","a18","a19","a20","a21","a22","a23","a24","a25","a26","a27","a28","a29","a30","a31","a32","a33","a34","a35","a36","a37","a38","a39","a40","a41","a42","a43","a44","a45","a46","a47","a48","a49","a50","a51","a52","a53","a54","a55","a56","a57","a58","a59","a60","a61","a62","a63","a64","a65","a66","a67","a68","a69","a70","a71","a72","a73","a74","a75","a76","a77","a78","a79","a80","a81","a82","a83","a84","a85","a86","a87","a88","a89","a90","a91","a92","a93","a94","a95","a96","a97","a98","a99","a100","a101","a102","a103","a104","a105","a106","a107","a108","a109","a110","a111","a112","a113","a114","a115","a116","a117","a118","a119","a120","a121","a122","a123","a124","a125","a126","a127","a128","a129","a130","a131","a132","a133","a134","a135","a136","a137","a138","a139","a140","a141","a142","a143","a144","a145","a146","a147","a148","a149","a150","a151","a152","a153","a154","a155","a156","a157","a158","a159","a160","a161","a162","a163","a164","a165","a166","a167","a168","a169","a170","a171","a172","a173","a174","a175","a176","a177","a178","a179","a180","a181","a182","a183","a184","a185","a186","a187","a188","a189","a190","a191","a192","a193","a194","a195","a196","a197","a198","a199","a200","a201","a202","a203","a204","a205","a206","a207","a208","a209","a210","a211","a212","a213","a214","a215","a216","a217","a218","a219","a220","a221","a222","a223","a224","a225","a226","a227","a228","a229","a230","a231","a232","a233","a234","a235","a236","a237","a238","a239","a240","a241","a242","a243","a244","a245","a246","a247","a248","a249","a250","a251","a252","a253","a254","a255"
template <int R> DI void ld_act(unsigned voff, const rsrc_t& rs, unsigned soff) {
  asm volatile("buffer_load_dwordx4 a[%c3:%c4], %0, %1, %2 offen offset:%5 sc1" ::"v"(voff), "s"(rs), "s"(soff), "i"(4 * R), "i"(4 * R + 3), "n"((R & 1) * 1024) : "memory");
}
template <int R, bool SW, bool FIRST> DI void mfa(f32x4& c, bf16x8 w) {
  if constexpr (FIRST && SW) asm volatile("v_mfma_f32_16x16x32_bf16 %0, a[%c2:%c3], %1, 0" : "=&v"(c) : "v"(w), "i"(4 * R), "i"(4 * R + 3));
  else if constexpr (FIRST) asm volatile("v_mfma_f32_16x16x32_bf16 %0, %1, a[%c2:%c3], 0" : "=&v"(c) : "v"(w), "i"(4 * R), "i"(4 * R + 3));
  else if constexpr (SW) asm volatile("v_mfma_f32_16x16x32_bf16 %0, a[%c2:%c3], %1, %0" : "+v"(c) : "v"(w), "i"(4 * R), "i"(4 * R + 3));
  else asm volatile("v_mfma_f32_16x16x32_bf16 %0, %1, a[%c2:%c3], %0" : "+v"(c) : "v"(w), "i"(4 * R), "i"(4 * R + 3));
}
DI void mfv(f32x4& d, const u32x4& a, const u32x4& b) { asm volatile("v_mfma_f32_16x16x32_bf16 %0, %1, %2, 0" : "=&v"(d) : "v"(a), "v"(b)); }
DI void mfma_fence() { GAP(); asm volatile("s_nop 7\n\ts_nop 1" ::: "memory"); GAP(); }
DI bf16x4 ldsT(unsigned addr) { return __builtin_amdgcn_ds_read_tr16_b64_v4i16((LDS_PTR(bf16x4))(uintptr_t)addr); }
DI bf16x8 cat(bf16x4 lo, bf16x4 hi) { return __builtin_shufflevector(lo, hi, 0, 1, 2, 3, 4, 5, 6, 7); }
DI u32x4 pack8(const f32x4& a, const f32x4& b) { return (u32x4){pk2(a[0], a[1]), pk2(a[2], a[3]), pk2(b[0], b[1]), pk2(b[2], b[3])}; }
DI void v_mul(float& d, float a, float b) { asm volatile("v_mul_f32 %0, %1, %2" : "=v"(d) : "v"(a), "v"(b)); }
DI void v_mulc(float& d, float a) { asm volatile("v_mul_f32 %0, 0xbfb8aa3b, %1" : "=v"(d) : "v"(a)); }
DI void v_exp(float& d, float a) { asm volatile("v_exp_f32 %0, %1" : "=v"(d) : "v"(a)); }
DI void v_fmac(float& d) { asm volatile("v_fma_f32 %0, %0, %1, %1" : "+v"(d) : "s"(-1.4426950408889634f)); }
DI void v_rcp(float& d) { asm volatile("v_rcp_f32 %0, %0" : "+v"(d)); }
DI unsigned v_cvt(float a, float b) { unsigned d; asm volatile("v_cvt_pk_bf16_f32 %0, %1, %2" : "=v"(d) : "v"(a), "v"(b)); return d; }

DI float fmax1(float a, float b) { return __builtin_amdgcn_fmed3f(a, b, __builtin_inff()); }
DI float xadd16(float v) { auto r = __builtin_amdgcn_permlane16_swap(__float_as_uint(v), __float_as_uint(v), false, false); return __uint_as_float(r[0]) + __uint_as_float(r[1]); }
DI float xadd32(float v) { auto r = __builtin_amdgcn_permlane32_swap(__float_as_uint(v), __float_as_uint(v), false, false); return __uint_as_float(r[0]) + __uint_as_float(r[1]); }
DI float add2(float u, float v) {
  auto r = __builtin_amdgcn_permlane16_swap(__float_as_uint(u), __float_as_uint(v), false, false);
  return xadd32(__uint_as_float(r[0]) + __uint_as_float(r[1]));
}
DI f32x2 spread2(float t) {
  auto r = __builtin_amdgcn_permlane16_swap(__float_as_uint(t), __float_as_uint(t), false, false);
  return (f32x2){__uint_as_float(r[0]), __uint_as_float(r[1])};
}
template <int CTRL> DI float dpp_add(float v) { return v + __uint_as_float(__builtin_amdgcn_update_dpp(0, __float_as_uint(v), CTRL, 0xf, 0xf, true)); }
DI f32x2 red8(const f32x2 (&a)[4]) {
  f32x2 o;
#pragma unroll
  for (int e = 0; e < 2; ++e) {
    auto r0 = __builtin_amdgcn_permlane16_swap(__float_as_uint(a[0][e]), __float_as_uint(a[1][e]), false, false);
    auto r1 = __builtin_amdgcn_permlane16_swap(__float_as_uint(a[2][e]), __float_as_uint(a[3][e]), false, false);
    const float u = __uint_as_float(r0[0]) + __uint_as_float(r0[1]), v = __uint_as_float(r1[0]) + __uint_as_float(r1[1]);
    auto r2 = __builtin_amdgcn_permlane32_swap(__float_as_uint(u), __float_as_uint(v), false, false);
    o[e] = dpp_add<0x128>(__uint_as_float(r2[0]) + __uint_as_float(r2[1]));
  }
  return o;
}
DI float row16_sum(float v) {
  v = dpp_add<0xB1>(v);
  v = dpp_add<0x4E>(v);
  v = dpp_add<0x141>(v);
  v = dpp_add<0x140>(v);
  return v;
}
DI f32x2 unpk(unsigned u) { return (f32x2){lo2f(u), hi2f(u)}; }
DI f32x2 rbf2(f32x2 v) { return (f32x2){rbf(v[0]), rbf(v[1])}; }
DI float ln_rstd(float var) { return __builtin_amdgcn_rcpf(rbf(__builtin_amdgcn_sqrtf(rbf(rbf(var) + 1e-5f)))); }
DI float silu_bf(u16 c) { const float x = bf2f(c); return rbf(x * __builtin_amdgcn_rcpf(1.f + __builtin_amdgcn_exp2f(x * -1.4426950408889634f))); }
DI unsigned umax4(const u32x4& v) { return max(max(v[0], v[1]), max(v[2], v[3])); }
DI unsigned umax16(const u32x4 (&v)[4]) { return max(max(umax4(v[0]), umax4(v[1])), max(umax4(v[2]), umax4(v[3]))); }
template <class T> DI T lds_ld(unsigned a) { return *(const LDS_PTR(T))(uintptr_t)a; }
template <class T> DI void lds_st(unsigned a, const T& v) { *(LDS_PTR(T))(uintptr_t)a = v; }
DI u32x4 swz_rows(const u32x4& o) {
  auto r0 = __builtin_amdgcn_permlane16_swap(o[0], o[2], false, false);
  auto r1 = __builtin_amdgcn_permlane16_swap(o[1], o[3], false, false);
  return (u32x4){r0[0], r1[0], r0[1], r1[1]};
}

constexpr unsigned W_H1 = 1u << 16, W_O = W_H1 + (4u << 20), W_X1 = W_O + (4u << 20), W_H2 = W_X1 + (4u << 20), W_HH = W_H2 + (4u << 20);
constexpr unsigned W_PART = 32u << 20, W_LATE = W_PART + (128u << 10), W_LN1 = W_LATE + (128u << 10), W_FLAGS = W_LN1 + (32u << 10), W_STATS = W_FLAGS + (128u << 10), W_HALO = W_STATS + (256u << 10);
constexpr unsigned W_XCH = 34u << 20, W_END = 42u << 20;
enum { PH_LN1 = 0, PH_G1 = 2, PH_G2, PH_G3, PH_G4 };
constexpr unsigned POISON = 0xffffffffu;
constexpr int AUX_SC1 = 16;
constexpr unsigned SPIN_CAP = 1u << 17;
constexpr int SLOT = 16384, L_KV = 8 * SLOT, L_WV = L_KV + 20480, L_RED = L_WV + 4096, L_TCH = L_RED + 1024, LDS_TOTAL = 163840;

#define STAMP()
#define STAMP0()
#define FST()
DI int opaque_tid() { int t = threadIdx.x; asm volatile("" : "+v"(t)); return t; }
DI int opaque_bid() { int b = blockIdx.x; asm volatile("" : "+s"(b)); return b; }

#define MAPV 1
DI int panel(int b) { return ((b >> 2) & 1) * 8 + (b >> 5); }
DI void map_g1(int b, int& tm, int& tn) { tm = panel(b); tn = (b & 3) * 4 + (((b >> 3) + 3) & 3); }
DI void map_g2(int b, int& tm, int& tn) { tm = panel(b); tn = (b & 2) * 4 + ((b >> 3) & 3) * 2 + (b & 1); }
DI void map_g3(int b, int& tm, int& tn) { tm = panel(b); tn = (b & 3) * 4 + ((b >> 3) & 3); }
#define BAND1 ((b & 3) * 4)
#define BAND2 ((b & 2) * 4)

struct Ctx {
  const u16 *x, *wqkv, *wo, *w1, *w3, *w2, *cc, *wmod; u16* out; char* ws;
  rsrc_t rs, rC, rQ, rO, r1, r3, r2, rM;
  f32x4 acc[13][2][2][2];
  bf16x4 wl[8][2], wh[8][2];
  float t[8]; u32x4 o;
  unsigned bW[2][2], bM[2][2], vA, vK[4], v1[4], v2[4], v3[4], vM[4];
  unsigned lds0, sBB[2], sAA[2], sA0, mB, so[2], sB4[2][2], sA4[2], fl3, fl4, xs4;
  int wave; unsigned long long pc0;
  u32x4 kf[2][2], vf[2][2], hl[4]; unsigned kvw, hso;
  u32x4 xr[2][2], lw[8]; float ls[8]; f32x2 ga[4];
  u32x4 mp[2][4]; u32x2 mv[12]; unsigned mo, tag[2]; f32x2 eg[2][4], er[2][2][4], sc1[8], sh[8];
  unsigned ck[2], cw[2][2], zo, pollv, pollo, flv, tch, vT1, vT2, vT3, vT4, vT5, vC, vG0; u32x4 xl[8];
};
DI void st16(const Ctx& c, unsigned off, const u32x4& v) { __builtin_amdgcn_raw_buffer_store_b128(v, c.rs, off, 0, AUX_SC1); }
DI void ld16x4(const Ctx& c, u32x4 (&v)[4], unsigned off, unsigned step) {
  asm volatile("buffer_load_dwordx4 %0, %4, %5, 0 offen sc1\n\tbuffer_load_dwordx4 %1, %4, %5, %6 offen sc1\n\tbuffer_load_dwordx4 %2, %4, %5, %7 offen sc1\n\tbuffer_load_dwordx4 %3, %4, %5, %8 offen sc1\n\ts_waitcnt vmcnt(0)"
               : "=&v"(v[0]), "=&v"(v[1]), "=&v"(v[2]), "=&v"(v[3]) : "v"(off), "s"(c.rs), "s"(step), "s"(2 * step), "s"(3 * step) : "memory");
}
DI void retry(const Ctx& c, u32x4 (&v)[4], unsigned off, unsigned step) {
  if (__builtin_expect(__any(umax16(v) == POISON), 0)) {
#pragma unroll 1
    for (unsigned spins = 0; spins < SPIN_CAP; ++spins) { ld16x4(c, v, off, step); if (!__any(umax16(v) == POISON)) break; __builtin_amdgcn_s_sleep(1); }
  }
}
DI bool tagged(const Ctx& c, const u32x4 (&v)[4]) {
  const unsigned d = ((v[0][2] ^ c.tag[0]) | (v[1][2] ^ c.tag[0]) | (v[2][2] ^ c.tag[0]) | (v[3][2] ^ c.tag[0])) | ((v[0][3] ^ c.tag[1]) | (v[1][3] ^ c.tag[1]) | (v[2][3] ^ c.tag[1]) | (v[3][3] ^ c.tag[1]));
  return __all(d == 0);
}
DI unsigned flag_off(int tm, int wave, int col, int ph) { return W_FLAGS + ((tm * 4 + wave) * 16 + col) * 128 + ph * 4; }
DI void set_flag(const Ctx& c, unsigned off, unsigned val) {
  asm volatile("s_mov_b64 exec, 1\n\tbuffer_store_dword %0, off, %1, %2 sc1\n\ts_mov_b64 exec, -1" ::"v"(val), "s"(c.rs), "s"(off) : "memory");
}
DI void spin(const Ctx& c, unsigned voff, unsigned need) {
#pragma unroll 1
  for (unsigned spins = 0; spins < SPIN_CAP; ++spins) {
    if (__all(__builtin_amdgcn_raw_buffer_load_b32(c.rs, voff, 0, AUX_SC1) >= need)) break;
    __builtin_amdgcn_s_sleep(1);
    asm volatile("" ::: "memory");
  }
  asm volatile("" ::: "memory");
}
template <int PH> DI void await(const Ctx& c, unsigned line) {
#pragma unroll 1
  for (unsigned spins = 0; spins < SPIN_CAP; ++spins) {
    const u32x4 v = __builtin_amdgcn_raw_buffer_load_b128(c.rs, line, 0, AUX_SC1);
    if (__all(v[0] == c.tag[0] && v[1] == c.tag[1] && (PH == PH_LN1 || v[PH] != 0))) break;
    __builtin_amdgcn_s_sleep(1);
    asm volatile("" ::: "memory");
  }
  asm volatile("" ::: "memory");
}
#define L4 7
#define NP1 8
#define NP3 7
#define PRE3 4
#define BS 4
#define QW 16
#define HP4 11
#define LG 5
#define AW 1000
#define DENSE 20
#define VQ 2
#define GNT " nt"
#define OUTM 2
#define OUTA 16
#define PAIR 16
#define WB2 0
#define UP0 73
#define UP1 93
#define NOFLAG 8
#define DLY3 0
#define CAP 7
#define NFD 12
#define NF1 0
#define NF2 4
#define XD1 12
#define XD2 8
#define XD3 10
namespace en {
constexpr int RD = 4, NT = 116, NH = 8 * NT, T3 = 32, T4 = 88, NB4 = 14 - L4;
constexpr int seg_t0(int s) { return s == 1 ? 0 : s == 2 ? 24 : s == 3 ? T3 : NT; }
constexpr int seg_of(int H) { return H < 192 ? 1 : H < 256 ? 2 : 3; }
constexpr int seg_end(int H) { return 8 * seg_t0(seg_of(H) + 1); }
constexpr int seg_np(int s) { return s == 1 ? NP1 : s == 2 ? 8 : NP3; }
constexpr int seg_pre(int s) { return s == 3 ? PRE3 : 8; }
enum { U_K, U_V, U_Q, U_O, U_G0, U_U0, U_G1, U_U1, U_M, U_G2, U_U2, U_S, U_P };
constexpr int unit(int T) {
  if (T >= T4) { const int s = T - T4; return U_S + (s < 2 * NB4 ? s & 1 : s >= 2 * NB4 + L4); }
  return T < 32 ? T >> 3 : T < 64 ? U_G0 + ((T - 32) >> 4) * 2 + (T & 1) : T < 72 ? U_M : T - 72 < LG || (T - 72 < 16 - LG && ((T - 72 - LG) & 1)) ? U_G2 : U_U2;
}
constexpr int grp(int T) {
  if (T >= T4) { const int s = T - T4; return s < 2 * NB4 ? s / 2 : s < 2 * NB4 + L4 ? s - NB4 : s - NB4 - L4; }
  return T < 32 ? T & 7 : T < 64 ? (T >> 1) & 7 : T < 72 ? T & 7 : T - 72 < LG ? T - 72 : T - 72 >= 16 - LG ? T - 80 : (T - 72 - LG) / 2 + (unit(T) == U_G2 ? LG : 0);
}
constexpr int u2stage(int j) { int T = 72; while (unit(T) != U_U2 || grp(T) != j) ++T; return T; }
constexpr int g4first(int j) { return T4 + (j < NB4 ? 2 * j : j + NB4); }
constexpr int g4last(int j) { return T4 + (j < NB4 ? 2 * j + 1 : j + NB4 + L4); }
constexpr int g4kb(int j) { return j < 8 ? 32 * (j / 4) + 4 * (j % 4) : j < 10 ? 96 + 4 * (j - 8) : 64 + 4 * (j - 10); }
constexpr bool g4mix(int j) { return j == 8 || j == 9; }
constexpr bool barst(int T) { return T < NT - 1 && (BS == 1 || (T & 1) || (BS == 2 && (T - seg_t0(seg_of(8 * T)) < 16 || T >= 72)) || (BS == 4 && ((T >= 8 && T < 24) || (T >= 46 && T < 76) || (T > 103 && T < 110))) || T == 6 || T == 8); }
constexpr bool isbar(int H) { return (H & 7) == 8 - RD && barst(H >> 3); }
constexpr int cert(int T) { int b = T - 1; while (!barst(b)) --b; return 8 * b + 8 - RD; }
enum { K_A = 1, K_D, K_A4, K_P, K_XS, K_XL, K_M, K_T, K_C, K_G, K_Z };
enum { A_CHK1 = 1, A_CHK2, A_CHK3, A_CHK4, A_FLAG, A_FLAG4, A_CHKX, A_CK = 16 };
enum { P_K, P_GA, P_V, P_GB, P_GC, P_PREV, P_GD, P_GR, P_GO, P_UA, P_UB, PR_HH };
constexpr int NPR = PR_HH + 28, HH_ST = 13;
constexpr bool pr_up(int i) { return i - PR_HH < 20 ? (i - PR_HH) & 1 : i - PR_HH >= 24; }
constexpr int pr_blk(int i) { return i - PR_HH < 20 ? (i - PR_HH) / 2 : 10 + (i - PR_HH) % 4; }
constexpr int pr_avail(int i) {
  const int a[PR_HH] = {64, 106, 128, 0, 146, 165, 0, 0, 187, 192, 0}, n = i - PR_HH;
  return n < 0 ? a[i] : 8 * T3 + (n < 8 ? 128 : n < 16 ? 256 : n < 20 ? 320 : n < 24 ? 448 - 8 * LG : 448) - (n < 16 && !pr_up(i) ? 8 : 0);
}
constexpr int pr_len(int i) { return i == P_K || i == P_V ? 24 : i == P_PREV || i == P_GR || i == P_GO ? 1 : i == P_UA ? 64 : i == P_UB ? 48 : i < PR_HH ? 32 : pr_up(i) ? HH_ST + 1 : 40; }
constexpr bool noflag(int s) { return (NOFLAG >> s) & 1; }
constexpr int E_CK = 8192;
enum { S_M0 = 1, S_SB, S_SA, S_SA4 };
struct Plan { short sc[4 * NH], nr[4 * NH], op[NH + 1], e[4 * NH], act[NH + 1], vw[NH + 1], d0[5], d1[5], slack, halo; };
constexpr Plan mk() {
  Plan p{};
  int atA[64] = {}, atD[4 * NT] = {}, atA4[112] = {}, req[NH + 1] = {}, nvm[NH + 1] = {};
  bool quiet[NH + 64] = {};
  for (int H = 0; H <= NH; ++H) req[H] = -1000;
  auto need = [&](int H, int x) { if (x > req[H]) req[H] = x; };
  auto window = [&](int H, int o0, int o1 = 0, int o2 = 0, int o3 = 0, int o4 = 0, int o5 = 0, int o6 = 0) {
    const int o[7] = {o0, o1, o2, o3, o4, o5, o6};
    for (int i = 0; i < 7 && o[i]; ++i) p.op[H++] = o[i];
    for (int i = 0; i < QW && seg_of(H + i) == seg_of(H - 1); ++i) quiet[H + i] = true;
  };
  auto poll = [&](int H, int a) { p.op[H - 16] = K_P * 1024; p.act[H] = a; need(H, H - 16); };
  constexpr int T = K_T * 1024, G = K_G * 1024, C = K_C * 1024, H3 = 8 * T3, H4 = 8 * T4;
  window(84, G, G + 1, G + 2, G + 3); window(120, G + 4, G + 5, G + 6, G + 7); window(153, C, T, T + 1, T + 2); window(172, C + 1, T + 3, T + 4);
  need(108, 87); need(148, 123);
  window(H3 + 130, C, T + 5, T + 6); window(H3 + 168, T + 7, T + 8); window(H3 + 270, C, T + 9, T + 10); window(H3 + 310, T + 11, T + 12);
  p.op[58] = T + 23; p.op[210] = T + 17; p.op[211] = T + 18; p.op[H3 + 67] = T + 19; p.op[H3 + 75] = T + 20; p.op[H3 + 235] = T + 21; p.op[H3 + 243] = T + 22;
  p.op[H3 + 366] = T + 13; p.op[H3 + 430] = T + 14; p.op[H3 + 496] = T + 16; p.op[H3 + 460] = C; p.op[H3 + 506] = T + 15; p.op[H3 + 546] = T + 16; p.op[H3 + 548] = T + 15;
  if (noflag(3)) for (int i = 0; i < 4; ++i) p.op[98 + i] = K_Z * 1024 + i;
  const int HA0 = H4 - 8 * LG;
  const int chk[4] = {H3 + 354, H3 + 418, H4 + 18, H4 + 8 * HP4 + 16}, hx0 = 8 * g4first(13) + 10;
  for (int i = 0; i < 4; ++i) poll(chk[i], A_CHK1 + i);
  int nd = 0, n4 = 0, nxs = 0, nxl = 0, hchk = 100000;
  p.slack = 1000;
  for (int s = 1; s <= 3; ++s) {
    const int T0 = seg_t0(s), T1 = seg_t0(s + 1), dbl = s == 3;
    int na = 2 * seg_pre(s), lim = T0 + 7;
    p.d0[s] = nd; if (nd < 4 * (T0 + seg_np(s))) nd = 4 * (T0 + seg_np(s)); p.d1[s] = nd;
    for (int n = 0; n < na; ++n) atA[n] = 8 * T0 - na + n;
    auto useA = [&](int n) { return 8 * T0 + (n >> 3) * (8 << dbl) + (n & 6) - (!noflag(s) ? 0 : n < 8 ? 2 : 8); };
    for (int H = 8 * T0; H < 8 * T1; ++H) {
      if (isbar(H)) lim = (H >> 3) + 7;
      if (p.op[H] || quiet[H]) continue;
      if (H >= hx0 && nxs < 8) {
        p.op[H] = K_XS * 1024 + nxs++;
        if (nxs == 8) {
          const int hf = (H + XD1 + 1) & ~1; hchk = (hf + XD2 + XD3 + 1) & ~1;
          p.act[hf] = A_FLAG4; need(hf, H); p.op[hf + XD2] = K_P * 1024 + 1; p.act[hchk] = A_CHKX; need(hchk, hf + XD2);
        }
        continue;
      }
      if (H >= hchk && nxl < 8) { p.op[H] = K_XL * 1024 + nxl++; continue; }
      if (CAP < 8 && s == 3 && (H & 7) == 3 && H - 8 * T0 > 8 && H < HA0 - 40) continue;
      const int Td = nd >> 2, j = n4 >> 3;
      const bool dok = Td < NT && Td <= lim && Td < T1 + 8;
      const int dd = !dok ? 100000 : Td < T1 ? cert(Td) : 8 * T1 + 2 * (Td - T1);
      int da = 100000;
      if (na < 64) da = useA(na);
      else if (n4 < 112 && H >= 8 * (j < 8 ? u2stage(j) : g4last(j - 8)) + (n4 & 6) + 2 && H >= chk[j < 8 ? j >> 2 : j < 10 ? 2 : 3] && (H < HA0 || H >= cert(g4first(j)) - AW)) da = cert(g4first(j));
      if (da <= dd && da < 100000) { if (na < 64) { atA[na] = H; p.op[H] = K_A * 1024 + na++; } else { atA4[n4] = H; p.op[H] = K_A4 * 1024 + n4++; } }
      else if (dok && (H >= 8 * (Td - (barst(Td - 7) && barst(Td - 8) ? 7 : 6)) + 4 + 2 * (nd & 3) || da < 100000)) { atD[nd] = H; p.op[H] = K_D * 1024 + nd++; }
    }
    if (nd < 4 * T1 || na < 64) p.slack = -5;
    for (int n = noflag(s) ? 3 : 1; n < 64; n += 2) need(useA(n), atA[n]);
    if (noflag(s)) { for (int g = 1; g < 8; ++g) p.act[useA(8 * g) + 8] = A_CK + g; for (int k = 1; k < 4; ++k) p.act[8 * T0 + 2 * k] = A_CK; }
    for (int t = p.d1[s] >> 2; t < T1; ++t) need(cert(t), atD[4 * t + 3]);
  }
  for (int j = 0; j < 14; ++j) need(cert(g4first(j)), atA4[8 * j + 7]);
  if (n4 < 112 || nxl < 8 || nd < 4 * NT) p.slack = -1;
  int used[4 * NH] = {};
  for (int q = 0; q < 4 * NH; ++q) { p.nr[q] = (q >> 2) + RD < seg_end(q >> 2); used[q] = p.nr[q]; }
  for (int H = 0; H < NH; ++H) {
    if (p.op[H]) { nvm[H]++; used[4 * H] = 3; if ((H < 8 * UP0 || H >= 8 * UP1) && seg_of(H) != 2) { p.nr[4 * H + 1] += p.nr[4 * H]; p.nr[4 * H] = 0; used[4 * H + 1] = p.nr[4 * H + 1]; } }
    if ((H & 1) && H + 1 < seg_end(H)) { used[4 * H + 3]++; if (WB2 && isbar(H + 1)) used[4 * H + 2]++; }
  }
  for (int H = 0; H < NH; ++H) {
    const int k = p.op[H] / 1024, i = p.op[H] % 1024, h0 = 8 * seg_t0(seg_of(H));
    auto put = [&](int code, int back) {
      for (int q = 4 * H - 1; q >= 4 * (H - back) + 1 && q >= 4 * h0; --q) if (used[q] < 2 && !p.sc[q]) { p.sc[q] = code; used[q]++; return; }
      for (int q = 4 * H - 1; q >= 4 * (H - back) + 1 && q >= 4 * h0; --q) if (!p.sc[q]) { p.sc[q] = code; used[q]++; return; }
      p.slack = -8;
    };
    if (k == K_D && !(i & 3)) { put(S_M0 * 1024 + (i >> 2), 1); put(S_SB * 1024 + (i >> 2), 3); }
    if (k == K_A && !(i & 7) && H > h0) put(S_SA * 1024 + (i >> 3), 3);
    if (k == K_A4 && !(i & 7)) put(S_SA4 * 1024 + (i >> 3), 3);
  }
  int pr = 0, ix = 0;
  for (int q = 0; q < 4 * NH; ++q) p.e[q] = -1;
  for (int s = 1; s <= 3; ++s) if (noflag(s)) for (int ks = 1; ks < 32; ++ks) {
    const int q0 = 4 * (8 * seg_t0(s) + (ks >> 2) * (s == 3 ? 16 : 8) + 2 * (ks & 3) - (ks < 4 ? 2 : 8));
    int fr = 0, st = 0;
    for (int q = q0; q < q0 + 7; ++q) fr += used[q] < 2;
    for (int q = q0; q < q0 + 7 && st < 3; ++q) if (used[q] < 2 || (used[q] == 2 && fr < 3 - st)) { fr -= used[q] < 2; p.e[q] = E_CK + 4 * ks + st++; used[q] = 3; }
    if (st < 3) p.slack = -9;
  }
  for (int q = 0; q < 4 * NH; ++q) {
    const int H = q >> 2;
    if (pr == NPR || q < 4 * pr_avail(pr) + 8) continue;
    const int n = pr - PR_HH;
    const bool st = n >= 0 && pr_up(pr) && ix == HH_ST, big = st || (n >= 0 && !pr_up(pr) && ((ix >> 3) & 1)), dense = n >= DENSE;
    if (st && (q & 7) == 7) continue;
    if (big && (n < PAIR || st) && p.nr[q]) {
      if (used[q] == 1 && (q & 3) < 3 && used[q + 1] == 1 && p.nr[q + 1] == 1) { p.nr[q] = 0; p.nr[q + 1] = 2; used[q + 1] = 2; }
      else if (used[q] >= 2) continue;
    } else if (used[q] >= 2 && !(dense && used[q] == 2 && p.nr[q] == 1 && !((q & 3) == 3 && isbar(H + 1)))) continue;
    p.e[q] = pr * 64 + ix;
    if (pr == P_PREV) p.halo = H;
    if ((pr == P_GR && H > 182) || (pr == P_GO && H > 191) || (pr == P_V && H > 149) || (pr == P_UB && H > 255)) p.slack = -6;
    if (pr >= PR_HH && pr_up(pr) && ix == HH_ST) {
      nvm[H]++;
      if (n == 7 || n == 15 || n == 19 || n == 27) {
        int hb = H + 22; while (!isbar(hb) || p.act[hb]) ++hb;
        p.act[hb] = A_FLAG; need(hb, H);
        if (hb + 16 > chk[n == 7 ? 0 : n == 15 ? 1 : n == 19 ? 2 : 3] - 16) p.slack = -3;
      }
    }
    if (++ix == pr_len(pr)) { ix = 0; ++pr; }
  }
  if (pr < NPR) p.slack = -2;
  for (int H = 0; H < NH; ++H) {
    int n = -1;
    if (req[H] != -1000) {
      const int h0 = 8 * seg_t0(seg_of(H));
      n = req[H] < h0 ? h0 - 1 - req[H] : 0;
      for (int h = req[H] < h0 ? h0 : req[H] + 1; h < H; ++h) n += nvm[h];
      if (req[H] >= h0 && H - req[H] < p.slack) p.slack = H - req[H];
    }
    p.vw[H] = n > 63 ? 63 : n;
  }
  { int n = 0; for (int h = p.halo + 1; h < 192; ++h) n += nvm[h]; p.halo = n; }
  return p;
}
constexpr Plan PL = mk();
static_assert(PL.slack >= 7, "every load is issued well before it is needed");

#define PGTAB 54, 268, 470, 661, 888, 1136, 1335, 1534, 1731, 1928, 2127, 2321, 2509, 2695, 2868, 3060, 3277, 3488
constexpr int PGS[] = {PGTAB}, NPG = sizeof(PGS) / sizeof(int);
constexpr int pg_of(int q) { for (int i = 0; i < NPG; ++i) if (PGS[i] == q) return i; return -1; }
constexpr int pg_grp(int i) { return i < NPG && PGS[i] >= 0 ? PGS[i] >= 32 * T3 : -1; }
constexpr int pg_first(int g) { int i = 0; while (i < NPG && pg_grp(i) != g) ++i; return i; }
constexpr int pg_lab(int i, int g) { return pg_grp(i) == g ? i : 1000 + g; }
template <int I> DI void pg_mark() { asm volatile(".Lpg%c0:\n\ts_cbranch_execz .Lpg%c1" ::"i"(I), "i"(pg_lab(I + 4, pg_grp(I)))); }
template <int G> DI void pg_hop(int wave) {
  constexpr int f = pg_first(G);
  asm volatile("s_mov_b64 exec, 0\n\ts_cmp_eq_u32 %0, 0\n\ts_cbranch_scc1 .Lpg%c1\n\ts_cmp_eq_u32 %0, 1\n\ts_cbranch_scc1 .Lpg%c2\n\ts_cmp_eq_u32 %0, 2\n\ts_cbranch_scc1 .Lpg%c3\n\ts_branch .Lpg%c4\n"
               ".Lpg%c5:\n\ts_mov_b64 exec, -1" ::"s"(wave), "i"(pg_lab(f, G)), "i"(pg_lab(f + 1, G)), "i"(pg_lab(f + 2, G)), "i"(pg_lab(f + 3, G)), "i"(1000 + G) : "scc", "memory");
}
template <int H, int R> DI void rd(Ctx& c) {
  constexpr bool mx = unit(H >> 3) == U_M;
  constexpr int cb = R >> 1, hi = R & 1, off = ((H >> 3) & 7) * SLOT + ((H >> 1) & 3) * 4096 + (H & 1) * (mx ? 1024 : 256) + hi * 2048;
  const unsigned ad = (mx ? c.bM[cb][off >> 16] : c.bW[cb][off >> 16]) + (off & 65535);
  if constexpr (hi) c.wh[H & 7][cb] = ldsT(ad); else c.wl[H & 7][cb] = ldsT(ad);
}
template <int T> DI void dma_off(Ctx& c) {
  constexpr int u = unit(T), j = grp(T);
  unsigned& sB = c.sBB[T & 1];
  if constexpr (u <= U_Q) sB = j * 128 * 6144 + (u + 1) % 3 * 2048;
  else if constexpr (u == U_O) sB = c.sB4[0][0] + j * 128 * 2048;
  else if constexpr (u < U_S) sB = j * 128 * F_ * 2 + (u == U_M ? 0 : u >= U_G2 ? 4096 : u >= U_G1 ? 2048 : 0);
  else sB = c.sB4[g4mix(j)][u - U_S] + g4kb(j) * 32 * 2048;
  pins(sB);
}
template <int D> DI void dma(Ctx& c) {
  constexpr int u = unit(D >> 2), i = D & 3;
  if constexpr (u <= U_Q) bdma<i * 1024>(c.v1[i], c.rQ, c.sBB[(D >> 2) & 1]);
  else if constexpr (u == U_O) bdma<i * 1024>(c.v2[i], c.rO, c.sBB[(D >> 2) & 1]);
  else if constexpr (u == U_M) bdma<i * 1024>(c.vM[i], (i & 1) ? c.r3 : c.r1, c.sBB[(D >> 2) & 1]);
  else if constexpr (u < U_S) bdma<i * 1024>(c.v3[i], u == U_U0 || u == U_U1 || u == U_U2 ? c.r3 : c.r1, c.sBB[(D >> 2) & 1]);
  else bdma<i * 1024>(c.v2[i], c.r2, c.sBB[(D >> 2) & 1]);
}
template <int D> DI void dma1(Ctx& c) {
  if constexpr (!(D & 3)) { dma_off<(D >> 2)>(c); m0_add<((D >> 2) & 7) * SLOT>(c.mB); nops<1>(); }
  dma<D>(c);
}
template <int B, int... Ds> DI void dmas_(Ctx& c, iseq<Ds...>) { (dma1<B + Ds>(c), ...); }
template <int S, int A, int B> DI void dmas(Ctx& c) {
  constexpr int n = PL.d1[S] - PL.d0[S], a = A < n ? A : n, b = B < n ? B : n;
  GAP(); dmas_<PL.d0[S] + a>(c, make_iseq<b - a>{}); GAP();
}
template <int S, int B, int E, int... Is> DI void dma_at_(Ctx& c, int n, iseq<Is...>) { ((n == Is && B + Is < E ? dmas<S, B + Is, B + Is + 1>(c) : void()), ...); }
template <int S, int B, int E> DI void dma_at(Ctx& c, int n) { dma_at_<S, B, E>(c, n, make_iseq<16>{}); }
constexpr int NFREE2 = 4 * 31 - PL.d0[2];
constexpr int NFREE3 = 4 * 39 - PL.d0[3];

template <int N, bool UP, int I> DI void e_hh(Ctx& c) {
  constexpr bool mx = N == 8 || N == 9;
  constexpr int n = N < 8 ? N : mx ? N - 8 : N - 2, S = (n >> 1) & 1, B = n & 1, ug = mx ? U_M : N < 8 ? U_G0 + (N >> 2) * 2 : U_G2, e = I & 7, ph = I >> 3;
  f32x4& g = c.acc[ug][S][B][e >> 2];
  float r;
  if constexpr (UP) {
    if constexpr (I < 8) v_mul(c.t[e], g[e & 3], c.acc[mx ? U_M : ug + 1][mx ? 1 : S][B][e >> 2][e & 3]);
    else if constexpr (I < 12) c.o[e] = v_cvt(c.t[2 * e], c.t[2 * e + 1]);
    else if constexpr (I == HH_ST) asm volatile("buffer_store_dwordx4 %0, %1, %2, %3 offen offset:%4 sc1" ::"v"(c.o), "v"(c.vA), "s"(c.rs), "s"(c.so[mx] + (mx ? 96 : (n >> 2) * 32 + S) * KBS), "n"(B * 1024) : "memory");
  } else if constexpr (ph == 0) { v_mulc(r, g[e & 3]); g[e & 3] = r; }
  else if constexpr (ph == 1) v_exp(c.t[e], g[e & 3]);
  else if constexpr (ph == 2) v_fmac(c.t[e]);
  else if constexpr (ph == 3) v_rcp(c.t[e]);
  else { v_mul(r, g[e & 3], c.t[e]); g[e & 3] = r; }
}
template <int U, int F> DI void e_kv(Ctx& c) {
  u32x4 (&d)[2][2] = U ? c.vf : c.kf;
  if constexpr (F < 16) { const f32x4& a = c.acc[U][F >> 3][(F >> 2) & 1][(F >> 1) & 1]; d[F >> 3][(F >> 2) & 1][F & 3] = v_cvt(a[2 * (F & 1)], a[2 * (F & 1) + 1]); }
  else if constexpr (F == 18 || F == 22) lds_st(c.kvw + 4096 + U * 2048 + (F == 22) * 1024, d[F == 22][1]);
  else if constexpr (F == 17) {
    if (c.wave == 3) {
#pragma unroll
      for (int h = 0; h < 2; ++h) {
        const u32x4 v = d[h][1];
        st16(c, c.vA + c.hso + 65536 + U * 2048 + h * 1024, (u32x4){min(v[0], POISON - 1), min(v[1], POISON - 1), min(v[2], POISON - 1), min(v[3], POISON - 1)});
      }
    }
  }
}
DI void e_prev(Ctx& c) {
  asm volatile("s_cmp_eq_u32 %4, 0\n\ts_cbranch_scc1 1f\n\tds_read_b128 %0, %5\n\tds_read_b128 %1, %5 offset:1024\n\tds_read_b128 %2, %5 offset:2048\n\tds_read_b128 %3, %5 offset:3072\n\ts_branch 2f\n"
               "1:\n\tbuffer_load_dwordx4 %0, %6, %7, %8 offen sc1\n\tbuffer_load_dwordx4 %1, %6, %7, %8 offen offset:1024 sc1\n\tbuffer_load_dwordx4 %2, %6, %7, %8 offen offset:2048 sc1\n\tbuffer_load_dwordx4 %3, %6, %7, %8 offen offset:3072 sc1\n2:"
               : "=&v"(c.hl[0]), "=&v"(c.hl[1]), "=&v"(c.hl[2]), "=&v"(c.hl[3]) : "s"(c.wave), "v"(c.kvw), "v"(c.vA), "s"(c.rs), "s"(c.hso < W_HALO ? W_END : c.hso) : "scc", "memory");
}
DI void gemv_out(const Ctx& c, unsigned off, int lane) {
  const unsigned rb = c.lds0 + L_RED + (lane & 31) * 8;
  const f32x2 sum = (lds_ld<f32x2>(rb) + lds_ld<f32x2>(rb + 256)) + (lds_ld<f32x2>(rb + 512) + lds_ld<f32x2>(rb + 768));
  if (lane < 32) st16(c, off + lane * 16, (u32x4){__float_as_uint(sum[0]), __float_as_uint(sum[1]), c.tag[0], c.tag[1]});
}
template <int P, int I> DI void e_gv(Ctx& c) {
  if constexpr (P == P_GR) lds_st(c.lds0 + L_RED + c.wave * 256 + ((c.vA >> 4) & 7) * 32 + (c.vA >> 8) * 8, red8(c.ga));
  else if constexpr (P == P_GO) { if (c.wave == 3) gemv_out(c, W_LATE + blockIdx.x * 512, c.vA >> 4); }
  else {
    constexpr int r = 2 * (P == P_GA ? 0 : P == P_GB ? 1 : P == P_GC ? 2 : 3) + (I >> 4), e = I & 7;
    if constexpr (!(I & 8) && (e & 1)) asm volatile("v_and_b32 %0, 0xffff0000, %1" : "=v"(c.t[e]) : "v"(c.lw[r][e >> 1]));
    else if constexpr (!(I & 8)) asm volatile("v_lshlrev_b32 %0, 16, %1" : "=v"(c.t[e]) : "v"(c.lw[r][e >> 1]));
    else {
      float a = r ? c.ga[e >> 1][e & 1] : 0.f;
      if constexpr (r == 0) v_mul(a, c.t[e], c.ls[r]); else asm volatile("v_fmac_f32 %0, %1, %2" : "+v"(a) : "v"(c.t[e]), "v"(c.ls[r]));
      c.ga[e >> 1][e & 1] = a;
    }
  }
}
template <int I> DI void e_un(Ctx& c) {
  auto un = [](unsigned u) { float r; if constexpr (I & 1) asm volatile("v_and_b32 %0, 0xffff0000, %1" : "=v"(r) : "v"(u)); else asm volatile("v_lshlrev_b32 %0, 16, %1" : "=v"(r) : "v"(u)); return r; };
  constexpr int j = (I & 15) >> 1;
  if constexpr (I < 32) c.er[I >> 4][(I >> 3) & 1][(I >> 1) & 3][I & 1] = un(c.xr[I >> 4][(I >> 3) & 1][(I >> 1) & 3]);
  else if constexpr (I < 48) c.eg[j >> 2][j & 3][I & 1] = un(c.mv[j >> 1][j & 1]);
  else if constexpr (I < 64) c.sh[j][I & 1] = un(c.mv[4 + (j >> 1)][j & 1]);
  else if constexpr (I < 80) c.sc1[j][I & 1] = un(c.mv[8 + (j >> 1)][j & 1]);
  else {
    float r = c.sc1[j][I & 1];
    if constexpr (I < 96) asm volatile("v_add_f32 %0, 1.0, %0" : "+v"(r)); else asm volatile("v_cvt_pk_bf16_f32 %0, 0, %0" : "+v"(r));
    c.sc1[j][I & 1] = r;
  }
}
DI void touch(Ctx& c, unsigned v, const rsrc_t& r, unsigned o) { asm volatile("buffer_load_dword %0, %1, %2, %3 offen" : "+v"(c.tch) : "v"(v), "s"(r), "s"(o) : "memory"); }
template <int W> DI void warm(Ctx& c) {
  if constexpr (W == 0) touch(c, c.vT2, c.rO, 0);
  else if constexpr (W < 7) touch(c, c.vT3, (W & 1) ? c.r1 : c.r3, (W - 1) / 2 * 2048);
  else if constexpr (W < 9) touch(c, c.vT4, (W & 1) ? c.r1 : c.r3, 0);
  else if constexpr (W < 13) touch(c, c.vT5, c.r2, (W - 9) * (2 << 20));
  else if constexpr (W < 17) touch(c, c.vT2, c.r2, (W - 13) * (2 << 20));
  else if constexpr (W == 23) touch(c, c.vT1, c.rQ, 0);
  else touch(c, c.vT3 | (c.vA < 256 ? 0x40000000u : 0u), (W & 1) ? c.r1 : c.r3, (W - 17) / 2 * 2048);
}
DI unsigned slow_col(const void* base, int first, int n) {
  const unsigned k = (3 - (unsigned)((uintptr_t)base >> 7) - first) & 7;
  return k < n ? (first + k) * 128 : 0x40000000u;
}
DI void warm_code(Ctx& c) {
  unsigned o;
  asm volatile("s_mov_b32 %0, .-.Lblk_begin" : "=s"(o));
  touch(c, c.vC, c.rC, o);
}
template <int KS, int ST> DI void e_ck(Ctx& c) {
  if constexpr (ST < 2) asm volatile("v_accvgpr_read_b32 %0, a%c1" : "=v"(c.cw[KS < 4][ST]) : "i"(8 * KS + 4 * ST + 3));
  else if constexpr (KS & 3) asm volatile("v_max3_u32 %0, %0, %1, %2" : "+v"(c.ck[KS < 4]) : "v"(c.cw[KS < 4][0]), "v"(c.cw[KS < 4][1]));
  else asm volatile("v_max_u32 %0, %1, %2" : "=v"(c.ck[KS < 4]) : "v"(c.cw[KS < 4][0]), "v"(c.cw[KS < 4][1]));
}
template <int G, int... Is> DI void ck_group(Ctx& c, iseq<Is...>) { (e_ck<4 * G + Is / 3, Is % 3>(c), ...); }
template <int G, int... Is> DI void ld_group(Ctx& c, iseq<Is...>) { (ld_act<8 * G + Is>(c.vK[Is >> 1], c.rs, c.sA0 + G * 4 * KBS), ...); }
template <int G> DI void ck_retry(Ctx& c) {
  if (__builtin_expect(__any(c.ck[G == 0] == POISON), 0)) {
#pragma unroll 1
    for (unsigned spins = 0; spins < SPIN_CAP; ++spins) {
      ld_group<G>(c, make_iseq<8>{});
      wait_vm<0>();
      ck_group<G>(c, make_iseq<12>{});
      if (!__any(c.ck[G == 0] == POISON)) break;
    }
  }
}
template <int A> DI void action(Ctx& c) {
  if constexpr (A >= A_CK) ck_retry<A - A_CK>(c);
  else
  if constexpr (A == A_FLAG) { c.flv += 1; set_flag(c, c.fl3, c.flv); }
  else if constexpr (A == A_FLAG4) set_flag(c, c.fl4, c.flv);
  else {
    constexpr unsigned need = A == A_CHKX ? 1 : A;
    pin(c.pollv);
    if (__builtin_expect(!__all(c.pollv >= need), 0)) spin(c, c.pollo + (A == A_CHKX ? (c.fl4 ^ 128) : 0), need);
    if constexpr (A == A_CHK4) { c.pollo = 0; pin(c.pollo); }
  }
}
constexpr int rd_before(int q) { int n = 0; for (int i = q & ~3; i < q; ++i) n += PL.nr[i]; return n; }
template <int H, int YOUNG> DI void wait_for(Ctx& c) {
  constexpr int v = PL.vw[H] < 0 ? 63 : PL.vw[H];
  __builtin_amdgcn_s_waitcnt((v & 15) | ((v >> 4) << 14) | 0x70 | (YOUNG << 8));
  asm volatile("" ::: "memory");
}
template <int H> DI void waits(Ctx& c) {
  static_assert(!(H & 1) || (PL.vw[H] < 0 && !PL.act[H]), "waits sit in front of even half-steps");
  if constexpr (isbar(H)) bar();
  GAP();
  if constexpr (PL.act[H] != 0) { action<PL.act[H]>(c); GAP(); }
}
template <int H, int M> DI void slot(Ctx& c) {
  constexpr int cb = M >> 1, rb = M & 1, u = unit(H >> 3), j = grp(H >> 3), k = PL.op[H] / 1024, i = PL.op[H] % 1024, e = PL.e[4 * H + M];
  mfa<(j & 7) * 8 + (H & 6) + rb, u == U_V, j == 0 && (H & 6) == 0>(c.acc[u][H & 1][rb][cb], cat(c.wl[H & 7][cb], c.wh[H & 7][cb]));
  if constexpr (pg_of(4 * H + M) >= 0) pg_mark<pg_of(4 * H + M)>();
  GAP();
  constexpr int q = 4 * H + M, sk = PL.sc[q] / 1024, si = PL.sc[q] % 1024;
  if constexpr (sk == S_M0) m0_add<(si & 7) * SLOT>(c.mB);
  if constexpr (sk == S_SB) dma_off<si>(c);
  if constexpr (sk == S_SA) { c.sAA[si & 1] = c.sA0 + si * 4 * KBS; pins(c.sAA[si & 1]); }
  if constexpr (sk == S_SA4) { c.sAA[si & 1] = c.sA4[g4mix(si)] + g4kb(si) * KBS; pins(c.sAA[si & 1]); }
  if constexpr ((H & 1) && H + 1 < seg_end(H) && M == (WB2 && isbar(H + 1) ? 2 : 3)) wait_for<H + 1, (H + 5 <= seg_end(H) ? 4 + rd_before(q) : 0)>(c);
  if constexpr (M == 3 && H + 1 < seg_end(H)) waits<H + 1>(c);
  GAP();
  if constexpr (PL.nr[q] >= 1) rd<H + RD, rd_before(q)>(c);
  if constexpr (PL.nr[q] == 2) { GAP(); rd<H + RD, rd_before(q) + 1>(c); }
  GAP();
  if constexpr (M == 0) {
    if constexpr (k == K_D) dma<i>(c);
    if constexpr (k == K_T) warm<i>(c);
    if constexpr (k == K_C && i == 0) warm_code(c);
    if constexpr (k == K_C && i == 1) touch(c, c.vA << 20, c.rC, (((blockIdx.x >> 3) & 7) * 4 + c.wave) << 12);
    if constexpr (k == K_G && i < 8) asm volatile("buffer_load_dwordx4 %0, %1, %2, %3 offen" GNT : "=v"(c.lw[i & 7]) : "v"(c.vG0), "s"(c.rM), "s"(i * 6144 * 2) : "memory");
    if constexpr (k == K_A || k == K_A4) ld_act<i & 63>(c.vK[(i >> 1) & 3], c.rs, c.sAA[(i >> 3) & 1]);
    if constexpr (k == K_P) asm volatile("buffer_load_dword %0, %1, %2, %3 offen sc1" : "+v"(c.pollv) : "v"(c.pollo), "s"(c.rs), "s"(i ? c.fl4 ^ 128 : 0) : "memory");
    if constexpr (k == K_Z) asm volatile("buffer_store_dwordx4 %0, %1, %2, %3 offen offset:%4 sc1\n\ts_nop 1" ::"v"((u32x4){POISON, POISON, POISON, POISON}), "v"(c.vK[i >> 1]), "s"(c.rs), "s"(c.zo), "n"((i & 1) * 1024) : "memory");
    if constexpr (k == K_XS) asm volatile("buffer_store_dwordx4 %0, %1, %2, %3 offen offset:%4 sc1" ::"v"(c.acc[U_S][i >> 2][(i >> 1) & 1][i & 1]), "v"(c.vA), "s"(c.rs), "s"(c.xs4 + (i >> 2) * 4096), "n"((i & 3) * 1024) : "memory");
    if constexpr (k == K_XL) asm volatile("buffer_load_dwordx4 %0, %1, %2, %3 offen offset:%4 sc1" : "=v"(c.xl[i]) : "v"(c.vA), "s"(c.rs), "s"((c.xs4 ^ 32768) + (i >> 2) * 4096), "n"((i & 3) * 1024) : "memory");
  }
  GAP();
  if constexpr (e >= E_CK) e_ck<(e - E_CK) / 4, (e - E_CK) % 4>(c);
  else if constexpr (e >= 64 * PR_HH) e_hh<pr_blk(e / 64), pr_up(e / 64), e % 64>(c);
  else if constexpr (e == 64 * P_PREV) e_prev(c);
  else if constexpr (e >= 0 && (e / 64 == P_K || e / 64 == P_V)) e_kv<e / 64 == P_V, e % 64>(c);
  else if constexpr (e >= 64 * P_UA) e_un<e - 64 * P_UA>(c);
  else if constexpr (e >= 0) e_gv<e / 64, e % 64>(c);
  GAP();
}
template <int H0, int... Hs> DI void hsteps_(Ctx& c, iseq<Hs...>) { ((slot<H0 + Hs, 0>(c), slot<H0 + Hs, 1>(c), slot<H0 + Hs, 2>(c), slot<H0 + Hs, 3>(c)), ...); }
template <int Ta, int Tb> DI void stages(Ctx& c) { hsteps_<8 * Ta>(c, make_iseq<8 * (Tb - Ta)>{}); }
template <int H0, int... Is> DI void rd0(Ctx& c, iseq<Is...>) { (rd<H0 + Is / 4, Is % 4>(c), ...); }
template <int... Is> DI void ld0(Ctx& c, iseq<Is...>) { (ld_act<Is>(c.vK[(Is >> 1) & 3], c.rs, c.sA0 + (Is >> 3) * 4 * KBS), ...); }
template <int S> DI void start(Ctx& c, unsigned base) {
  c.sA0 = base; pins(c.sA0);
  ld0(c, make_iseq<2 * seg_pre(S)>{});
  static_assert(seg_pre(S) % 4 == 0 && PL.op[8 * seg_t0(S)] / 1024 == K_A, "the stream opens with the next group");
  c.sAA[(seg_pre(S) / 4) & 1] = base + seg_pre(S) * KBS; pins(c.sAA[(seg_pre(S) / 4) & 1]);
  bar();
  rd0<8 * seg_t0(S)>(c, make_iseq<4 * RD>{});
  GAP();
  wait_for<8 * seg_t0(S), 8>(c);
  GAP();
}
template <int S> DI void start_nf(Ctx& c, unsigned base) {
  c.sA0 = base; pins(c.sA0);
  ld0(c, make_iseq<2 * seg_pre(S)>{});
  c.sAA[(seg_pre(S) / 4) & 1] = base + seg_pre(S) * KBS; pins(c.sAA[(seg_pre(S) / 4) & 1]);
  wait_vm<2 * seg_pre(S) - 2>();
  ck_group<0>(c, make_iseq<3>{});
  ck_retry<0>(c);
  GAP();
  wait_for<8 * seg_t0(S), 8>(c);
  GAP();
}
DI void lane_off(unsigned (&v)[4], int lane, int wave, int pitch, int col) {
  const int g = lane >> 4, q = (lane >> 2) & 3, p = lane & 3;
#pragma unroll
  for (int i = 0; i < 4; ++i) { v[i] = (32 * wave + 8 * i + 4 * (g >> 1) + q) * pitch + col + (g & 1) * 64 + (p ^ (g & 2)) * 16 - 1024 * i; pin(v[i]); }
}
DI void mod_load(Ctx& c) {
  asm volatile("buffer_load_dwordx4 %0, %8, %9, 0 offen sc1\n\tbuffer_load_dwordx4 %1, %8, %9, 0 offen offset:512 sc1\n\tbuffer_load_dwordx4 %2, %8, %9, 0 offen offset:1024 sc1\n\tbuffer_load_dwordx4 %3, %8, %9, 0 offen offset:1536 sc1\n\t"
               "buffer_load_dwordx4 %4, %8, %9, %10 offen sc1\n\tbuffer_load_dwordx4 %5, %8, %9, %10 offen offset:512 sc1\n\tbuffer_load_dwordx4 %6, %8, %9, %10 offen offset:1024 sc1\n\tbuffer_load_dwordx4 %7, %8, %9, %10 offen offset:1536 sc1"
               : "=&v"(c.mp[0][0]), "=&v"(c.mp[0][1]), "=&v"(c.mp[0][2]), "=&v"(c.mp[0][3]), "=&v"(c.mp[1][0]), "=&v"(c.mp[1][1]), "=&v"(c.mp[1][2]), "=&v"(c.mp[1][3]) : "v"(c.mo), "s"(c.rs), "s"(65536) : "memory");
}
template <int N> DI void mod_wait(Ctx& c) {
  asm volatile("s_waitcnt vmcnt(%8)" : "+v"(c.mp[0][0]), "+v"(c.mp[0][1]), "+v"(c.mp[0][2]), "+v"(c.mp[0][3]), "+v"(c.mp[1][0]), "+v"(c.mp[1][1]), "+v"(c.mp[1][2]), "+v"(c.mp[1][3]) : "n"(N) : "memory");
}
DI void mod_vectors(Ctx& c, int lane) {
  const int g = lane >> 4;
  if (__builtin_expect(!(tagged(c, c.mp[0]) && tagged(c, c.mp[1])), 0)) {
#pragma unroll 1
    for (unsigned spins = 0; spins < SPIN_CAP; ++spins) { mod_load(c); mod_wait<0>(c); if (tagged(c, c.mp[0]) && tagged(c, c.mp[1])) break; }
  }
  dmas<2, 15, 17>(c);
  f32x4 a;
#pragma unroll
  for (int e = 0; e < 4; ++e) { const u32x4 (&m)[4] = c.mp[e >> 1]; a[e] = (__uint_as_float(m[0][e & 1]) + __uint_as_float(m[1][e & 1])) + (__uint_as_float(m[2][e & 1]) + __uint_as_float(m[3][e & 1])); }
  const unsigned pb = c.lds0 + L_WV + c.wave * 1024;
  lds_st(pb + lane * 4, pk2(a[0], a[1])); lds_st(pb + 256 + lane * 4, pk2(a[2], a[3]));
#pragma unroll
  for (int i = 0; i < 12; ++i) c.mv[i] = lds_ld<u32x2>(pb + i * 32 + g * 8);
}
}

DI u32x4 gated_resid(const f32x4& a0, const f32x4& a1, const f32x2 (&eg)[4], const f32x2 (&er)[4]) {
  u32x4 o;
#pragma unroll
  for (int j = 0; j < 4; ++j) {
    const f32x4& v = j < 2 ? a0 : a1;
    const f32x2 r = (f32x2){v[2 * (j & 1)], v[2 * (j & 1) + 1]} * eg[j] + er[j];
    o[j] = pk2(r[0], r[1]);
  }
  return o;
}

DI void phase1(Ctx& c) {
  using namespace en;
  const int tid = opaque_tid(), lane = tid & 63, b = opaque_bid(), wave = c.wave;
  const int fr = lane & 15, g = lane >> 4, q = (lane >> 2) & 3, p = lane & 3;
  c.tch = 0;
  asm volatile("global_load_dword %0, %1, %2" : "+v"(c.tch) : "v"((b >> 3) * 256 + (tid & 1) * 128), "s"(c.pc0) : "memory");
  GAP();
  int tm, tn; map_g1(b, tm, tn);
  int tm2, tn2; map_g2(b, tm2, tn2);
  u32x4 gw[4], xs[4], lc; u32x2 gc, ep;
  c.rM = mk_rsrc(c.wmod, D_ * 6 * D_ * 2);
  {
    const int k0 = (b & 7) * 128 + (tid >> 3) * 4;
    asm volatile("global_load_dwordx4 %0, %1, %2" : "=v"(lc) : "v"((tid >> 3) * 16), "s"(c.cc + (b & 3) * 256) : "memory");
    asm volatile("global_load_dwordx2 %4, %10, %11\n\tbuffer_load_dwordx4 %0, %5, %6, 0 offen" GNT "\n\tbuffer_load_dwordx4 %1, %5, %6, %7 offen" GNT "\n\tbuffer_load_dwordx4 %2, %5, %6, %8 offen" GNT "\n\tbuffer_load_dwordx4 %3, %5, %6, %9 offen" GNT
                 : "=&v"(gw[0]), "=&v"(gw[1]), "=&v"(gw[2]), "=&v"(gw[3]), "=&v"(gc)
                 : "v"((k0 * 6144 + (b >> 3) * 64 + (tid & 7) * 8) * 2), "s"(c.rM), "s"(12288), "s"(24576), "s"(36864), "v"(k0 * 2), "s"(c.cc) : "memory");
    asm volatile("global_load_dwordx2 %0, %1, %2 sc1" : "=v"(ep) : "v"(0), "s"(c.ws) : "memory");
    asm volatile("global_load_dwordx4 %0, %4, %5\n\tglobal_load_dwordx4 %1, %4, %5 offset:512\n\tglobal_load_dwordx4 %2, %4, %5 offset:1024\n\tglobal_load_dwordx4 %3, %4, %5 offset:1536"
                 : "=&v"(xs[0]), "=&v"(xs[1]), "=&v"(xs[2]), "=&v"(xs[3]) : "v"((tid >> 5) * 2048 + (tid & 31) * 16), "s"(c.x + (tm2 * 128 + tn2 * 8) * D_) : "memory");
  }
  GAP();
  c.rs = mk_rsrc(c.ws, W_END); c.rQ = mk_rsrc(c.wqkv, D_ * 3 * D_ * 2); c.rO = mk_rsrc(c.wo, D_ * D_ * 2);
  c.r1 = mk_rsrc(c.w1, D_ * F_ * 2); c.r3 = mk_rsrc(c.w3, D_ * F_ * 2); c.r2 = mk_rsrc(c.w2, D_ * F_ * 2);
  { unsigned n; asm volatile("s_mov_b32 %0, .Lblk_end-.Lblk_begin" : "=s"(n)); c.rC = mk_rsrc((const void*)c.pc0, n); }
  c.mB = c.lds0 + wave * 4096;
  pins(c.rs); pins(c.rQ); pins(c.rO); pins(c.rM); pins(c.r1); pins(c.r3); pins(c.r2); pins(c.rC); pins(c.mB);
  const int j8 = ((b >> 3) * 4 + wave) * 8;
  const unsigned slow1 = (j8 + (lane & 7)) * 6144 + slow_col(c.wqkv, BAND1, 4), row1 = (b * 4 + wave) * 6144 + fr * 128;
  c.vT1 = sel(lane < 8, slow1, OOB); pin(c.vT1);
  c.vC = sel(lane == 2, (b * 4 + wave) * 128, j8 * 32 + (lane & 1) * 128);
  FST();
  warm_code(c);
  unsigned tk = sel(lane < 16, row1, sel(lane < 24, slow1, OOB)), tvq = sel(lane < 32, row1 + (lane >> 4) * 4096, sel(lane < 40, slow1 + 4096, OOB));
  unsigned i0 = W_HALO + (((tm2 - 1) & 15) * 16 + tn2) * 4096 + wave * 1024 + lane * 16, iz = lane < 16 ? POISON : 0u;
  unsigned i1 = sel(lane < 16, W_STATS + (tm2 * 4 + wave) * 4096 + tn2 * 256 + lane * 16, sel(lane == 16, flag_off(tm2, wave, tn2, PH_G1), OOB));
  unsigned xo = (fr * D_ + (g & 1) * 16 + (g >> 1) * 8) * 2, xo1 = xo + 16 * D_ * 2;
  unsigned o0 = W_PART + ((wave >> 1) * 16 + tn2) * 4096 + (wave & 1) * 2048 + lane * 16, o1 = W_LN1 + (tm2 * 128 + wave * 32 + fr) * 16;
  unsigned fl1 = sel(lane < 16, flag_off(tm, wave, fr, 0), flag_off(min(tm + 1, 15), wave, tn, 0));
  pin(tk); pin(tvq); pin(i0); pin(i1); pin(iz); pin(xo); pin(xo1); pin(o0); pin(o1); pin(fl1);
  lane_off(c.v1, lane, wave, 6144, tn * 128);
  c.vA = lane * 16; pin(c.vA); c.vK[0] = c.vA;
#pragma unroll
  for (int i = 1; i < 4; ++i) { c.vK[i] = c.vA + i * KBS; pin(c.vK[i]); }
  c.vT2 = sel(lane < 16, (b * 4 + wave) * 2048 + lane * 128, sel(lane < 24, (j8 + lane - 16) * 2048 + slow_col(c.wo, BAND2, 8), OOB));
  c.vT3 = sel(lane < 16, (b * 4 + wave) * (F_ * 2) + lane * 128, sel(lane < 24, (j8 + lane - 16) * (F_ * 2) + slow_col(c.w1, (b & 3) * 4, 4), OOB));
  c.vG0 = (((b & 3) * 256 + (tid >> 3) * 8) * 6144 + 2048 + (b >> 2) * 64 + (tid & 7) * 8) * 2;
  pin(c.vG0);
  pin(c.vT2); pin(c.vT3);
#pragma unroll
  for (int w = 0; w < 2; ++w) {
    const unsigned bw = c.lds0 + w * 65536 + g * 512 + q * 64 + p * 8;
    c.bW[0][w] = bw + (g & 1) * 32; c.bW[1][w] = bw + ((g & 1) ^ 1) * 32;
    pin(c.bW[0][w]); pin(c.bW[1][w]);
  }
  lane_off(c.v2, lane, wave, 2048, 0);
  { int tm3, tn3; map_g3(b, tm3, tn3); lane_off(c.v3, lane, wave, F_ * 2, tn3 * 128); }
  c.sB4[0][0] = tn2 * 128; c.zo = W_H2 + 2 * tn2 * KBS + (tm2 * 8 + 2 * wave) * 1024; pins(c.zo);
  c.mo = W_LATE + ((lane >> 5) * 16 + tn2) * 2048 + (lane & 31) * 16; pin(c.mo);
  c.kvw = c.lds0 + L_KV + wave * 4096 + lane * 16; pin(c.kvw);
  c.hso = W_HALO + ((tm - 1) * 16 + tn) * 4096;
  unsigned e_sel[4]; int e_base; f32x2 e_bias;
  {
    e_base = (fr & ~1) - 4 * g;
    const bool first = tm == 0 && wave == 0;
    e_bias = (f32x2){first && e_base < 0 ? -3.0e38f : 0.f, first && e_base < 2 ? -3.0e38f : 0.f};
    const unsigned s0 = (fr & 1) ? 0x01000c0cu : 0x0c0c0100u, s1 = (fr & 1) ? 0x03020c0cu : 0x0c0c0302u, z = 0x0c0c0c0cu;
    e_sel[0] = e_base >= 0 ? z : s0; e_sel[1] = e_base >= 2 ? z : s1; e_sel[2] = e_base >= 0 ? s0 : z; e_sel[3] = e_base >= 2 ? s1 : z;
    pin(e_base); pin(e_bias); pin(e_sel[0]); pin(e_sel[1]); pin(e_sel[2]); pin(e_sel[3]);
  }
  GAP();
  float sv[4];
  asm volatile("s_waitcnt vmcnt(10)" : "+v"(gc), "+v"(lc)::"memory");
#pragma unroll
  for (int i = 0; i < 4; ++i) { sv[i] = silu_bf((u16)(gc[i >> 1] >> (16 * (i & 1)))); pin(sv[i]); }
#pragma unroll
  for (int i = 0; i < 8; ++i) { c.ls[i] = silu_bf((u16)(lc[i >> 1] >> (16 * (i & 1)))); pin(c.ls[i]); }
  GAP();
  if constexpr (VQ == 3) { touch(c, tk, c.rQ, 2048); touch(c, tvq, c.rQ, 0); }
  FST();
  asm volatile("s_waitcnt vmcnt(%5)" : "+v"(gw[0]), "+v"(gw[1]), "+v"(gw[2]), "+v"(gw[3]), "+v"(ep) : "n"(VQ == 3 ? 7 : 5) : "memory");
  c.tag[0] = __builtin_amdgcn_readfirstlane(ep[0]) + 1; c.tag[1] = __builtin_amdgcn_readfirstlane(ep[1]);
  if constexpr (VQ != 3) touch(c, tk, c.rQ, 2048);
  GAP();
  FST();
  {
    f32x2 acc[4];
#pragma unroll
    for (int j = 0; j < 4; ++j) acc[j] = (f32x2){0.f, 0.f};
#pragma unroll
    for (int i = 0; i < 4; ++i) {
#pragma unroll
      for (int j = 0; j < 4; ++j) acc[j] += unpk(gw[i][j]) * sv[i];
    }
    lds_st(c.lds0 + L_RED + wave * 256 + (lane & 7) * 32 + g * 8, red8(acc));
    wait_lgkm<0>(); bar();
    if (wave == 3) gemv_out(c, W_PART + b * 512, lane);
    GAP();
    asm volatile("global_load_dwordx4 %0, %4, %6\n\tglobal_load_dwordx4 %1, %4, %6 offset:64\n\tglobal_load_dwordx4 %2, %5, %6\n\tglobal_load_dwordx4 %3, %5, %6 offset:64"
                 : "=&v"(c.xr[0][0]), "=&v"(c.xr[0][1]), "=&v"(c.xr[1][0]), "=&v"(c.xr[1][1]) : "v"(xo), "v"(xo1), "s"(c.x + (tm2 * 128 + wave * 32) * D_ + tn2 * 64) : "memory");
    if constexpr (VQ == 0) touch(c, tvq, c.rQ, 0);
    asm volatile("s_waitcnt vmcnt(%4)" : "+v"(xs[0]), "+v"(xs[1]), "+v"(xs[2]), "+v"(xs[3]) : "n"(6 + (VQ == 0 || VQ == 3)) : "memory");
    f32x2 f[16], s2 = {0.f, 0.f};
#pragma unroll
    for (int i = 0; i < 16; ++i) { f[i] = unpk(xs[i >> 2][i & 3]); s2 += f[i]; }
    const float mean = xadd16(row16_sum(s2[0] + s2[1])) * (1.f / 1024.f);
    f32x2 q2 = {0.f, 0.f};
#pragma unroll
    for (int i = 0; i < 16; ++i) { const f32x2 d = f[i] - mean; q2 += d * d; }
    const float rstd = ln_rstd(xadd16(row16_sum(q2[0] + q2[1])) * (1.f / 1024.f));
    if (!(lane & 31)) st16(c, W_LN1 + (tm2 * 128 + tn2 * 8 + (tid >> 5)) * 16, (u32x4){__float_as_uint(rbf(mean)), __float_as_uint(rstd), c.tag[0], c.tag[1]});
  }
  st16(c, i0, (u32x4){POISON, POISON, POISON, POISON}); st16(c, i1, (u32x4){iz, iz, iz, iz});
  GAP();
  FST();
  bar();
  dmas<1, 0, NFD>(c);
  if constexpr (VQ == 1) touch(c, tvq, c.rQ, 0);
  FST();
  f32x4 ln;
  {
    u32x4 v[4];
#pragma unroll 1
    for (unsigned spins = 0; spins < SPIN_CAP; ++spins) {
      asm volatile("buffer_load_dwordx4 %0, %4, %6, 0 offen sc1\n\tbuffer_load_dwordx4 %1, %4, %6, 0 offen offset:1024 sc1\n\tbuffer_load_dwordx4 %2, %5, %6, 0 offen sc1\n\tbuffer_load_dwordx4 %3, %5, %6, 0 offen offset:256 sc1\n\ts_waitcnt vmcnt(0)"
                   : "=&v"(v[0]), "=&v"(v[1]), "=&v"(v[2]), "=&v"(v[3]) : "v"(o0), "v"(o1), "s"(c.rs) : "memory");
      if (tagged(c, v)) break;
      __builtin_amdgcn_s_sleep(1);
    }
    pin(c.xr[0][0]); pin(c.xr[0][1]); pin(c.xr[1][0]); pin(c.xr[1][1]);
    FST();
    ln = (f32x4){__uint_as_float(v[2][0]), __uint_as_float(v[2][1]), __uint_as_float(v[3][0]), __uint_as_float(v[3][1])};
    lds_st(c.lds0 + L_RED + wave * 256 + (lane & 31) * 8, (f32x2){xadd32(__uint_as_float(v[0][0]) + __uint_as_float(v[1][0])), xadd32(__uint_as_float(v[0][1]) + __uint_as_float(v[1][1]))});
    wait_lgkm<0>(); bar();
    if constexpr (VQ == 2) touch(c, tvq, c.rQ, 0);
  }
  FST();
  {
    const f32x4 r = lds_ld<f32x4>(c.lds0 + L_RED + (g & 1) * 512 + fr * 16) + lds_ld<f32x4>(c.lds0 + L_RED + (g & 1) * 512 + 256 + fr * 16);
    const f32x2 one = (g & 1) ? (f32x2){1.f, 1.f} : (f32x2){0.f, 0.f};
    const f32x2 m0 = rbf2(rbf2((f32x2){r[0], r[1]}) + one), m1 = rbf2(rbf2((f32x2){r[2], r[3]}) + one);
    const unsigned Mt = c.lds0 + L_WV + wave * 1024, off = W_H1 + 2 * tn2 * KBS + (tm2 * 8 + 2 * wave) * 1024 + lane * 16;
    lds_st(Mt + (lane & 31) * 16, (f32x4){m0[0], m0[1], m1[0], m1[1]});
#pragma unroll
    for (int s = 0; s < 2; ++s) {
      const unsigned a = Mt + s * 128 + g * 16;
      const f32x4 sh0 = lds_ld<f32x4>(a), sh1 = lds_ld<f32x4>(a + 64), sc0 = lds_ld<f32x4>(a + 256), sc1 = lds_ld<f32x4>(a + 320);
      const f32x2 sc[4] = {{sc0[0], sc0[1]}, {sc0[2], sc0[3]}, {sc1[0], sc1[1]}, {sc1[2], sc1[3]}}, sh[4] = {{sh0[0], sh0[1]}, {sh0[2], sh0[3]}, {sh1[0], sh1[1]}, {sh1[2], sh1[3]}};
#pragma unroll
      for (int i = 0; i < 2; ++i) {
        c.xr[i][s] = swz_rows(c.xr[i][s]);
        u32x4 o;
#pragma unroll
        for (int j = 0; j < 4; ++j) {
          dma_at<1, NFD, NFD + NF1>(c, 8 * s + 4 * i + j - (16 - NF1));
          const f32x2 y = rbf2(rbf2(rbf2(unpk(c.xr[i][s][j]) - ln[2 * i]) * ln[2 * i + 1]) * sc[j]) + sh[j];
          o[j] = pk2(y[0], y[1]);
        }
        st16(c, off + s * KBS + i * 1024, o);
      }
    }
  }
  GAP();
  STAMP();
  dmas<1, NFD + NF1, NFD + NF1 + NF2>(c);
  wait_vm<NF2>();
  asm volatile("s_mov_b64 exec, 1\n\tbuffer_store_dwordx2 %0, off, %1, %2 sc1\n\ts_mov_b64 exec, -1" ::"v"((u32x2){c.tag[0], c.tag[1]}), "s"(c.rs), "s"(flag_off(tm2, wave, tn2, 0)) : "memory");
  STAMP();
  bar();
  FST();
  dmas<1, NFD + NF1 + NF2, 64>(c);
  FST();
  pg_hop<0>(wave);
  await<PH_LN1>(c, fl1);
  if (b + wave == 0) asm volatile("s_mov_b64 exec, 1\n\tbuffer_store_dwordx2 %0, off, %1, 0 sc1\n\ts_mov_b64 exec, -1" ::"v"((u32x2){c.tag[0], c.tag[1]}), "s"(c.rs) : "memory");
  STAMP();
  start<1>(c, W_H1 + (tm * 8 + 2 * wave) * 1024);
  STAMP();
  stages<0, 8>(c);
  STAMP();
  stages<8, 16>(c);
  STAMP();
  stages<16, 24>(c);
  mfma_fence();
  STAMP();
  {
    u32x4 qf[2][2];
#pragma unroll
    for (int i = 0; i < 4; ++i) qf[i >> 1][i & 1] = pack8(c.acc[U_Q][i >> 1][i & 1][0], c.acc[U_Q][i >> 1][i & 1][1]);
    asm volatile("s_waitcnt vmcnt(%4) lgkmcnt(0)" : "+v"(c.hl[0]), "+v"(c.hl[1]), "+v"(c.hl[2]), "+v"(c.hl[3]) : "n"(PL.halo) : "memory");
    if (wave == 0) retry(c, c.hl, c.vA + c.hso, 1024);
    dmas<2, 0, 1>(c);
    u32x4 vo[2][2][2];
#pragma unroll
    for (int h = 0; h < 2; ++h)
#pragma unroll
      for (int j = 0; j < 2; ++j) {
        vo[h][0][j] = (u32x4){c.hl[2 + h][2 * j], c.hl[2 + h][2 * j + 1], c.vf[h][0][2 * j], c.vf[h][0][2 * j + 1]};
        vo[h][1][j] = (u32x4){c.vf[h][0][2 * j], c.vf[h][0][2 * j + 1], c.vf[h][1][2 * j], c.vf[h][1][2 * j + 1]};
      }
#pragma unroll
    for (int i = 0; i < 4; ++i) { pin(qf[i >> 1][i & 1]); pin(c.hl[i]); }
    GAP(); nops<2>();
    f32x4 sp[2][2], sc[2][2];
#pragma unroll
    for (int h = 0; h < 2; ++h) { mfv(sp[h][0], c.hl[h], qf[h][0]); mfv(sc[h][0], c.kf[h][0], qf[h][0]); mfv(sp[h][1], c.kf[h][0], qf[h][1]); mfv(sc[h][1], c.kf[h][1], qf[h][1]); }
    GAP();
#pragma unroll
    for (int i = 0; i < 8; ++i) pin(vo[i >> 2][(i >> 1) & 1][i & 1]);
    dmas<2, 1, 2>(c);
    mfma_fence();
    const bool par = fr & 1, cur0 = e_base >= 0, cur1 = e_base >= 2;
    auto red2 = [](float& u, float& v, auto op) {
      auto r = __builtin_amdgcn_permlane16_swap(__float_as_uint(u), __float_as_uint(v), false, false);
      const float m = op(__uint_as_float(r[0]), __uint_as_float(r[1]));
      auto r2 = __builtin_amdgcn_permlane32_swap(__float_as_uint(m), __float_as_uint(m), false, false);
      const float t = op(__uint_as_float(r2[0]), __uint_as_float(r2[1]));
      auto r3 = __builtin_amdgcn_permlane16_swap(__float_as_uint(t), __float_as_uint(t), false, false);
      u = __uint_as_float(r3[0]); v = __uint_as_float(r3[1]);
    };
    f32x2 l[2][2]; float mx[2][2], sm[2][2]; u32x4 pw[2][2];
#pragma unroll
    for (int h = 0; h < 2; ++h)
#pragma unroll
      for (int i = 0; i < 2; ++i) {
        dma_at<2, 2, 6>(c, 2 * h + i);
        const float a0 = sp[h][i][0], a1 = sp[h][i][1], a2 = sp[h][i][2], a3 = sp[h][i][3], b0 = sc[h][i][0], b1 = sc[h][i][1], b2 = sc[h][i][2], b3 = sc[h][i][3];
        const float x0 = cur0 ? b0 : a0, x1 = cur0 ? b1 : a1, x2 = cur1 ? b2 : a2, x3 = cur1 ? b3 : a3;
        const float y0 = par ? x1 : x0, y1 = par ? x3 : x2;
        l[h][i] = rbf2(rbf2((f32x2){y0, y1}) * 0.17677669529663687f);
        if (i == 0) l[h][i] += e_bias;
        mx[h][i] = fmax1(l[h][i][0], l[h][i][1]);
      }
    dmas<2, 6, 7>(c);
#pragma unroll
    for (int h = 0; h < 2; ++h) red2(mx[h][0], mx[h][1], [](float x, float y) { return fmax1(x, y); });
#pragma unroll
    for (int h = 0; h < 2; ++h)
#pragma unroll
      for (int i = 0; i < 2; ++i) {
        if (i == 0) dma_at<2, 7, 9>(c, h);
        const f32x2 t = (l[h][i] - mx[h][i]) * 1.4426950408889634f;
        l[h][i] = (f32x2){__builtin_amdgcn_exp2f(t[0]), __builtin_amdgcn_exp2f(t[1])};
        sm[h][i] = l[h][i][0] + l[h][i][1];
      }
    dmas<2, 9, 10>(c);
#pragma unroll
    for (int h = 0; h < 2; ++h) red2(sm[h][0], sm[h][1], [](float x, float y) { return x + y; });
#pragma unroll
    for (int h = 0; h < 2; ++h)
#pragma unroll
      for (int i = 0; i < 2; ++i) {
        if (i == 0) dma_at<2, 10, 12>(c, h);
        const f32x2 pr = l[h][i] * __builtin_amdgcn_rcpf(sm[h][i]);
        const unsigned pp = pk2(pr[0], pr[1]);
        pw[h][i] = (u32x4){__builtin_amdgcn_perm(pp, pp, e_sel[0]), __builtin_amdgcn_perm(pp, pp, e_sel[1]), __builtin_amdgcn_perm(pp, pp, e_sel[2]), __builtin_amdgcn_perm(pp, pp, e_sel[3])};
        pin(pw[h][i]);
      }
    GAP(); nops<2>();
    f32x4 o[2][2][2];
#pragma unroll
    for (int i = 0; i < 8; ++i) mfv(o[i >> 2][(i >> 1) & 1][i & 1], vo[i >> 2][(i >> 1) & 1][i & 1], pw[i >> 2][(i >> 1) & 1]);
    dmas<2, 12, 13>(c);
    static_assert(NFREE2 >= 13, "slots of these pieces are free");
    mfma_fence();
    const unsigned oo = W_O + 2 * tn * KBS + (tm * 8 + 2 * wave) * 1024 + c.vA;
#pragma unroll
    for (int i = 0; i < 4; ++i) st16(c, oo + (i >> 1) * KBS + (i & 1) * 1024, pack8(o[i >> 1][i & 1][0], o[i >> 1][i & 1][1]));
  }
  GAP();
  mod_load(c);
  STAMP();
  wait_vm<8>();
  set_flag(c, flag_off(tm, wave, tn, PH_G1), 1);
  STAMP();
}

DI void setup3(Ctx& c) {
  using namespace en;
  const int lane = opaque_tid() & 63, b = opaque_bid(), wave = c.wave;
  int tm, tn, tm4, tn4; map_g3(b, tm, tn); map_g2(b, tm4, tn4);
  {
    const int g = lane >> 4, q = (lane >> 2) & 3, p = lane & 3;
#pragma unroll
    for (int i = 0; i < 4; ++i) {
      c.bM[i >> 1][i & 1] = c.bW[i >> 1][i & 1] - g * 256; pin(c.bM[i >> 1][i & 1]);
      c.vM[i] = (32 * wave + 16 * (i >> 1) + 4 * g + q) * (F_ * 2) + (96 + tn) * 64 + (p ^ (2 * (g & 1))) * 16 - 1024 * i; pin(c.vM[i]);
    }
    const int j8 = ((b >> 3) * 4 + wave) * 8;
    c.vT4 = sel(lane < 16, (j8 + (lane >> 1)) * (F_ * 2) + 6144 + (b & 3) * 256 + (lane & 1) * 128, OOB);
    c.vT5 = sel(lane < 16, c.vT2, OOB);
    c.vT2 = sel((lane >> 3) == 2, (j8 + lane - 16) * 2048 + slow_col(c.w2, BAND2, 8), OOB);
    pin(c.vT2); pin(c.vT4); pin(c.vT5);
    const int kh = tn4 & 1;
#pragma unroll
    for (int i = 0; i < 4; ++i) { c.sB4[i >> 1][i & 1] = (tn4 ^ 1 ^ (i & 1)) * 128 + kh * (16 >> (i >> 1)) * 32 * 2048; pins(c.sB4[i >> 1][i & 1]); }
#pragma unroll
    for (int i = 0; i < 2; ++i) { c.sA4[i] = W_HH + kh * (16 >> i) * KBS + (tm4 * 8 + 2 * wave) * 1024; pins(c.sA4[i]); }
    c.so[1] = W_HH + tn * KBS + (tm * 8 + 2 * wave) * 1024; c.so[0] = c.so[1] + tn * KBS;
    c.fl3 = flag_off(tm, wave, tn, PH_G3); c.fl4 = flag_off(tm4, wave, tn4, PH_G4);
    c.xs4 = W_XCH + (tm4 * 16 + tn4) * 32768 + wave * 8192;
    c.pollo = flag_off(tm4, wave, kh * 8 + (lane & 7), PH_G3); c.flv = 0; c.pollv = 0;
    pin(c.pollo); pin(c.flv); pins(c.so[0]); pins(c.so[1]); pins(c.fl3); pins(c.fl4); pins(c.xs4);
  }
}
DI void phase2(Ctx& c) {
  using namespace en;
  const int tid = opaque_tid(), lane = tid & 63, b = opaque_bid(), wave = c.wave;
  const int fr = lane & 15, g = lane >> 4;
  int tm, tn; map_g2(b, tm, tn);
  bar();
  dmas<2, 13, 15>(c);
  mod_wait<3>(c);
  mod_vectors(c, lane);
  dmas<2, 17, 64>(c);
  STAMP();
  await<PH_G1>(c, flag_off(tm, wave, fr, 0));
  STAMP();
  start<2>(c, W_O + (tm * 8 + 2 * wave) * 1024);
  STAMP();
  stages<24, 32>(c);
  mfma_fence();
  STAMP();
  f32x2 d[2][8]; float s1[2];

#pragma unroll
  for (int i = 0; i < 2; ++i) {
#pragma unroll
    for (int s = 0; s < 2; ++s) {
      dma_at<3, 0, NFREE3>(c, 4 * i + 2 * s);
      c.xr[i][s] = gated_resid(c.acc[U_O][s][i][0], c.acc[U_O][s][i][1], c.eg[s], c.er[i][s]);
      dma_at<3, 0, NFREE3>(c, 4 * i + 2 * s + 1);
#pragma unroll
      for (int j = 0; j < 4; ++j) d[i][4 * s + j] = unpk(c.xr[i][s][j]);
    }
    const f32x2 s2 = ((d[i][0] + d[i][1]) + (d[i][2] + d[i][3])) + ((d[i][4] + d[i][5]) + (d[i][6] + d[i][7]));
    s1[i] = s2[0] + s2[1];
  }
  const f32x2 mu = spread2(add2(s1[0], s1[1]) * (1.f / 64.f));
#pragma unroll
  for (int i = 0; i < 2; ++i) {
    f32x2 q2 = {0.f, 0.f};
#pragma unroll
    for (int j = 0; j < 8; ++j) { if (!(j & 3)) dma_at<3, 8, NFREE3>(c, 2 * i + (j >> 2)); d[i][j] -= mu[i]; q2 += d[i][j] * d[i][j]; }
    s1[i] = q2[0] + q2[1];
  }
  const f32x2 m2 = spread2(add2(s1[0], s1[1]));
  const unsigned sb = W_STATS + (tm * 4 + wave) * 4096;
  if (g == 0) st16(c, sb + tn * 256 + fr * 16, (u32x4){min(__float_as_uint(mu[0]), POISON - 1), min(__float_as_uint(m2[0]), POISON - 1), min(__float_as_uint(mu[1]), POISON - 1), min(__float_as_uint(m2[1]), POISON - 1)});
  GAP();
  dmas<3, 12, (NFREE3 < 16 ? NFREE3 : 16)>(c);
  STAMP();
  u32x4 pv[4];
  pg_hop<1>(wave);
  ld16x4(c, pv, sb + c.vA, 1024);
  retry(c, pv, sb + c.vA, 1024);
  STAMP();
  {
    f32x2 um[4], s2 = {0.f, 0.f}, q2 = {0.f, 0.f};
#pragma unroll
    for (int t = 0; t < 4; ++t) { um[t] = (f32x2){__uint_as_float(pv[t][0]), __uint_as_float(pv[t][2])}; s2 += um[t]; q2 += (f32x2){__uint_as_float(pv[t][1]), __uint_as_float(pv[t][3])}; }
    const float mg = add2(s2[0], s2[1]) * (1.f / 16.f);
    const f32x2 mean = spread2(mg);
    f32x2 e2 = {0.f, 0.f};
#pragma unroll
    for (int t = 0; t < 4; ++t) { const f32x2 dd = um[t] - mean; e2 += dd * dd; }
    q2 += e2 * 64.f;
    const f32x2 rs = spread2(ln_rstd(add2(q2[0], q2[1]) * (1.f / 1024.f))), mb = rbf2(mean);
    const unsigned ob = W_H2 + 2 * tn * KBS + (tm * 8 + 2 * wave) * 1024 + c.vA;
#pragma unroll
    for (int i = 0; i < 2; ++i) {
      const float dm = (mu[i] - mb[i]) * rs[i];
#pragma unroll
      for (int s = 0; s < 2; ++s) {
        u32x4 o;
#pragma unroll
        for (int j = 0; j < 4; ++j) {
          const f32x2 y = (d[i][4 * s + j] * rs[i] + dm) * c.sc1[4 * s + j] + c.sh[4 * s + j];
          o[j] = pk2(y[0], y[1]);
        }
        o[3] = min(o[3], POISON - 1);
        st16(c, ob + s * KBS + i * 1024, o);
      }
    }
  }
  GAP();
  STAMP();
  if constexpr (!noflag(3)) { drain_vm(); set_flag(c, flag_off(tm, wave, tn, PH_G2), 1); }
  STAMP();
}

DI void phase3(Ctx& c) {
  using namespace en;
  int tid = opaque_tid(), lane = tid & 63; const int b = opaque_bid(), wave = c.wave;
  int tm, tn, tm4, tn4; map_g3(b, tm, tn); map_g2(b, tm4, tn4);
  if constexpr (noflag(3)) {
    static_assert(!noflag(3) || (MAPV != 0 && PL.d1[3] - PL.d0[3] <= NFREE3 && NFREE3 <= 16), "readers know the writers' poison; every piece is older than the stores");
    bar();
    rd0<8 * T3>(c, make_iseq<4 * RD>{});
    setup3(c);
    __builtin_amdgcn_s_sleep(DLY3);
    STAMP(); STAMP();
    start_nf<3>(c, W_H2 + (tm * 8 + 2 * wave) * 1024);
  } else {
    bar();
    dmas<3, (NFREE3 < 16 ? NFREE3 : 16), 64>(c);
    setup3(c);
    STAMP();
    await<PH_G2>(c, flag_off(tm, wave, lane & 15, 0));
    STAMP();
    start<3>(c, W_H2 + (tm * 8 + 2 * wave) * 1024);
  }
  STAMP();
  stages<32, 40>(c);
  STAMP();
  stages<40, 48>(c);
  STAMP();
  stages<48, 56>(c);
  STAMP();
  stages<56, 64>(c);
  STAMP();
  stages<64, 72>(c);
  STAMP();
  stages<72, 80>(c);
  STAMP();
  stages<80, 88>(c);
  STAMP();
  stages<88, 96>(c);
  STAMP();
  stages<96, 104>(c);
  STAMP();
  stages<104, 110>(c);
  STAMP();
  stages<110, NT - 1>(c);
  tid = opaque_tid(); lane = tid & 63;
  const int g = lane >> 4;
  {
    const unsigned pb = c.lds0 + L_WV + wave * 1024 + 384 + g * 8;
#pragma unroll
    for (int i = 0; i < 4; ++i) {
      const u32x2 gv = lds_ld<u32x2>(pb + i * 32);
      c.eg[i >> 1][2 * (i & 1)] = unpk(gv[0]); c.eg[i >> 1][2 * (i & 1) + 1] = unpk(gv[1]);
#pragma unroll
      for (int e = 0; e < 4; ++e) { c.er[i >> 1][i & 1][e] = unpk(c.xr[i >> 1][i & 1][e]); pin(c.er[i >> 1][i & 1][e]); }
    }
#pragma unroll
    for (int i = 0; i < 8; ++i) pin(c.eg[i >> 2][i & 3]);
  }
  unsigned oo = ((tm4 * 128 + wave * 32 + (lane & 7)) * D_ + tn4 * 64 + (lane & 8) * 4 + (g & 1) * 16 + (g >> 1) * 8) * 2;
  pin(oo);
  GAP();
  stages<NT - 1, NT>(c);
  mfma_fence();
  STAMP();
  wait_vm<0>();
  pin(c.tch);
#pragma unroll
  for (int u = 0; u < 8; ++u) pin(c.xl[u]);
  const rsrc_t ro = mk_rsrc(c.out, S_ * D_ * 2);
#pragma unroll
  for (int rb = 0; rb < 2; ++rb) {
    u32x4 o[2];
#pragma unroll
    for (int s = 0; s < 2; ++s)
      o[s] = swz_rows(gated_resid(c.acc[U_P][s][rb][0] + __builtin_bit_cast(f32x4, c.xl[4 * s + 2 * rb]), c.acc[U_P][s][rb][1] + __builtin_bit_cast(f32x4, c.xl[4 * s + 2 * rb + 1]), c.eg[s], c.er[rb][s]));
    u32x4 w[2];
#pragma unroll
    for (int e = 0; e < 4; ++e) {
      w[0][e] = __builtin_amdgcn_update_dpp(o[0][e], o[1][e], 0x128, 0xf, 0xc, false);
      w[1][e] = __builtin_amdgcn_update_dpp(o[1][e], o[0][e], 0x128, 0xf, 0x3, false);
    }
#pragma unroll
    for (int s = 0; s < 2; ++s) __builtin_amdgcn_raw_buffer_store_b128(w[s], ro, oo + (rb * 16 + s * 8) * D_ * 2, 0, OUTA);
  }
  STAMP();
}

__global__ __launch_bounds__(256, 1) __attribute__((aligned(4096))) void block_kernel(const u16* x, const u16* wqkv, const u16* wo, const u16* w1, const u16* w3, const u16* w2,
                                                       const u16* cc, const u16* wmod, u16* out, char* ws) {
  extern __shared__ __attribute__((aligned(16))) char smem[];
  asm volatile("" ::: ACLOB);
  Ctx c;
  c.x = x; c.wqkv = wqkv; c.wo = wo; c.w1 = w1; c.w3 = w3; c.w2 = w2; c.cc = cc; c.wmod = wmod; c.out = out; c.ws = ws;
  c.lds0 = (unsigned)(uintptr_t)(LDS_PTR(char))smem;
  asm volatile("s_getpc_b64 %0\n.Lblk_begin:" : "=s"(c.pc0));
  c.wave = __builtin_amdgcn_readfirstlane(threadIdx.x >> 6);
  STAMP0();
  phase1(c);
  phase2(c);
  phase3(c);
  asm volatile(".Lblk_end:");
}

extern "C" void kernel_launch(void* const* d_in, const int* in_sizes, int n_in, void* d_out, int out_size, void* d_ws,
                              size_t ws_size, hipStream_t stream) {
  (void)hipFuncSetAttribute((const void*)block_kernel, hipFuncAttributeMaxDynamicSharedMemorySize, LDS_TOTAL);
  block_kernel<<<256, 256, LDS_TOTAL, stream>>>((const u16*)d_in[0], (const u16*)d_in[1], (const u16*)d_in[2], (const u16*)d_in[4],
                                                (const u16*)d_in[5], (const u16*)d_in[6], (const u16*)d_in[7], (const u16*)d_in[8], (u16*)d_out, (char*)d_ws);
}
```

```cpp
#include <hip/hip_runtime.h>
#include <hip/hip_bf16.h>
#include <stdint.h>
#include <utility>

typedef unsigned short u16;
typedef short bf16x8 __attribute__((ext_vector_type(8)));
typedef short bf16x4 __attribute__((ext_vector_type(4)));
typedef float f32x4 __attribute__((ext_vector_type(4)));
typedef float f32x2 __attribute__((ext_vector_type(2)));
typedef unsigned int u32x4 __attribute__((ext_vector_type(4)));
typedef unsigned int u32x2 __attribute__((ext_vector_type(2)));
typedef __amdgpu_buffer_rsrc_t rsrc_t;
#define LDS_PTR(T) __attribute__((address_space(3))) T*
#define DI __device__ __forceinline__
#define GAP() __builtin_amdgcn_sched_barrier(0)
template <int... Is> using iseq = std::integer_sequence<int, Is...>;
template <int N> using make_iseq = std::make_integer_sequence<int, N>;

constexpr int S_ = 2048, D_ = 1024, F_ = 3584;
constexpr int KBS = (S_ / 16) * 1024;

DI float bf2f(u16 v) { return __uint_as_float(((unsigned)v) << 16); }
typedef __bf16 bf16x2_t __attribute__((ext_vector_type(2)));
DI unsigned pk2(float lo, float hi) { const f32x2 v = {lo, hi}; return __builtin_bit_cast(unsigned, __builtin_convertvector(v, bf16x2_t)); }
DI float rbf(float v) { return __uint_as_float(pk2(0.f, v)); }
DI float lo2f(unsigned u) { return __uint_as_float(u << 16); }
DI float hi2f(unsigned u) { return __uint_as_float(u & 0xffff0000u); }
template <int N> DI void wait_vm() { asm volatile("s_waitcnt vmcnt(%0)" ::"n"(N) : "memory"); }
template <int N> DI void wait_lgkm() { __builtin_amdgcn_s_waitcnt(0xC07F | (N << 8)); asm volatile("" ::: "memory"); }
DI void bar() { asm volatile("" ::: "memory"); __builtin_amdgcn_s_barrier(); asm volatile("" ::: "memory"); }
template <int IMM> DI void m0_add(unsigned base) { asm volatile("s_add_u32 m0, %0, %1" ::"s"(base), "n"(IMM) : "scc", "memory"); }
template <int N> DI void nops() { asm volatile("s_nop %0" ::"n"(N - 1)); }
template <class T> DI void pin(T& v) { asm volatile("" : "+v"(v)); }
template <class T> DI void pins(T& v) { asm volatile("" : "+s"(v)); }
DI unsigned sel(bool c, unsigned a, unsigned b) { pin(a); pin(b); return c ? a : b; }
constexpr unsigned OOB = 0x7fffffff;
DI rsrc_t mk_rsrc(const void* p, unsigned bytes) { return __builtin_amdgcn_make_buffer_rsrc((void*)p, 0, bytes, 0x00020000); }
template <int OFF> DI void bdma(unsigned voff, const rsrc_t& rs, unsigned soff) {
  asm volatile("buffer_load_dwordx4 %0, %1, %2 offen offset:%3 lds" ::"v"(voff), "s"(rs), "s"(soff), "n"(OFF) : "memory");
}
#define ACLOB "a0","a1","a2","a3","a4","a5","a6","a7","a8","a9","a10","a11","a12","a13","a14","a15","a16","a17","a18","a19","a20","a21","a22","a23","a24","a25","a26","a27","a28","a29","a30","a31","a32","a33","a34","a35","a36","a37","a38","a39","a40","a41","a42","a43","a44","a45","a46","a47","a48","a49","a50","a51","a52","a53","a54","a55","a56","a57","a58","a59","a60","a61","a62","a63","a64","a65","a66","a67","a68","a69","a70","a71","a72","a73","a74","a75","a76","a77","a78","a79","a80","a81","a82","a83","a84","a85","a86","a87","a88","a89","a90","a91","a92","a93","a94","a95","a96","a97","a98","a99","a100","a101","a102","a103","a104","a105","a106","a107","a108","a109","a110","a111","a112","a113","a114","a115","a116","a117","a118","a119","a120","a121","a122","a123","a124","a125","a126","a127","a128","a129","a130","a131","a132","a133","a134","a135","a136","a137","a138","a139","a140","a141","a142","a143","a144","a145","a146","a147","a148","a149","a150","a151","a152","a153","a154","a155","a156","a157","a158","a159","a160","a161","a162","a163","a164","a165","a166","a167","a168","a169","a170","a171","a172","a173","a174","a175","a176","a177","a178","a179","a180","a181","a182","a183","a184","a185","a186","a187","a188","a189","a190","a191","a192","a193","a194","a195","a196","a197","a198","a199","a200","a201","a202","a203","a204","a205","a206","a207","a208","a209","a210","a211","a212","a213","a214","a215","a216","a217","a218","a219","a220","a221","a222","a223","a224","a225","a226","a227","a228","a229","a230","a231","a232","a233","a234","a235","a236","a237","a238","a239","a240","a241","a242","a243","a244","a245","a246","a247","a248","a249","a250","a251","a252","a253","a254","a255"
template <int R> DI void ld_act(unsigned voff, const rsrc_t& rs, unsigned soff) {
  asm volatile("buffer_load_dwordx4 a[%c3:%c4], %0, %1, %2 offen offset:%5 sc1" ::"v"(voff), "s"(rs), "s"(soff), "i"(4 * R), "i"(4 * R + 3), "n"((R & 1) * 1024) : "memory");
}
template <int R, bool SW, bool FIRST> DI void mfa(f32x4& c, bf16x8 w) {
  if constexpr (FIRST && SW) asm volatile("v_mfma_f32_16x16x32_bf16 %0, a[%c2:%c3], %1, 0" : "=&v"(c) : "v"(w), "i"(4 * R), "i"(4 * R + 3));
  else if constexpr (FIRST) asm volatile("v_mfma_f32_16x16x32_bf16 %0, %1, a[%c2:%c3], 0" : "=&v"(c) : "v"(w), "i"(4 * R), "i"(4 * R + 3));
  else if constexpr (SW) asm volatile("v_mfma_f32_16x16x32_bf16 %0, a[%c2:%c3], %1, %0" : "+v"(c) : "v"(w), "i"(4 * R), "i"(4 * R + 3));
  else asm volatile("v_mfma_f32_16x16x32_bf16 %0, %1, a[%c2:%c3], %0" : "+v"(c) : "v"(w), "i"(4 * R), "i"(4 * R + 3));
}
DI void mfv(f32x4& d, const u32x4& a, const u32x4& b) { asm volatile("v_mfma_f32_16x16x32_bf16 %0, %1, %2, 0" : "=&v"(d) : "v"(a), "v"(b)); }
DI void mfma_fence() { GAP(); asm volatile("s_nop 7\n\ts_nop 1" ::: "memory"); GAP(); }
DI bf16x4 ldsT(unsigned addr) { return __builtin_amdgcn_ds_read_tr16_b64_v4i16((LDS_PTR(bf16x4))(uintptr_t)addr); }
DI bf16x8 cat(bf16x4 lo, bf16x4 hi) { return __builtin_shufflevector(lo, hi, 0, 1, 2, 3, 4, 5, 6, 7); }
DI u32x4 pack8(const f32x4& a, const f32x4& b) { return (u32x4){pk2(a[0], a[1]), pk2(a[2], a[3]), pk2(b[0], b[1]), pk2(b[2], b[3])}; }
DI void v_mul(float& d, float a, float b) { asm volatile("v_mul_f32 %0, %1, %2" : "=v"(d) : "v"(a), "v"(b)); }
DI void v_mulc(float& d, float a) { asm volatile("v_mul_f32 %0, 0xbfb8aa3b, %1" : "=v"(d) : "v"(a)); }
DI void v_exp(float& d, float a) { asm volatile("v_exp_f32 %0, %1" : "=v"(d) : "v"(a)); }
DI void v_fmac(float& d) { asm volatile("v_fma_f32 %0, %0, %1, %1" : "+v"(d) : "s"(-1.4426950408889634f)); }
DI void v_rcp(float& d) { asm volatile("v_rcp_f32 %0, %0" : "+v"(d)); }
DI unsigned v_cvt(float a, float b) { unsigned d; asm volatile("v_cvt_pk_bf16_f32 %0, %1, %2" : "=v"(d) : "v"(a), "v"(b)); return d; }

DI float fmax1(float a, float b) { return __builtin_amdgcn_fmed3f(a, b, __builtin_inff()); }
DI float xadd16(float v) { auto r = __builtin_amdgcn_permlane16_swap(__float_as_uint(v), __float_as_uint(v), false, false); return __uint_as_float(r[0]) + __uint_as_float(r[1]); }
DI float xadd32(float v) { auto r = __builtin_amdgcn_permlane32_swap(__float_as_uint(v), __float_as_uint(v), false, false); return __uint_as_float(r[0]) + __uint_as_float(r[1]); }
DI float add2(float u, float v) {
  auto r = __builtin_amdgcn_permlane16_swap(__float_as_uint(u), __float_as_uint(v), false, false);
  return xadd32(__uint_as_float(r[0]) + __uint_as_float(r[1]));
}
DI f32x2 spread2(float t) {
  auto r = __builtin_amdgcn_permlane16_swap(__float_as_uint(t), __float_as_uint(t), false, false);
  return (f32x2){__uint_as_float(r[0]), __uint_as_float(r[1])};
}
template <int CTRL> DI float dpp_add(float v) { return v + __uint_as_float(__builtin_amdgcn_update_dpp(0, __float_as_uint(v), CTRL, 0xf, 0xf, true)); }
DI f32x2 red8(const f32x2 (&a)[4]) {
  f32x2 o;
#pragma unroll
  for (int e = 0; e < 2; ++e) {
    auto r0 = __builtin_amdgcn_permlane16_swap(__float_as_uint(a[0][e]), __float_as_uint(a[1][e]), false, false);
    auto r1 = __builtin_amdgcn_permlane16_swap(__float_as_uint(a[2][e]), __float_as_uint(a[3][e]), false, false);
    const float u = __uint_as_float(r0[0]) + __uint_as_float(r0[1]), v = __uint_as_float(r1[0]) + __uint_as_float(r1[1]);
    auto r2 = __builtin_amdgcn_permlane32_swap(__float_as_uint(u), __float_as_uint(v), false, false);
    o[e] = dpp_add<0x128>(__uint_as_float(r2[0]) + __uint_as_float(r2[1]));
  }
  return o;
}
DI float row16_sum(float v) {
  v = dpp_add<0xB1>(v);
  v = dpp_add<0x4E>(v);
  v = dpp_add<0x141>(v);
  v = dpp_add<0x140>(v);
  return v;
}
DI f32x2 unpk(unsigned u) { return (f32x2){lo2f(u), hi2f(u)}; }
DI f32x2 rbf2(f32x2 v) { return (f32x2){rbf(v[0]), rbf(v[1])}; }
DI float ln_rstd(float var) { return __builtin_amdgcn_rcpf(rbf(__builtin_amdgcn_sqrtf(rbf(rbf(var) + 1e-5f)))); }
DI float silu_bf(u16 c) { const float x = bf2f(c); return rbf(x * __builtin_amdgcn_rcpf(1.f + __builtin_amdgcn_exp2f(x * -1.4426950408889634f))); }
DI unsigned umax4(const u32x4& v) { return max(max(v[0], v[1]), max(v[2], v[3])); }
DI unsigned umax16(const u32x4 (&v)[4]) { return max(max(umax4(v[0]), umax4(v[1])), max(umax4(v[2]), umax4(v[3]))); }
template <class T> DI T lds_ld(unsigned a) { return *(const LDS_PTR(T))(uintptr_t)a; }
template <class T> DI void lds_st(unsigned a, const T& v) { *(LDS_PTR(T))(uintptr_t)a = v; }
DI u32x4 swz_rows(const u32x4& o) {
  auto r0 = __builtin_amdgcn_permlane16_swap(o[0], o[2], false, false);
  auto r1 = __builtin_amdgcn_permlane16_swap(o[1], o[3], false, false);
  return (u32x4){r0[0], r1[0], r0[1], r1[1]};
}

constexpr unsigned W_H1 = 1u << 16, W_O = W_H1 + (4u << 20), W_X1 = W_O + (4u << 20), W_H2 = W_X1 + (4u << 20), W_HH = W_H2 + (4u << 20);
constexpr unsigned W_PART = 32u << 20, W_LATE = W_PART + (128u << 10), W_LN1 = W_LATE + (128u << 10), W_FLAGS = W_LN1 + (32u << 10), W_STATS = W_FLAGS + (128u << 10), W_HALO = W_STATS + (256u << 10);
constexpr unsigned W_XCH = 34u << 20, W_END = 42u << 20;
enum { PH_LN1 = 0, PH_G1 = 2, PH_G2, PH_G3, PH_G4 };
constexpr unsigned POISON = 0xffffffffu;
constexpr int AUX_SC1 = 16;
constexpr unsigned SPIN_CAP = 1u << 17;
constexpr int SLOT = 16384, L_KV = 8 * SLOT, L_WV = L_KV + 20480, L_RED = L_WV + 4096, LDS_TOTAL = 163840;

#define STAMP()
#define STAMP0()
#define FST()
DI int opaque_tid() { int t = threadIdx.x; asm volatile("" : "+v"(t)); return t; }
DI int opaque_bid() { int b = blockIdx.x; asm volatile("" : "+s"(b)); return b; }

DI int panel(int b) { return ((b >> 2) & 1) * 8 + (b >> 5); }
DI void map_g1(int b, int& tm, int& tn) { tm = panel(b); tn = (b & 3) * 4 + (((b >> 3) + 3) & 3); }
DI void map_g2(int b, int& tm, int& tn) { tm = panel(b); tn = (b & 2) * 4 + ((b >> 3) & 3) * 2 + (b & 1); }
DI void map_g3(int b, int& tm, int& tn) { tm = panel(b); tn = (b & 3) * 4 + ((b >> 3) & 3); }

struct Ctx {
  const u16 *x, *wqkv, *wo, *w1, *w3, *w2, *cc, *wmod; u16* out; char* ws;
  rsrc_t rs, rC, rQ, rO, r1, r3, r2, rM;
  f32x4 acc[13][2][2][2];
  bf16x4 wl[8][2], wh[8][2];
  float t[8]; u32x4 o;
  unsigned bW[2][2], bM[2][2], vA, vK[4], v1[4], v2[4], v3[4], vM[4];
  unsigned lds0, sBB[2], sAA[2], sA0, mB, so[2], sB4[2][2], sA4[2], fl3, fl4, xs4;
  int wave; unsigned long long pc0;
  u32x4 kf[2][2], vf[2][2], hl[4]; unsigned kvw, hso, hst;
  u32x4 xr[2][2], lw[8]; float ls[8]; f32x2 ga[4];
  u32x4 mp[2][4]; u32x2 mv[12]; unsigned mo, tag[2]; f32x2 eg[2][4], er[2][2][4], sc1[8], sh[8];
  unsigned ck, cw[2], zo, pollv, pollo, flv, tch, vT1, vT2, vT3, vT4, vT5, vC, vG0; u32x4 xl[8];
};
DI void st16(const Ctx& c, unsigned off, const u32x4& v) { __builtin_amdgcn_raw_buffer_store_b128(v, c.rs, off, 0, AUX_SC1); }
DI void ld16x4(const Ctx& c, u32x4 (&v)[4], unsigned off, unsigned step) {
  asm volatile("buffer_load_dwordx4 %0, %4, %5, 0 offen sc1\n\tbuffer_load_dwordx4 %1, %4, %5, %6 offen sc1\n\tbuffer_load_dwordx4 %2, %4, %5, %7 offen sc1\n\tbuffer_load_dwordx4 %3, %4, %5, %8 offen sc1\n\ts_waitcnt vmcnt(0)"
               : "=&v"(v[0]), "=&v"(v[1]), "=&v"(v[2]), "=&v"(v[3]) : "v"(off), "s"(c.rs), "s"(step), "s"(2 * step), "s"(3 * step) : "memory");
}
DI void retry(const Ctx& c, u32x4 (&v)[4], unsigned off, unsigned step) {
  if (__builtin_expect(__any(umax16(v) == POISON), 0)) {
#pragma unroll 1
    for (unsigned spins = 0; spins < SPIN_CAP; ++spins) { ld16x4(c, v, off, step); if (!__any(umax16(v) == POISON)) break; __builtin_amdgcn_s_sleep(1); }
  }
}
DI u32x4 granule(const Ctx& c, float a, float b) { return (u32x4){__float_as_uint(a), c.tag[0], __float_as_uint(b), c.tag[1]}; }
DI bool tagged(const Ctx& c, const u32x4 (&v)[4]) {
  const unsigned d = ((v[0][1] ^ c.tag[0]) | (v[1][1] ^ c.tag[0]) | (v[2][1] ^ c.tag[0]) | (v[3][1] ^ c.tag[0])) | ((v[0][3] ^ c.tag[1]) | (v[1][3] ^ c.tag[1]) | (v[2][3] ^ c.tag[1]) | (v[3][3] ^ c.tag[1]));
  return __all(d == 0);
}
DI unsigned flag_off(int tm, int wave, int col, int ph) { return W_FLAGS + ((tm * 4 + wave) * 16 + col) * 128 + ph * 4; }
DI void set_flag(const Ctx& c, unsigned off, unsigned val) {
  asm volatile("s_mov_b64 exec, 1\n\tbuffer_store_dword %0, off, %1, %2 sc1\n\ts_mov_b64 exec, -1" ::"v"(val), "s"(c.rs), "s"(off) : "memory");
}
DI void spin(const Ctx& c, unsigned voff, unsigned need) {
#pragma unroll 1
  for (unsigned spins = 0; spins < SPIN_CAP; ++spins) {
    if (__all(__builtin_amdgcn_raw_buffer_load_b32(c.rs, voff, 0, AUX_SC1) >= need)) break;
    __builtin_amdgcn_s_sleep(1);
    asm volatile("" ::: "memory");
  }
  asm volatile("" ::: "memory");
}
template <int PH> DI void await(const Ctx& c, unsigned line) {
#pragma unroll 1
  for (unsigned spins = 0; spins < SPIN_CAP; ++spins) {
    const u32x4 v = __builtin_amdgcn_raw_buffer_load_b128(c.rs, line, 0, AUX_SC1);
    if (__all(v[0] == c.tag[0] && v[1] == c.tag[1] && (PH == PH_LN1 || v[PH] != 0))) break;
    __builtin_amdgcn_s_sleep(1);
    asm volatile("" ::: "memory");
  }
  asm volatile("" ::: "memory");
}
#define L4 7
#define NP3 7
#define PRE3 4
#define QW 16
#define HP4 11
#define LG 5
#define DENSE 20
#define PAIR 16
#define UP0 73
#define UP1 93
#define NFD 12
#define NF2 4
#define XD1 12
#define XD2 8
#define XD3 10
namespace en {
constexpr int RD = 4, NT = 116, NH = 8 * NT, T3 = 32, T4 = 88, NB4 = 14 - L4;
constexpr int seg_t0(int s) { return s == 1 ? 0 : s == 2 ? 24 : s == 3 ? T3 : NT; }
constexpr int seg_of(int H) { return H < 192 ? 1 : H < 256 ? 2 : 3; }
constexpr int seg_end(int H) { return 8 * seg_t0(seg_of(H) + 1); }
constexpr int seg_np(int s) { return s == 1 ? 8 : s == 2 ? 8 : NP3; }
constexpr int seg_pre(int s) { return s == 3 ? PRE3 : 8; }
enum { U_K, U_V, U_Q, U_O, U_G0, U_U0, U_G1, U_U1, U_M, U_G2, U_U2, U_S, U_P };
constexpr int unit(int T) {
  if (T >= T4) { const int s = T - T4; return U_S + (s < 2 * NB4 ? s & 1 : s >= 2 * NB4 + L4); }
  return T < 32 ? T >> 3 : T < 64 ? U_G0 + ((T - 32) >> 4) * 2 + (T & 1) : T < 72 ? U_M : T - 72 < LG || (T - 72 < 16 - LG && ((T - 72 - LG) & 1)) ? U_G2 : U_U2;
}
constexpr int grp(int T) {
  if (T >= T4) { const int s = T - T4; return s < 2 * NB4 ? s / 2 : s < 2 * NB4 + L4 ? s - NB4 : s - NB4 - L4; }
  return T < 32 ? T & 7 : T < 64 ? (T >> 1) & 7 : T < 72 ? T & 7 : T - 72 < LG ? T - 72 : T - 72 >= 16 - LG ? T - 80 : (T - 72 - LG) / 2 + (unit(T) == U_G2 ? LG : 0);
}
constexpr int u2stage(int j) { int T = 72; while (unit(T) != U_U2 || grp(T) != j) ++T; return T; }
constexpr int g4first(int j) { return T4 + (j < NB4 ? 2 * j : j + NB4); }
constexpr int g4last(int j) { return T4 + (j < NB4 ? 2 * j + 1 : j + NB4 + L4); }
constexpr int g4kb(int j) { return j < 8 ? 32 * (j / 4) + 4 * (j % 4) : j < 10 ? 96 + 4 * (j - 8) : 64 + 4 * (j - 10); }
constexpr bool g4mix(int j) { return j == 8 || j == 9; }
constexpr bool barst(int T) { return T < NT - 1 && ((T & 1) || (T >= 6 && T < 24) || (T >= 46 && T < 76) || (T > 103 && T < 110)); }
constexpr bool isbar(int H) { return (H & 7) == 8 - RD && barst(H >> 3); }
constexpr int cert(int T) { int b = T - 1; while (!barst(b)) --b; return 8 * b + 8 - RD; }
enum { K_A = 1, K_D, K_A4, K_P, K_XS, K_XL, K_T, K_C, K_G, K_Z };
enum { A_CHK1 = 1, A_CHK2, A_CHK3, A_CHK4, A_FLAG, A_FLAG4, A_CHKX, A_CK = 16 };
enum { P_K, P_GA, P_V, P_GB, P_GC, P_PREV, P_GD, P_GR, P_GO, P_UA, P_UB, PR_HH };
constexpr int P_FA = PR_HH + 28, NPR = P_FA + 1, HH_ST = 13;
constexpr bool pr_up(int i) { return i - PR_HH < 20 ? (i - PR_HH) & 1 : i - PR_HH >= 24; }
constexpr int pr_blk(int i) { return i - PR_HH < 20 ? (i - PR_HH) / 2 : 10 + (i - PR_HH) % 4; }
constexpr int pr_avail(int i) {
  const int a[PR_HH] = {64, 106, 128, 0, 146, 165, 0, 0, 187, 192, 0}, n = i - PR_HH;
  if (i == P_FA) return NH - 56;
  return n < 0 ? a[i] : 8 * T3 + (n < 8 ? 128 : n < 16 ? 256 : n < 20 ? 320 : n < 24 ? 448 - 8 * LG : 448) - (n < 16 && !pr_up(i) ? 8 : 0);
}
constexpr int pr_len(int i) { return i == P_FA ? 56 : i == P_K || i == P_V ? 32 : i == P_PREV || i == P_GR || i == P_GO ? 1 : i == P_UA ? 64 : i == P_UB ? 48 : i < PR_HH ? 32 : pr_up(i) ? HH_ST + 1 : 40; }
constexpr bool noflag(int s) { return s == 3; }
constexpr int E_CK = 8192;
enum { S_M0 = 1, S_SB, S_SA, S_SA4 };
struct Plan { short sc[4 * NH], nr[4 * NH], op[NH + 1], e[4 * NH], act[NH + 1], vw[NH + 1], d0[5], d1[5], slack, halo; };
constexpr Plan mk() {
  Plan p{};
  int atA[64] = {}, atD[4 * NT] = {}, atA4[112] = {}, req[NH + 1] = {}, nvm[NH + 1] = {};
  bool quiet[NH + 64] = {};
  for (int H = 0; H <= NH; ++H) req[H] = -1000;
  auto need = [&](int H, int x) { if (x > req[H]) req[H] = x; };
  auto window = [&](int H, int o0, int o1 = 0, int o2 = 0, int o3 = 0, int o4 = 0, int o5 = 0, int o6 = 0) {
    const int o[7] = {o0, o1, o2, o3, o4, o5, o6};
    for (int i = 0; i < 7 && o[i]; ++i) p.op[H++] = o[i];
    for (int i = 0; i < QW && seg_of(H + i) == seg_of(H - 1); ++i) quiet[H + i] = true;
  };
  auto poll = [&](int H, int a) { const int l = a == A_CHK4 ? 16 : 24; p.op[H - l] = K_P * 1024; p.act[H] = a; need(H, H - l); };
  constexpr int T = K_T * 1024, G = K_G * 1024, C = K_C * 1024, H3 = 8 * T3, H4 = 8 * T4;
  window(84, G, G + 1, G + 2, G + 3); window(120, G + 4, G + 5, G + 6, G + 7); window(153, C, T, T + 1, T + 2); window(176, C + 1, T + 3, T + 4);
  need(108, 87); need(148, 123);
  window(H3 + 146, C, T + 7, T + 8, T + 9); window(H3 + 267, T + 5, T + 6); window(H3 + 308, C, T + 10, T + 11, T + 12);
  p.op[58] = T + 23; p.op[210] = T + 17; p.op[211] = T + 18; p.op[H3 + 67] = T + 19; p.op[H3 + 75] = T + 20;
  p.op[H3 + 366] = T + 13; p.op[H3 + 430] = T + 14; p.op[H3 + 496] = T + 16; p.op[H3 + 460] = C; p.op[H3 + 506] = T + 15; p.op[H3 + 546] = T + 16; p.op[H3 + 548] = T + 15;
  for (int i = 0; i < 4; ++i) p.op[98 + i] = K_Z * 1024 + i;
  const int chk[4] = {H3 + 360, H3 + 438, H4 + 42, H4 + 8 * HP4 + 16}, hx0 = 8 * g4first(13) + 10;
  for (int i = 0; i < 4; ++i) poll(chk[i], A_CHK1 + i);
  int nd = 0, n4 = 0, nxs = 0, nxl = 0, hchk = 100000;
  p.slack = 1000;
  for (int s = 1; s <= 3; ++s) {
    const int T0 = seg_t0(s), T1 = seg_t0(s + 1), dbl = s == 3;
    int na = 2 * seg_pre(s), lim = T0 + 7;
    p.d0[s] = nd; if (nd < 4 * (T0 + seg_np(s))) nd = 4 * (T0 + seg_np(s)); p.d1[s] = nd;
    for (int n = 0; n < na; ++n) atA[n] = 8 * T0 - na + n;
    auto useA = [&](int n) { return 8 * T0 + (n >> 3) * (8 << dbl) + (n & 6) - (!noflag(s) ? 0 : n < 8 ? 2 : n < 2 * 8 ? 8 : 16 - (n & 6)); };
    for (int H = 8 * T0; H < 8 * T1; ++H) {
      if (isbar(H)) lim = (H >> 3) + 7;
      if (p.op[H] || quiet[H]) continue;
      if (H >= hx0 && nxs < 8) {
        p.op[H] = K_XS * 1024 + nxs++;
        if (nxs == 8) {
          const int hf = (H + XD1 + 1) & ~1; hchk = (hf + XD2 + XD3 + 1) & ~1;
          p.act[hf] = A_FLAG4; need(hf, H); p.op[hf + XD2] = K_P * 1024 + 1; p.act[hchk] = A_CHKX; need(hchk, hf + XD2);
        }
        continue;
      }
      if (H >= hchk && nxl < 8) { p.op[H] = K_XL * 1024 + nxl++; continue; }
      if (s == 3 && (H & 7) == 3 && H - 8 * T0 > 8 && H < H4 - 8 * LG - 40) continue;
      const int Td = nd >> 2, j = n4 >> 3;
      const bool dok = Td < NT && Td <= lim && Td < T1 + 8;
      const int dd = !dok ? 100000 : Td < T1 ? cert(Td) : 8 * T1 + 2 * (Td - T1);
      int da = 100000;
      if (na < 64) da = useA(na);
      else if (n4 < 112 && H >= 8 * (j < 8 ? u2stage(j) : g4last(j - 8)) + (n4 & 6) + 2 && H >= chk[j < 8 ? j >> 2 : j < 10 ? 2 : 3]) da = cert(g4first(j));
      if (da <= dd && da < 100000) { if (na < 64) { atA[na] = H; p.op[H] = K_A * 1024 + na++; } else { atA4[n4] = H; p.op[H] = K_A4 * 1024 + n4++; } }
      else if (dok && (H >= 8 * (Td - (barst(Td - 7) && barst(Td - 8) ? 7 : 6)) + 4 + 2 * (nd & 3) || da < 100000)) { atD[nd] = H; p.op[H] = K_D * 1024 + nd++; }
    }
    if (nd < 4 * T1 || na < 64) p.slack = -5;
    for (int n = noflag(s) ? 3 : 1; n < 64; n += 2) need(useA(n), atA[n]);
    if (noflag(s)) { for (int g = 1; g < 8; ++g) p.act[8 * T0 + 16 * g] = A_CK + g; for (int k = 1; k < 4; ++k) p.act[8 * T0 + 2 * k] = A_CK; }
    for (int t = p.d1[s] >> 2; t < T1; ++t) need(cert(t), atD[4 * t + 3]);
  }
  for (int j = 0; j < 14; ++j) need(cert(g4first(j)), atA4[8 * j + 7]);
  if (n4 < 112 || nxl < 8 || nd < 4 * NT) p.slack = -1;
  int used[4 * NH] = {};
  for (int q = 0; q < 4 * NH; ++q) { p.nr[q] = (q >> 2) + RD < seg_end(q >> 2); used[q] = p.nr[q]; }
  for (int H = 0; H < NH; ++H) {
    if (p.op[H]) { nvm[H]++; used[4 * H] = 3; if ((H < 8 * UP0 || H >= 8 * UP1) && seg_of(H) != 2) { p.nr[4 * H + 1] += p.nr[4 * H]; p.nr[4 * H] = 0; used[4 * H + 1] = p.nr[4 * H + 1]; } }
    if ((H & 1) && H + 1 < seg_end(H)) { used[4 * H + 3]++; }
  }
  for (int H = 0; H < NH; ++H) {
    const int k = p.op[H] / 1024, i = p.op[H] % 1024, h0 = 8 * seg_t0(seg_of(H));
    auto put = [&](int code, int back) {
      for (int q = 4 * H - 1; q >= 4 * (H - back) + 1 && q >= 4 * h0; --q) if (used[q] < 2 && !p.sc[q]) { p.sc[q] = code; used[q]++; return; }
      for (int q = 4 * H - 1; q >= 4 * (H - back) + 1 && q >= 4 * h0; --q) if (!p.sc[q]) { p.sc[q] = code; used[q]++; return; }
      p.slack = -8;
    };
    if (k == K_D && !(i & 3)) { put(S_M0 * 1024 + (i >> 2), 1); put(S_SB * 1024 + (i >> 2), 3); }
    if (k == K_A && !(i & 7) && H > h0) put(S_SA * 1024 + (i >> 3), 3);
    if (k == K_A4 && !(i & 7)) put(S_SA4 * 1024 + (i >> 3), 3);
  }
  int pr = 0, ix = 0;
  for (int q = 0; q < 4 * NH; ++q) p.e[q] = -1;
  for (int s = 1; s <= 3; ++s) if (noflag(s)) for (int ks = 1; ks < 32; ++ks) {
    const int w = ks < 8 ? 2 : 1, nq = ks < 8 ? 7 : 15, no = 6 / w;
    const int q0 = 4 * (8 * seg_t0(s) + (ks >> 2) * 16 + 2 * (ks & 3) - (ks < 4 ? 2 : ks < 8 ? 8 : 16 - 2 * (ks & 3)));
    int fr = 0, st = 0;
    for (int q = q0; q < q0 + nq; ++q) fr += used[q] < 2;
    for (int q = q0; q < q0 + nq && st < no; ++q) if (used[q] < 2 || (used[q] == 2 && fr < no - st)) { fr -= used[q] < 2; p.e[q] = E_CK + 8 * ks + w * st++; used[q] = 3; }
    st = st * w / 2;
    if (st < 3) p.slack = -9;
  }
  for (int q = 0; q < 4 * NH; ++q) {
    const int H = q >> 2;
    if (pr == NPR || q < 4 * pr_avail(pr) + 8) continue;
    const int n = pr >= P_FA ? -1 : pr - PR_HH;
    const bool st = n >= 0 && pr_up(pr) && ix == HH_ST, big = st || (n >= 0 && !pr_up(pr) && ((ix >> 3) & 1)), dense = n >= DENSE;
    if (st && (q & 7) == 7) continue;
    if (big && (n < PAIR || st) && p.nr[q]) {
      if (used[q] == 1 && (q & 3) < 3 && used[q + 1] == 1 && p.nr[q + 1] == 1) { p.nr[q] = 0; p.nr[q + 1] = 2; used[q + 1] = 2; }
      else if (used[q] >= 2) continue;
    } else if (used[q] >= 2 && !(dense && used[q] == 2 && p.nr[q] == 1 && !((q & 3) == 3 && isbar(H + 1)))) continue;
    p.e[q] = pr * 64 + ix;
    if (pr == P_PREV) p.halo = H;
    if ((pr == P_GR && H > 182) || (pr == P_GO && H > 191) || (pr == P_V && H > 149) || (pr == P_UB && H > 255)) p.slack = -6;
    if (st) {
      nvm[H]++;
      if (n == 7 || n == 15 || n == 19 || n == 27) {
        int hb = H + (n == 27 ? 22 : 30); while (!isbar(hb) || p.act[hb]) ++hb;
        p.act[hb] = A_FLAG; need(hb, H);
        if (hb + 16 > chk[n == 7 ? 0 : n == 15 ? 1 : n == 19 ? 2 : 3] - (n == 27 ? 16 : 24)) p.slack = -3;
      }
    }
    if (++ix == pr_len(pr)) { ix = 0; ++pr; }
  }
  if (pr < NPR) p.slack = -2;
  for (int H = 0; H < NH; ++H) {
    int n = -1;
    if (req[H] != -1000) {
      const int h0 = 8 * seg_t0(seg_of(H));
      n = req[H] < h0 ? h0 - 1 - req[H] : 0;
      for (int h = req[H] < h0 ? h0 : req[H] + 1; h < H; ++h) n += nvm[h];
      if (req[H] >= h0 && H - req[H] < p.slack) p.slack = H - req[H];
    }
    p.vw[H] = n > 63 ? 63 : n;
  }
  { int n = 0; for (int h = p.halo + 1; h < 192; ++h) n += nvm[h]; p.halo = n; }
  return p;
}
constexpr Plan PL = mk();
static_assert(PL.slack >= 7, "every load is issued well before it is needed");

#define PGTAB 53, 267, 471, 665, 889, 1124, 1310, 1498, 1695, 1893, 2092, 2285, 2474, 2664, 2837, 3024, 3238, 3451, 3656
constexpr int PGS[] = {PGTAB}, NPG = sizeof(PGS) / sizeof(int);
constexpr int pg_of(int q) { for (int i = 0; i < NPG; ++i) if (PGS[i] == q) return i; return -1; }
constexpr int pg_grp(int i) { return i < NPG && PGS[i] >= 0 ? PGS[i] >= 32 * T3 : -1; }
constexpr int pg_first(int g) { int i = 0; while (i < NPG && pg_grp(i) != g) ++i; return i; }
constexpr int pg_lab(int i, int g) { return pg_grp(i) == g ? i : 1000 + g; }
template <int I> DI void pg_mark() { asm volatile(".Lpg%c0:\n\ts_cbranch_execz .Lpg%c1" ::"i"(I), "i"(pg_lab(I + 4, pg_grp(I)))); }
template <int G> DI void pg_hop(int wave) {
  constexpr int f = pg_first(G);
  asm volatile("s_mov_b64 exec, 0\n\ts_cmp_eq_u32 %0, 0\n\ts_cbranch_scc1 .Lpg%c1\n\ts_cmp_eq_u32 %0, 1\n\ts_cbranch_scc1 .Lpg%c2\n\ts_cmp_eq_u32 %0, 2\n\ts_cbranch_scc1 .Lpg%c3\n\ts_branch .Lpg%c4\n"
               ".Lpg%c5:\n\ts_mov_b64 exec, -1" ::"s"(wave), "i"(pg_lab(f, G)), "i"(pg_lab(f + 1, G)), "i"(pg_lab(f + 2, G)), "i"(pg_lab(f + 3, G)), "i"(1000 + G) : "scc", "memory");
}
template <int H, int R> DI void rd(Ctx& c) {
  constexpr bool mx = unit(H >> 3) == U_M;
  constexpr int cb = R >> 1, hi = R & 1, off = ((H >> 3) & 7) * SLOT + ((H >> 1) & 3) * 4096 + (H & 1) * (mx ? 1024 : 256) + hi * 2048;
  const unsigned ad = (mx ? c.bM[cb][off >> 16] : c.bW[cb][off >> 16]) + (off & 65535);
  if constexpr (hi) c.wh[H & 7][cb] = ldsT(ad); else c.wl[H & 7][cb] = ldsT(ad);
}
template <int T> DI void dma_off(Ctx& c) {
  constexpr int u = unit(T), j = grp(T);
  unsigned& sB = c.sBB[T & 1];
  if constexpr (u <= U_Q) sB = j * 128 * 6144 + (u + 1) % 3 * 2048;
  else if constexpr (u == U_O) sB = c.sB4[0][0] + j * 128 * 2048;
  else if constexpr (u < U_S) sB = j * 128 * F_ * 2 + (u == U_M ? 0 : u >= U_G2 ? 4096 : u >= U_G1 ? 2048 : 0);
  else sB = c.sB4[g4mix(j)][u - U_S] + g4kb(j) * 32 * 2048;
  pins(sB);
}
template <int D> DI void dma(Ctx& c) {
  constexpr int u = unit(D >> 2), i = D & 3;
  if constexpr (u <= U_Q) bdma<i * 1024>(c.v1[i], c.rQ, c.sBB[(D >> 2) & 1]);
  else if constexpr (u == U_O) bdma<i * 1024>(c.v2[i], c.rO, c.sBB[(D >> 2) & 1]);
  else if constexpr (u == U_M) bdma<i * 1024>(c.vM[i], (i & 1) ? c.r3 : c.r1, c.sBB[(D >> 2) & 1]);
  else if constexpr (u < U_S) bdma<i * 1024>(c.v3[i], u == U_U0 || u == U_U1 || u == U_U2 ? c.r3 : c.r1, c.sBB[(D >> 2) & 1]);
  else bdma<i * 1024>(c.v2[i], c.r2, c.sBB[(D >> 2) & 1]);
}
template <int D> DI void dma1(Ctx& c) {
  if constexpr (!(D & 3)) { dma_off<(D >> 2)>(c); m0_add<((D >> 2) & 7) * SLOT>(c.mB); nops<1>(); }
  dma<D>(c);
}
template <int B, int... Ds> DI void dmas_(Ctx& c, iseq<Ds...>) { (dma1<B + Ds>(c), ...); }
template <int S, int A, int B> DI void dmas(Ctx& c) {
  constexpr int n = PL.d1[S] - PL.d0[S], a = A < n ? A : n, b = B < n ? B : n;
  GAP(); dmas_<PL.d0[S] + a>(c, make_iseq<b - a>{}); GAP();
}
template <int S, int B, int E, int... Is> DI void dma_at_(Ctx& c, int n, iseq<Is...>) { ((n == Is && B + Is < E ? dmas<S, B + Is, B + Is + 1>(c) : void()), ...); }
template <int S, int B, int E> DI void dma_at(Ctx& c, int n) { dma_at_<S, B, E>(c, n, make_iseq<16>{}); }
constexpr int NFREE2 = 4 * 31 - PL.d0[2];
constexpr int NFREE3 = 4 * 39 - PL.d0[3];

template <int N, bool UP, int I> DI void e_hh(Ctx& c) {
  constexpr bool mx = N == 8 || N == 9;
  constexpr int n = N < 8 ? N : mx ? N - 8 : N - 2, S = (n >> 1) & 1, B = n & 1, ug = mx ? U_M : N < 8 ? U_G0 + (N >> 2) * 2 : U_G2, e = I & 7, ph = I >> 3;
  f32x4& g = c.acc[ug][S][B][e >> 2];
  float r;
  if constexpr (UP) {
    if constexpr (I < 8) v_mul(c.t[e], g[e & 3], c.acc[mx ? U_M : ug + 1][mx ? 1 : S][B][e >> 2][e & 3]);
    else if constexpr (I < 12) c.o[e] = v_cvt(c.t[2 * e], c.t[2 * e + 1]);
    else if constexpr (I == HH_ST) asm volatile("buffer_store_dwordx4 %0, %1, %2, %3 offen offset:%4 sc1" ::"v"(c.o), "v"(c.vA), "s"(c.rs), "s"(c.so[mx] + (mx ? 96 : (n >> 2) * 32 + S) * KBS), "n"(B * 1024) : "memory");
  } else if constexpr (ph == 0) { v_mulc(r, g[e & 3]); g[e & 3] = r; }
  else if constexpr (ph == 1) v_exp(c.t[e], g[e & 3]);
  else if constexpr (ph == 2) v_fmac(c.t[e]);
  else if constexpr (ph == 3) v_rcp(c.t[e]);
  else { v_mul(r, g[e & 3], c.t[e]); g[e & 3] = r; }
}
template <int U, int F> DI void e_kv(Ctx& c) {
  u32x4 (&d)[2][2] = U ? c.vf : c.kf;
  if constexpr (F < 8 || F >= 24) { constexpr int B = F < 8; const f32x4& a = c.acc[U][(F >> 2) & 1][B][(F >> 1) & 1]; d[(F >> 2) & 1][B][F & 3] = v_cvt(a[2 * (F & 1)], a[2 * (F & 1) + 1]); }
  else if constexpr (F < 16) { unsigned r = d[(F >> 2) & 1][1][F & 3]; asm volatile("v_min_u32 %0, -2, %0" : "+v"(r)); d[(F >> 2) & 1][1][F & 3] = r; }
  else if constexpr (F == 17 || F == 19) asm volatile("buffer_store_dwordx4 %0, %1, %2, %3 offen offset:%4 sc1" ::"v"(d[F == 19][1]), "v"(c.vA), "s"(c.rs), "s"(c.hst), "n"(U * 2048 + (F == 19) * 1024) : "memory");
  else if constexpr (F == 20 || F == 23) lds_st(c.kvw + 4096 + U * 2048 + (F == 23) * 1024, d[F == 23][1]);
}
DI void e_prev(Ctx& c) {
  asm volatile("s_cmp_eq_u32 %4, 0\n\ts_cbranch_scc1 1f\n\tds_read_b128 %0, %5\n\tds_read_b128 %1, %5 offset:1024\n\tds_read_b128 %2, %5 offset:2048\n\tds_read_b128 %3, %5 offset:3072\n\ts_branch 2f\n"
               "1:\n\tbuffer_load_dwordx4 %0, %6, %7, %8 offen sc1\n\tbuffer_load_dwordx4 %1, %6, %7, %8 offen offset:1024 sc1\n\tbuffer_load_dwordx4 %2, %6, %7, %8 offen offset:2048 sc1\n\tbuffer_load_dwordx4 %3, %6, %7, %8 offen offset:3072 sc1\n2:"
               : "=&v"(c.hl[0]), "=&v"(c.hl[1]), "=&v"(c.hl[2]), "=&v"(c.hl[3]) : "s"(c.wave), "v"(c.kvw), "v"(c.vA), "s"(c.rs), "s"(c.hso < W_HALO ? W_END : c.hso) : "scc", "memory");
}
DI void gemv_out(const Ctx& c, unsigned off, int lane) {
  const unsigned rb = c.lds0 + L_RED + (lane & 31) * 8;
  const f32x2 sum = (lds_ld<f32x2>(rb) + lds_ld<f32x2>(rb + 256)) + (lds_ld<f32x2>(rb + 512) + lds_ld<f32x2>(rb + 768));
  if (lane < 32) st16(c, off + lane * 16, granule(c, sum[0], sum[1]));
}
template <int P, int I> DI void e_gv(Ctx& c) {
  if constexpr (P == P_GR) lds_st(c.lds0 + L_RED + c.wave * 256 + ((c.vA >> 4) & 7) * 32 + (c.vA >> 8) * 8, red8(c.ga));
  else if constexpr (P == P_GO) { if (c.wave == 3) gemv_out(c, W_LATE + blockIdx.x * 512, c.vA >> 4); }
  else {
    constexpr int r = 2 * (P == P_GA ? 0 : P == P_GB ? 1 : P == P_GC ? 2 : 3) + (I >> 4), e = I & 7;
    if constexpr (!(I & 8) && (e & 1)) asm volatile("v_and_b32 %0, 0xffff0000, %1" : "=v"(c.t[e]) : "v"(c.lw[r][e >> 1]));
    else if constexpr (!(I & 8)) asm volatile("v_lshlrev_b32 %0, 16, %1" : "=v"(c.t[e]) : "v"(c.lw[r][e >> 1]));
    else {
      float a = r ? c.ga[e >> 1][e & 1] : 0.f;
      if constexpr (r == 0) v_mul(a, c.t[e], c.ls[r]); else asm volatile("v_fmac_f32 %0, %1, %2" : "+v"(a) : "v"(c.t[e]), "v"(c.ls[r]));
      c.ga[e >> 1][e & 1] = a;
    }
  }
}
template <int I> DI void e_un(Ctx& c) {
  auto un = [](unsigned u) { float r; if constexpr (I & 1) asm volatile("v_and_b32 %0, 0xffff0000, %1" : "=v"(r) : "v"(u)); else asm volatile("v_lshlrev_b32 %0, 16, %1" : "=v"(r) : "v"(u)); return r; };
  constexpr int j = (I & 15) >> 1;
  if constexpr (I < 32) c.er[I >> 4][(I >> 3) & 1][(I >> 1) & 3][I & 1] = un(c.xr[I >> 4][(I >> 3) & 1][(I >> 1) & 3]);
  else if constexpr (I < 48) c.eg[j >> 2][j & 3][I & 1] = un(c.mv[j >> 1][j & 1]);
  else if constexpr (I < 64) c.sh[j][I & 1] = un(c.mv[4 + (j >> 1)][j & 1]);
  else if constexpr (I < 80) c.sc1[j][I & 1] = un(c.mv[8 + (j >> 1)][j & 1]);
  else {
    float r = c.sc1[j][I & 1];
    if constexpr (I < 96) asm volatile("v_add_f32 %0, 1.0, %0" : "+v"(r)); else asm volatile("v_cvt_pk_bf16_f32 %0, 0, %0" : "+v"(r));
    c.sc1[j][I & 1] = r;
  }
}
template <int P, int I> DI void e_fin(Ctx& c) {
  if constexpr (P == 0 && I < 4) c.mv[I] = lds_ld<u32x2>(c.lds0 + L_WV + c.wave * 1024 + 384 + I * 32 + (c.vA >> 8) * 8);
  else if constexpr (P == 0 && I >= 8) e_un<I - 8>(c);
  else if constexpr (P == 1) {
    constexpr int rb = I >> 4, s = (I >> 3) & 1, j = (I >> 1) & 3;
    float r = c.er[rb][s][j][I & 1];
    asm volatile("v_fmac_f32 %0, %1, %2" : "+v"(r) : "v"(c.eg[s][j][I & 1]), "v"(c.xl[4 * s + 2 * rb + (j >> 1)][I & 3]));
    c.er[rb][s][j][I & 1] = r;
  }
}
template <int... Is> DI void fin_(Ctx& c, iseq<Is...>) { (e_fin<1, Is>(c), ...); }
DI void touch(Ctx& c, unsigned v, const rsrc_t& r, unsigned o) { asm volatile("buffer_load_dword %0, %1, %2, %3 offen" : "+v"(c.tch) : "v"(v), "s"(r), "s"(o) : "memory"); }
template <int W> DI void warm(Ctx& c) {
  if constexpr (W == 0) touch(c, c.vT2, c.rO, 0);
  else if constexpr (W < 7) touch(c, c.vT3, (W & 1) ? c.r1 : c.r3, (W - 1) / 2 * 2048);
  else if constexpr (W < 9) touch(c, c.vT4, (W & 1) ? c.r1 : c.r3, 0);
  else if constexpr (W < 13) touch(c, c.vT5, c.r2, (W - 9) * (2 << 20));
  else if constexpr (W < 17) touch(c, c.vT2, c.r2, (W - 13) * (2 << 20));
  else if constexpr (W == 23) touch(c, c.vT1, c.rQ, 0);
  else touch(c, c.vT3 | (c.vA < 256 ? 0x40000000u : 0u), (W & 1) ? c.r1 : c.r3, (W - 17) / 2 * 2048);
}
DI unsigned slow_col(const void* base, int first, int n) {
  const unsigned k = (3 - (unsigned)((uintptr_t)base >> 7) - first) & 7;
  return k < n ? (first + k) * 128 : 0x40000000u;
}
DI void warm_code(Ctx& c) {
  unsigned o;
  asm volatile("s_mov_b32 %0, .-.Lblk_begin" : "=s"(o));
  touch(c, c.vC, c.rC, o);
}
template <int KS, int ST> DI void e_ck(Ctx& c) {
  if constexpr (ST % 3 < 2) asm volatile("v_accvgpr_read_b32 %0, a%c1" : "=v"(c.cw[ST % 3]) : "i"(8 * KS + 4 * (ST / 3) + 2 * (ST % 3) + 1));
  else if constexpr ((KS & 3) || ST == 5) asm volatile("v_max3_u32 %0, %0, %1, %2" : "+v"(c.ck) : "v"(c.cw[0]), "v"(c.cw[1]));
  else asm volatile("v_max_u32 %0, %1, %2" : "=v"(c.ck) : "v"(c.cw[0]), "v"(c.cw[1]));
}
template <int G, int... Is> DI void ck_group(Ctx& c, iseq<Is...>) { (e_ck<4 * G + Is / 6, Is % 6>(c), ...); }
template <int G, int... Is> DI void ld_group(Ctx& c, iseq<Is...>) { (ld_act<8 * G + Is>(c.vK[Is >> 1], c.rs, c.sA0 + G * 4 * KBS), ...); }
template <int G> DI void ck_retry(Ctx& c) {
  if (__builtin_expect(__any(c.ck == POISON), 0)) {
#pragma unroll 1
    for (unsigned spins = 0; spins < SPIN_CAP; ++spins) {
      ld_group<G>(c, make_iseq<8>{});
      wait_vm<0>();
      ck_group<G>(c, make_iseq<24>{});
      if (!__any(c.ck == POISON)) break;
    }
  }
}
template <int A> DI void action(Ctx& c) {
  if constexpr (A >= A_CK) ck_retry<A - A_CK>(c);
  else
  if constexpr (A == A_FLAG) { c.flv += 1; set_flag(c, c.fl3, c.flv); }
  else if constexpr (A == A_FLAG4) set_flag(c, c.fl4, c.flv);
  else {
    constexpr unsigned need = A == A_CHKX ? 1 : A;
    pin(c.pollv);
    if (__builtin_expect(!__all(c.pollv >= need), 0)) spin(c, c.pollo + (A == A_CHKX ? (c.fl4 ^ 128) : 0), need);
    if constexpr (A == A_CHK4) { c.pollo = 0; pin(c.pollo); }
  }
}
constexpr int rd_before(int q) { int n = 0; for (int i = q & ~3; i < q; ++i) n += PL.nr[i]; return n; }
template <int H, int YOUNG> DI void wait_for(Ctx& c) {
  constexpr int v = PL.vw[H] < 0 ? 63 : PL.vw[H];
  __builtin_amdgcn_s_waitcnt((v & 15) | ((v >> 4) << 14) | 0x70 | (YOUNG << 8));
  asm volatile("" ::: "memory");
}
template <int H> DI void waits(Ctx& c) {
  static_assert(!(H & 1) || (PL.vw[H] < 0 && !PL.act[H]), "waits sit in front of even half-steps");
  if constexpr (isbar(H)) bar();
  GAP();
  if constexpr (PL.act[H] != 0) { action<PL.act[H]>(c); GAP(); }
}
template <int H, int M> DI void slot(Ctx& c) {
  constexpr int cb = M >> 1, rb = M & 1, u = unit(H >> 3), j = grp(H >> 3), k = PL.op[H] / 1024, i = PL.op[H] % 1024, e = PL.e[4 * H + M];
  mfa<(j & 7) * 8 + (H & 6) + rb, u == U_V, j == 0 && (H & 6) == 0>(c.acc[u][H & 1][rb][cb], cat(c.wl[H & 7][cb], c.wh[H & 7][cb]));
  if constexpr (pg_of(4 * H + M) >= 0) pg_mark<pg_of(4 * H + M)>();
  GAP();
  constexpr int q = 4 * H + M, sk = PL.sc[q] / 1024, si = PL.sc[q] % 1024;
  if constexpr (sk == S_M0) m0_add<(si & 7) * SLOT>(c.mB);
  if constexpr (sk == S_SB) dma_off<si>(c);
  if constexpr (sk == S_SA) { c.sAA[si & 1] = c.sA0 + si * 4 * KBS; pins(c.sAA[si & 1]); }
  if constexpr (sk == S_SA4) { c.sAA[si & 1] = c.sA4[g4mix(si)] + g4kb(si) * KBS; pins(c.sAA[si & 1]); }
  if constexpr ((H & 1) && H + 1 < seg_end(H) && M == 3) wait_for<H + 1, (H + 5 <= seg_end(H) ? 4 + rd_before(q) : 0)>(c);
  if constexpr (M == 3 && H + 1 < seg_end(H)) waits<H + 1>(c);
  GAP();
  if constexpr (PL.nr[q] >= 1) rd<H + RD, rd_before(q)>(c);
  if constexpr (PL.nr[q] == 2) { GAP(); rd<H + RD, rd_before(q) + 1>(c); }
  GAP();
  if constexpr (M == 0) {
    if constexpr (k == K_D) dma<i>(c);
    if constexpr (k == K_T) warm<i>(c);
    if constexpr (k == K_C && i == 0) warm_code(c);
    if constexpr (k == K_C && i == 1) touch(c, c.vA << 20, c.rC, (((blockIdx.x >> 3) & 7) * 4 + c.wave) << 12);
    if constexpr (k == K_G && i < 8) asm volatile("buffer_load_dwordx4 %0, %1, %2, %3 offen nt" : "=v"(c.lw[i & 7]) : "v"(c.vG0), "s"(c.rM), "s"(i * 6144 * 2) : "memory");
    if constexpr (k == K_A || k == K_A4) ld_act<i & 63>(c.vK[(i >> 1) & 3], c.rs, c.sAA[(i >> 3) & 1]);
    if constexpr (k == K_P) asm volatile("buffer_load_dword %0, %1, %2, %3 offen sc1" : "+v"(c.pollv) : "v"(c.pollo), "s"(c.rs), "s"(i ? c.fl4 ^ 128 : 0) : "memory");
    if constexpr (k == K_Z) asm volatile("buffer_store_dwordx4 %0, %1, %2, %3 offen offset:%4 sc1\n\ts_nop 1" ::"v"((u32x4){POISON, POISON, POISON, POISON}), "v"(c.vK[i >> 1]), "s"(c.rs), "s"(c.zo), "n"((i & 1) * 1024) : "memory");
    if constexpr (k == K_XS) asm volatile("buffer_store_dwordx4 %0, %1, %2, %3 offen offset:%4 sc1" ::"v"(c.acc[U_S][i >> 2][(i >> 1) & 1][i & 1]), "v"(c.vA), "s"(c.rs), "s"(c.xs4 + (i >> 2) * 4096), "n"((i & 3) * 1024) : "memory");
    if constexpr (k == K_XL) asm volatile("buffer_load_dwordx4 %0, %1, %2, %3 offen offset:%4 sc1" : "=v"(c.xl[i]) : "v"(c.vA), "s"(c.rs), "s"((c.xs4 ^ 32768) + (i >> 2) * 4096), "n"((i & 3) * 1024) : "memory");
  }
  GAP();
  if constexpr (e >= E_CK) { e_ck<(e - E_CK) / 8, (e - E_CK) % 8>(c); if constexpr (e - E_CK < 8 * 8) e_ck<(e - E_CK) / 8, (e - E_CK) % 8 + 1>(c); }
  else if constexpr (e >= 64 * P_FA) e_fin<e / 64 - P_FA, e % 64>(c);
  else if constexpr (e >= 64 * PR_HH) e_hh<pr_blk(e / 64), pr_up(e / 64), e % 64>(c);
  else if constexpr (e == 64 * P_PREV) e_prev(c);
  else if constexpr (e >= 0 && (e / 64 == P_K || e / 64 == P_V)) e_kv<e / 64 == P_V, e % 64>(c);
  else if constexpr (e >= 64 * P_UA) e_un<e - 64 * P_UA>(c);
  else if constexpr (e >= 0) e_gv<e / 64, e % 64>(c);
  GAP();
}
template <int H0, int... Hs> DI void hsteps_(Ctx& c, iseq<Hs...>) { ((slot<H0 + Hs, 0>(c), slot<H0 + Hs, 1>(c), slot<H0 + Hs, 2>(c), slot<H0 + Hs, 3>(c)), ...); }
template <int Ta, int Tb> DI void stages(Ctx& c) { hsteps_<8 * Ta>(c, make_iseq<8 * (Tb - Ta)>{}); }
template <int H0, int... Is> DI void rd0(Ctx& c, iseq<Is...>) { (rd<H0 + Is / 4, Is % 4>(c), ...); }
template <int... Is> DI void ld0(Ctx& c, iseq<Is...>) { (ld_act<Is>(c.vK[(Is >> 1) & 3], c.rs, c.sA0 + (Is >> 3) * 4 * KBS), ...); }
template <int S> DI void start(Ctx& c, unsigned base) {
  c.sA0 = base; pins(c.sA0);
  ld0(c, make_iseq<2 * seg_pre(S)>{});
  static_assert(seg_pre(S) % 4 == 0 && PL.op[8 * seg_t0(S)] / 1024 == K_A, "the stream opens with the next group");
  c.sAA[(seg_pre(S) / 4) & 1] = base + seg_pre(S) * KBS; pins(c.sAA[(seg_pre(S) / 4) & 1]);
  bar();
  rd0<8 * seg_t0(S)>(c, make_iseq<4 * RD>{});
  GAP();
  wait_for<8 * seg_t0(S), 8>(c);
  GAP();
}
template <int S> DI void start_nf(Ctx& c, unsigned base) {
  c.sA0 = base; pins(c.sA0);
  ld0(c, make_iseq<2 * seg_pre(S)>{});
  c.sAA[(seg_pre(S) / 4) & 1] = base + seg_pre(S) * KBS; pins(c.sAA[(seg_pre(S) / 4) & 1]);
  wait_vm<2 * seg_pre(S) - 2>();
  ck_group<0>(c, make_iseq<6>{});
  ck_retry<0>(c);
  GAP();
  wait_for<8 * seg_t0(S), 8>(c);
  GAP();
}
DI void lane_off(unsigned (&v)[4], int lane, int wave, int pitch, int col) {
  const int g = lane >> 4, q = (lane >> 2) & 3, p = lane & 3;
#pragma unroll
  for (int i = 0; i < 4; ++i) { v[i] = (32 * wave + 8 * i + 4 * (g >> 1) + q) * pitch + col + (g & 1) * 64 + (p ^ (g & 2)) * 16 - 1024 * i; pin(v[i]); }
}
DI void mod_load(Ctx& c) {
  asm volatile("buffer_load_dwordx4 %0, %8, %9, 0 offen sc1\n\tbuffer_load_dwordx4 %1, %8, %9, 0 offen offset:512 sc1\n\tbuffer_load_dwordx4 %2, %8, %9, 0 offen offset:1024 sc1\n\tbuffer_load_dwordx4 %3, %8, %9, 0 offen offset:1536 sc1\n\t"
               "buffer_load_dwordx4 %4, %8, %9, %10 offen sc1\n\tbuffer_load_dwordx4 %5, %8, %9, %10 offen offset:512 sc1\n\tbuffer_load_dwordx4 %6, %8, %9, %10 offen offset:1024 sc1\n\tbuffer_load_dwordx4 %7, %8, %9, %10 offen offset:1536 sc1"
               : "=&v"(c.mp[0][0]), "=&v"(c.mp[0][1]), "=&v"(c.mp[0][2]), "=&v"(c.mp[0][3]), "=&v"(c.mp[1][0]), "=&v"(c.mp[1][1]), "=&v"(c.mp[1][2]), "=&v"(c.mp[1][3]) : "v"(c.mo), "s"(c.rs), "s"(65536) : "memory");
}
template <int N> DI void mod_wait(Ctx& c) {
  asm volatile("s_waitcnt vmcnt(%8)" : "+v"(c.mp[0][0]), "+v"(c.mp[0][1]), "+v"(c.mp[0][2]), "+v"(c.mp[0][3]), "+v"(c.mp[1][0]), "+v"(c.mp[1][1]), "+v"(c.mp[1][2]), "+v"(c.mp[1][3]) : "n"(N) : "memory");
}
DI void mod_vectors(Ctx& c, int lane) {
  const int g = lane >> 4;
  if (__builtin_expect(!(tagged(c, c.mp[0]) && tagged(c, c.mp[1])), 0)) {
#pragma unroll 1
    for (unsigned spins = 0; spins < SPIN_CAP; ++spins) { mod_load(c); mod_wait<0>(c); if (tagged(c, c.mp[0]) && tagged(c, c.mp[1])) break; }
  }
  dmas<2, 15, 17>(c);
  f32x4 a;
#pragma unroll
  for (int e = 0; e < 4; ++e) { const u32x4 (&m)[4] = c.mp[e >> 1]; a[e] = (__uint_as_float(m[0][2 * (e & 1)]) + __uint_as_float(m[1][2 * (e & 1)])) + (__uint_as_float(m[2][2 * (e & 1)]) + __uint_as_float(m[3][2 * (e & 1)])); }
  const unsigned pb = c.lds0 + L_WV + c.wave * 1024;
  lds_st(pb + lane * 4, pk2(a[0], a[1])); lds_st(pb + 256 + lane * 4, pk2(a[2], a[3]));
#pragma unroll
  for (int i = 0; i < 12; ++i) c.mv[i] = lds_ld<u32x2>(pb + i * 32 + g * 8);
}
}

DI u32x4 gated_resid(const f32x4& a0, const f32x4& a1, const f32x2 (&eg)[4], const f32x2 (&er)[4]) {
  u32x4 o;
#pragma unroll
  for (int j = 0; j < 4; ++j) {
    const f32x4& v = j < 2 ? a0 : a1;
    const f32x2 r = (f32x2){v[2 * (j & 1)], v[2 * (j & 1) + 1]} * eg[j] + er[j];
    o[j] = pk2(r[0], r[1]);
  }
  return o;
}

DI void phase1(Ctx& c) {
  using namespace en;
  const int tid = opaque_tid(), lane = tid & 63, b = opaque_bid(), wave = c.wave;
  const int fr = lane & 15, g = lane >> 4, q = (lane >> 2) & 3, p = lane & 3;
  c.tch = 0;
  asm volatile("global_load_dword %0, %1, %2" : "+v"(c.tch) : "v"((b >> 3) * 256 + (tid & 1) * 128), "s"(c.pc0) : "memory");
  GAP();
  int tm, tn; map_g1(b, tm, tn);
  int tm2, tn2; map_g2(b, tm2, tn2);
  u32x4 gw[4], xs[4], lc; u32x2 gc, ep;
  c.rM = mk_rsrc(c.wmod, D_ * 6 * D_ * 2);
  {
    const int k0 = (b & 7) * 128 + (tid >> 3) * 4;
    asm volatile("global_load_dwordx4 %0, %1, %2" : "=v"(lc) : "v"((tid >> 3) * 16), "s"(c.cc + (b & 3) * 256) : "memory");
    asm volatile("global_load_dwordx2 %4, %10, %11\n\tbuffer_load_dwordx4 %0, %5, %6, 0 offen nt\n\tbuffer_load_dwordx4 %1, %5, %6, %7 offen nt\n\tbuffer_load_dwordx4 %2, %5, %6, %8 offen nt\n\tbuffer_load_dwordx4 %3, %5, %6, %9 offen nt"
                 : "=&v"(gw[0]), "=&v"(gw[1]), "=&v"(gw[2]), "=&v"(gw[3]), "=&v"(gc)
                 : "v"((k0 * 6144 + (b >> 3) * 64 + (tid & 7) * 8) * 2), "s"(c.rM), "s"(12288), "s"(24576), "s"(36864), "v"(k0 * 2), "s"(c.cc) : "memory");
    asm volatile("global_load_dwordx2 %0, %1, %2 sc1" : "=v"(ep) : "v"(0), "s"(c.ws) : "memory");
    asm volatile("global_load_dwordx4 %0, %4, %5\n\tglobal_load_dwordx4 %1, %4, %5 offset:512\n\tglobal_load_dwordx4 %2, %4, %5 offset:1024\n\tglobal_load_dwordx4 %3, %4, %5 offset:1536"
                 : "=&v"(xs[0]), "=&v"(xs[1]), "=&v"(xs[2]), "=&v"(xs[3]) : "v"((tid >> 5) * 2048 + (tid & 31) * 16), "s"(c.x + (tm2 * 128 + tn2 * 8) * D_) : "memory");
  }
  GAP();
  c.rs = mk_rsrc(c.ws, W_END); c.rQ = mk_rsrc(c.wqkv, D_ * 3 * D_ * 2); c.rO = mk_rsrc(c.wo, D_ * D_ * 2);
  c.r1 = mk_rsrc(c.w1, D_ * F_ * 2); c.r3 = mk_rsrc(c.w3, D_ * F_ * 2); c.r2 = mk_rsrc(c.w2, D_ * F_ * 2);
  { unsigned n; asm volatile("s_mov_b32 %0, .Lblk_end-.Lblk_begin" : "=s"(n)); c.rC = mk_rsrc((const void*)c.pc0, n); }
  c.mB = c.lds0 + wave * 4096;
  pins(c.rs); pins(c.rQ); pins(c.rO); pins(c.rM); pins(c.r1); pins(c.r3); pins(c.r2); pins(c.rC); pins(c.mB);
  const int j8 = ((b >> 3) * 4 + wave) * 8;
  const unsigned slow1 = (j8 + (lane & 7)) * 6144 + slow_col(c.wqkv, (b & 3) * 4, 4), row1 = (b * 4 + wave) * 6144 + fr * 128;
  c.vT1 = sel(lane < 8, slow1, OOB); pin(c.vT1);
  c.vC = sel(lane == 2, (b * 4 + wave) * 128, j8 * 32 + (lane & 1) * 128);
  FST();
  warm_code(c);
  unsigned tk = sel(lane < 16, row1, sel(lane < 24, slow1, OOB)), tvq = sel(lane < 32, row1 + (lane >> 4) * 4096, sel(lane < 40, slow1 + 4096, OOB));
  unsigned i0 = W_HALO + (((tm2 - 1) & 15) * 16 + tn2) * 4096 + wave * 1024 + lane * 16, iz = lane < 16 ? POISON : 0u;
  unsigned i1 = sel(lane < 16, W_STATS + (tm2 * 4 + wave) * 4096 + tn2 * 256 + lane * 16, sel(lane == 16, flag_off(tm2, wave, tn2, PH_G1), OOB));
  unsigned xo = (fr * D_ + (g & 1) * 16 + (g >> 1) * 8) * 2, xo1 = xo + 16 * D_ * 2;
  unsigned o0 = W_PART + ((wave >> 1) * 16 + tn2) * 4096 + (wave & 1) * 2048 + lane * 16, o1 = W_LN1 + (tm2 * 128 + wave * 32 + fr) * 16;
  unsigned fl1 = sel(lane < 16, flag_off(tm, wave, fr, 0), flag_off(min(tm + 1, 15), wave, tn, 0));
  pin(tk); pin(tvq); pin(i0); pin(i1); pin(iz); pin(xo); pin(xo1); pin(o0); pin(o1); pin(fl1);
  lane_off(c.v1, lane, wave, 6144, tn * 128);
  c.vA = lane * 16; pin(c.vA); c.vK[0] = c.vA;
#pragma unroll
  for (int i = 1; i < 4; ++i) { c.vK[i] = c.vA + i * KBS; pin(c.vK[i]); }
  c.vT2 = sel(lane < 16, (b * 4 + wave) * 2048 + lane * 128, sel(lane < 24, (j8 + lane - 16) * 2048 + slow_col(c.wo, (b & 2) * 4, 8), OOB));
  c.vT3 = sel(lane < 16, (b * 4 + wave) * (F_ * 2) + lane * 128, sel(lane < 24, (j8 + lane - 16) * (F_ * 2) + slow_col(c.w1, (b & 3) * 4, 4), OOB));
  c.vG0 = (((b & 3) * 256 + (tid >> 3) * 8) * 6144 + 2048 + (b >> 2) * 64 + (tid & 7) * 8) * 2;
  pin(c.vG0);
  pin(c.vT2); pin(c.vT3);
#pragma unroll
  for (int w = 0; w < 2; ++w) {
    const unsigned bw = c.lds0 + w * 65536 + g * 512 + q * 64 + p * 8;
    c.bW[0][w] = bw + (g & 1) * 32; c.bW[1][w] = bw + ((g & 1) ^ 1) * 32;
    pin(c.bW[0][w]); pin(c.bW[1][w]);
  }
  lane_off(c.v2, lane, wave, 2048, 0);
  { int tm3, tn3; map_g3(b, tm3, tn3); lane_off(c.v3, lane, wave, F_ * 2, tn3 * 128); }
  c.sB4[0][0] = tn2 * 128; c.zo = W_H2 + 2 * tn2 * KBS + (tm2 * 8 + 2 * wave) * 1024; pins(c.zo);
  c.mo = W_LATE + ((lane >> 5) * 16 + tn2) * 2048 + (lane & 31) * 16; pin(c.mo);
  c.kvw = c.lds0 + L_KV + wave * 4096 + lane * 16; pin(c.kvw);
  c.hso = W_HALO + ((tm - 1) * 16 + tn) * 4096;
  c.hst = wave == 3 ? c.hso + 65536 : W_END; pins(c.hst);
  unsigned e_sel[4]; int e_base; f32x2 e_bias;
  {
    e_base = (fr & ~1) - 4 * g;
    const bool first = tm == 0 && wave == 0;
    e_bias = (f32x2){first && e_base < 0 ? -3.0e38f : 0.f, first && e_base < 2 ? -3.0e38f : 0.f};
    const unsigned s0 = (fr & 1) ? 0x01000c0cu : 0x0c0c0100u, s1 = (fr & 1) ? 0x03020c0cu : 0x0c0c0302u, z = 0x0c0c0c0cu;
    e_sel[0] = e_base >= 0 ? z : s0; e_sel[1] = e_base >= 2 ? z : s1; e_sel[2] = e_base >= 0 ? s0 : z; e_sel[3] = e_base >= 2 ? s1 : z;
    pin(e_base); pin(e_bias); pin(e_sel[0]); pin(e_sel[1]); pin(e_sel[2]); pin(e_sel[3]);
  }
  GAP();
  float sv[4];
  asm volatile("s_waitcnt vmcnt(10)" : "+v"(gc), "+v"(lc)::"memory");
#pragma unroll
  for (int i = 0; i < 4; ++i) { sv[i] = silu_bf((u16)(gc[i >> 1] >> (16 * (i & 1)))); pin(sv[i]); }
#pragma unroll
  for (int i = 0; i < 8; ++i) { c.ls[i] = silu_bf((u16)(lc[i >> 1] >> (16 * (i & 1)))); pin(c.ls[i]); }
  GAP();
  FST();
  asm volatile("s_waitcnt vmcnt(%5)" : "+v"(gw[0]), "+v"(gw[1]), "+v"(gw[2]), "+v"(gw[3]), "+v"(ep) : "n"(5) : "memory");
  c.tag[0] = __builtin_amdgcn_readfirstlane(ep[0]) + 1; c.tag[1] = __builtin_amdgcn_readfirstlane(ep[1]) + 1;
  touch(c, tk, c.rQ, 2048);
  GAP();
  FST();
  {
    f32x2 acc[4];
#pragma unroll
    for (int j = 0; j < 4; ++j) acc[j] = (f32x2){0.f, 0.f};
#pragma unroll
    for (int i = 0; i < 4; ++i) {
#pragma unroll
      for (int j = 0; j < 4; ++j) acc[j] += unpk(gw[i][j]) * sv[i];
    }
    lds_st(c.lds0 + L_RED + wave * 256 + (lane & 7) * 32 + g * 8, red8(acc));
    wait_lgkm<0>(); bar();
    if (wave == 3) gemv_out(c, W_PART + b * 512, lane);
    GAP();
    asm volatile("global_load_dwordx4 %0, %4, %6\n\tglobal_load_dwordx4 %1, %4, %6 offset:64\n\tglobal_load_dwordx4 %2, %5, %6\n\tglobal_load_dwordx4 %3, %5, %6 offset:64"
                 : "=&v"(c.xr[0][0]), "=&v"(c.xr[0][1]), "=&v"(c.xr[1][0]), "=&v"(c.xr[1][1]) : "v"(xo), "v"(xo1), "s"(c.x + (tm2 * 128 + wave * 32) * D_ + tn2 * 64) : "memory");
    asm volatile("s_waitcnt vmcnt(%4)" : "+v"(xs[0]), "+v"(xs[1]), "+v"(xs[2]), "+v"(xs[3]) : "n"(6) : "memory");
    f32x2 f[16], s2 = {0.f, 0.f};
#pragma unroll
    for (int i = 0; i < 16; ++i) { f[i] = unpk(xs[i >> 2][i & 3]); s2 += f[i]; }
    const float mean = xadd16(row16_sum(s2[0] + s2[1])) * (1.f / 1024.f);
    f32x2 q2 = {0.f, 0.f};
#pragma unroll
    for (int i = 0; i < 16; ++i) { const f32x2 d = f[i] - mean; q2 += d * d; }
    const float rstd = ln_rstd(xadd16(row16_sum(q2[0] + q2[1])) * (1.f / 1024.f));
    if (!(lane & 31)) st16(c, W_LN1 + (tm2 * 128 + tn2 * 8 + (tid >> 5)) * 16, granule(c, rbf(mean), rstd));
  }
  st16(c, i0, (u32x4){POISON, POISON, POISON, POISON}); st16(c, i1, (u32x4){iz, iz, iz, iz});
  GAP();
  FST();
  bar();
  dmas<1, 0, NFD>(c);
  FST();
  f32x4 ln;
  {
    u32x4 v[4];
#pragma unroll 1
    for (unsigned spins = 0; spins < SPIN_CAP; ++spins) {
      asm volatile("buffer_load_dwordx4 %0, %4, %6, 0 offen sc1\n\tbuffer_load_dwordx4 %1, %4, %6, 0 offen offset:1024 sc1\n\tbuffer_load_dwordx4 %2, %5, %6, 0 offen sc1\n\tbuffer_load_dwordx4 %3, %5, %6, 0 offen offset:256 sc1\n\ts_waitcnt vmcnt(0)"
                   : "=&v"(v[0]), "=&v"(v[1]), "=&v"(v[2]), "=&v"(v[3]) : "v"(o0), "v"(o1), "s"(c.rs) : "memory");
      if (tagged(c, v)) break;
      __builtin_amdgcn_s_sleep(1);
    }
    pin(c.xr[0][0]); pin(c.xr[0][1]); pin(c.xr[1][0]); pin(c.xr[1][1]);
    FST();
    ln = (f32x4){__uint_as_float(v[2][0]), __uint_as_float(v[2][2]), __uint_as_float(v[3][0]), __uint_as_float(v[3][2])};
    lds_st(c.lds0 + L_RED + wave * 256 + (lane & 31) * 8, (f32x2){xadd32(__uint_as_float(v[0][0]) + __uint_as_float(v[1][0])), xadd32(__uint_as_float(v[0][2]) + __uint_as_float(v[1][2]))});
    wait_lgkm<0>(); bar();
    touch(c, tvq, c.rQ, 0);
  }
  FST();
  {
    const f32x4 r = lds_ld<f32x4>(c.lds0 + L_RED + (g & 1) * 512 + fr * 16) + lds_ld<f32x4>(c.lds0 + L_RED + (g & 1) * 512 + 256 + fr * 16);
    const f32x2 one = (g & 1) ? (f32x2){1.f, 1.f} : (f32x2){0.f, 0.f};
    const f32x2 m0 = rbf2(rbf2((f32x2){r[0], r[1]}) + one), m1 = rbf2(rbf2((f32x2){r[2], r[3]}) + one);
    const unsigned Mt = c.lds0 + L_WV + wave * 1024, off = W_H1 + 2 * tn2 * KBS + (tm2 * 8 + 2 * wave) * 1024 + lane * 16;
    lds_st(Mt + (lane & 31) * 16, (f32x4){m0[0], m0[1], m1[0], m1[1]});
#pragma unroll
    for (int s = 0; s < 2; ++s) {
      const unsigned a = Mt + s * 128 + g * 16;
      const f32x4 sh0 = lds_ld<f32x4>(a), sh1 = lds_ld<f32x4>(a + 64), sc0 = lds_ld<f32x4>(a + 256), sc1 = lds_ld<f32x4>(a + 320);
      const f32x2 sc[4] = {{sc0[0], sc0[1]}, {sc0[2], sc0[3]}, {sc1[0], sc1[1]}, {sc1[2], sc1[3]}}, sh[4] = {{sh0[0], sh0[1]}, {sh0[2], sh0[3]}, {sh1[0], sh1[1]}, {sh1[2], sh1[3]}};
#pragma unroll
      for (int i = 0; i < 2; ++i) {
        c.xr[i][s] = swz_rows(c.xr[i][s]);
        u32x4 o;
#pragma unroll
        for (int j = 0; j < 4; ++j) {
          const f32x2 y = rbf2(rbf2(rbf2(unpk(c.xr[i][s][j]) - ln[2 * i]) * ln[2 * i + 1]) * sc[j]) + sh[j];
          o[j] = pk2(y[0], y[1]);
        }
        st16(c, off + s * KBS + i * 1024, o);
      }
    }
  }
  GAP();
  STAMP();
  dmas<1, NFD, NFD + NF2>(c);
  wait_vm<NF2>();
  asm volatile("s_mov_b64 exec, 1\n\tbuffer_store_dwordx2 %0, off, %1, %2 sc1\n\ts_mov_b64 exec, -1" ::"v"((u32x2){c.tag[0], c.tag[1]}), "s"(c.rs), "s"(flag_off(tm2, wave, tn2, 0)) : "memory");
  STAMP();
  bar();
  FST();
  dmas<1, NFD + NF2, 64>(c);
  FST();
  pg_hop<0>(wave);
  await<PH_LN1>(c, fl1);
  if (b + wave == 0) asm volatile("s_mov_b64 exec, 1\n\tbuffer_store_dwordx2 %0, off, %1, 0 sc1\n\ts_mov_b64 exec, -1" ::"v"((u32x2){c.tag[0], c.tag[1]}), "s"(c.rs) : "memory");
  STAMP();
  start<1>(c, W_H1 + (tm * 8 + 2 * wave) * 1024);
  STAMP();
  stages<0, 8>(c);
  STAMP();
  stages<8, 16>(c);
  STAMP();
  stages<16, 24>(c);
  mfma_fence();
  STAMP();
  {
    u32x4 qf[2][2];
#pragma unroll
    for (int i = 0; i < 4; ++i) qf[i >> 1][i & 1] = pack8(c.acc[U_Q][i >> 1][i & 1][0], c.acc[U_Q][i >> 1][i & 1][1]);
    asm volatile("s_waitcnt vmcnt(%4) lgkmcnt(0)" : "+v"(c.hl[0]), "+v"(c.hl[1]), "+v"(c.hl[2]), "+v"(c.hl[3]) : "n"(PL.halo) : "memory");
    if (wave == 0) retry(c, c.hl, c.vA + c.hso, 1024);
    dmas<2, 0, 1>(c);
    u32x4 vo[2][2][2];
#pragma unroll
    for (int h = 0; h < 2; ++h)
#pragma unroll
      for (int j = 0; j < 2; ++j) {
        vo[h][0][j] = (u32x4){c.hl[2 + h][2 * j], c.hl[2 + h][2 * j + 1], c.vf[h][0][2 * j], c.vf[h][0][2 * j + 1]};
        vo[h][1][j] = (u32x4){c.vf[h][0][2 * j], c.vf[h][0][2 * j + 1], c.vf[h][1][2 * j], c.vf[h][1][2 * j + 1]};
      }
#pragma unroll
    for (int i = 0; i < 4; ++i) { pin(qf[i >> 1][i & 1]); pin(c.hl[i]); }
    GAP(); nops<2>();
    f32x4 sp[2][2], sc[2][2];
#pragma unroll
    for (int h = 0; h < 2; ++h) { mfv(sp[h][0], c.hl[h], qf[h][0]); mfv(sc[h][0], c.kf[h][0], qf[h][0]); mfv(sp[h][1], c.kf[h][0], qf[h][1]); mfv(sc[h][1], c.kf[h][1], qf[h][1]); }
    GAP();
#pragma unroll
    for (int i = 0; i < 8; ++i) pin(vo[i >> 2][(i >> 1) & 1][i & 1]);
    dmas<2, 1, 2>(c);
    mfma_fence();
    const bool par = fr & 1, cur0 = e_base >= 0, cur1 = e_base >= 2;
    auto red2 = [](float& u, float& v, auto op) {
      auto r = __builtin_amdgcn_permlane16_swap(__float_as_uint(u), __float_as_uint(v), false, false);
      const float m = op(__uint_as_float(r[0]), __uint_as_float(r[1]));
      auto r2 = __builtin_amdgcn_permlane32_swap(__float_as_uint(m), __float_as_uint(m), false, false);
      const float t = op(__uint_as_float(r2[0]), __uint_as_float(r2[1]));
      auto r3 = __builtin_amdgcn_permlane16_swap(__float_as_uint(t), __float_as_uint(t), false, false);
      u = __uint_as_float(r3[0]); v = __uint_as_float(r3[1]);
    };
    f32x2 l[2][2]; float mx[2][2], sm[2][2]; u32x4 pw[2][2];
#pragma unroll
    for (int h = 0; h < 2; ++h)
#pragma unroll
      for (int i = 0; i < 2; ++i) {
        dma_at<2, 2, 6>(c, 2 * h + i);
        const float a0 = sp[h][i][0], a1 = sp[h][i][1], a2 = sp[h][i][2], a3 = sp[h][i][3], b0 = sc[h][i][0], b1 = sc[h][i][1], b2 = sc[h][i][2], b3 = sc[h][i][3];
        const float x0 = cur0 ? b0 : a0, x1 = cur0 ? b1 : a1, x2 = cur1 ? b2 : a2, x3 = cur1 ? b3 : a3;
        const float y0 = par ? x1 : x0, y1 = par ? x3 : x2;
        l[h][i] = rbf2(rbf2((f32x2){y0, y1}) * 0.17677669529663687f);
        if (i == 0) l[h][i] += e_bias;
        mx[h][i] = fmax1(l[h][i][0], l[h][i][1]);
      }
    dmas<2, 6, 7>(c);
#pragma unroll
    for (int h = 0; h < 2; ++h) red2(mx[h][0], mx[h][1], [](float x, float y) { return fmax1(x, y); });
#pragma unroll
    for (int h = 0; h < 2; ++h)
#pragma unroll
      for (int i = 0; i < 2; ++i) {
        if (i == 0) dma_at<2, 7, 9>(c, h);
        const f32x2 t = (l[h][i] - mx[h][i]) * 1.4426950408889634f;
        l[h][i] = (f32x2){__builtin_amdgcn_exp2f(t[0]), __builtin_amdgcn_exp2f(t[1])};
        sm[h][i] = l[h][i][0] + l[h][i][1];
      }
    dmas<2, 9, 10>(c);
#pragma unroll
    for (int h = 0; h < 2; ++h) red2(sm[h][0], sm[h][1], [](float x, float y) { return x + y; });
#pragma unroll
    for (int h = 0; h < 2; ++h)
#pragma unroll
      for (int i = 0; i < 2; ++i) {
        if (i == 0) dma_at<2, 10, 12>(c, h);
        const f32x2 pr = l[h][i] * __builtin_amdgcn_rcpf(sm[h][i]);
        const unsigned pp = pk2(pr[0], pr[1]);
        pw[h][i] = (u32x4){__builtin_amdgcn_perm(pp, pp, e_sel[0]), __builtin_amdgcn_perm(pp, pp, e_sel[1]), __builtin_amdgcn_perm(pp, pp, e_sel[2]), __builtin_amdgcn_perm(pp, pp, e_sel[3])};
        pin(pw[h][i]);
      }
    GAP(); nops<2>();
    f32x4 o[2][2][2];
#pragma unroll
    for (int i = 0; i < 8; ++i) mfv(o[i >> 2][(i >> 1) & 1][i & 1], vo[i >> 2][(i >> 1) & 1][i & 1], pw[i >> 2][(i >> 1) & 1]);
    dmas<2, 12, 13>(c);
    static_assert(NFREE2 >= 13, "slots of these pieces are free");
    mfma_fence();
    const unsigned oo = W_O + 2 * tn * KBS + (tm * 8 + 2 * wave) * 1024 + c.vA;
#pragma unroll
    for (int i = 0; i < 4; ++i) st16(c, oo + (i >> 1) * KBS + (i & 1) * 1024, pack8(o[i >> 1][i & 1][0], o[i >> 1][i & 1][1]));
  }
  GAP();
  mod_load(c);
  STAMP();
  wait_vm<8>();
  set_flag(c, flag_off(tm, wave, tn, PH_G1), 1);
  STAMP();
}

DI void setup3(Ctx& c) {
  using namespace en;
  const int lane = opaque_tid() & 63, b = opaque_bid(), wave = c.wave;
  int tm, tn, tm4, tn4; map_g3(b, tm, tn); map_g2(b, tm4, tn4);
  {
    const int g = lane >> 4, q = (lane >> 2) & 3, p = lane & 3;
#pragma unroll
    for (int i = 0; i < 4; ++i) {
      c.bM[i >> 1][i & 1] = c.bW[i >> 1][i & 1] - g * 256; pin(c.bM[i >> 1][i & 1]);
      c.vM[i] = (32 * wave + 16 * (i >> 1) + 4 * g + q) * (F_ * 2) + (96 + tn) * 64 + (p ^ (2 * (g & 1))) * 16 - 1024 * i; pin(c.vM[i]);
    }
    const int j8 = ((b >> 3) * 4 + wave) * 8;
    c.vT4 = sel(lane < 16, (j8 + (lane >> 1)) * (F_ * 2) + 6144 + (b & 3) * 256 + (lane & 1) * 128, OOB);
    c.vT5 = sel(lane < 16, c.vT2, OOB);
    c.vT2 = sel((lane >> 3) == 2, (j8 + lane - 16) * 2048 + slow_col(c.w2, (b & 2) * 4, 8), OOB);
    pin(c.vT2); pin(c.vT4); pin(c.vT5);
    const int kh = tn4 & 1;
#pragma unroll
    for (int i = 0; i < 4; ++i) { c.sB4[i >> 1][i & 1] = (tn4 ^ 1 ^ (i & 1)) * 128 + kh * (16 >> (i >> 1)) * 32 * 2048; pins(c.sB4[i >> 1][i & 1]); }
#pragma unroll
    for (int i = 0; i < 2; ++i) { c.sA4[i] = W_HH + kh * (16 >> i) * KBS + (tm4 * 8 + 2 * wave) * 1024; pins(c.sA4[i]); }
    c.so[1] = W_HH + tn * KBS + (tm * 8 + 2 * wave) * 1024; c.so[0] = c.so[1] + tn * KBS;
    c.fl3 = flag_off(tm, wave, tn, PH_G3); c.fl4 = flag_off(tm4, wave, tn4, PH_G4);
    c.xs4 = W_XCH + (tm4 * 16 + tn4) * 32768 + wave * 8192;
    c.pollo = flag_off(tm4, wave, kh * 8 + (lane & 7), PH_G3); c.flv = 0; c.pollv = 0;
    pin(c.pollo); pin(c.flv); pins(c.so[0]); pins(c.so[1]); pins(c.fl3); pins(c.fl4); pins(c.xs4);
  }
}
DI void phase2(Ctx& c) {
  using namespace en;
  const int tid = opaque_tid(), lane = tid & 63, b = opaque_bid(), wave = c.wave;
  const int fr = lane & 15, g = lane >> 4;
  int tm, tn; map_g2(b, tm, tn);
  bar();
  dmas<2, 13, 15>(c);
  mod_wait<3>(c);
  mod_vectors(c, lane);
  dmas<2, 17, 64>(c);
  STAMP();
  await<PH_G1>(c, flag_off(tm, wave, fr, 0));
  STAMP();
  start<2>(c, W_O + (tm * 8 + 2 * wave) * 1024);
  STAMP();
  stages<24, 32>(c);
  mfma_fence();
  STAMP();
  f32x2 d[2][8]; float s1[2];

#pragma unroll
  for (int i = 0; i < 2; ++i) {
#pragma unroll
    for (int s = 0; s < 2; ++s) {
      dma_at<3, 0, NFREE3>(c, 4 * i + 2 * s);
      c.xr[i][s] = gated_resid(c.acc[U_O][s][i][0], c.acc[U_O][s][i][1], c.eg[s], c.er[i][s]);
      dma_at<3, 0, NFREE3>(c, 4 * i + 2 * s + 1);
#pragma unroll
      for (int j = 0; j < 4; ++j) d[i][4 * s + j] = unpk(c.xr[i][s][j]);
    }
    const f32x2 s2 = ((d[i][0] + d[i][1]) + (d[i][2] + d[i][3])) + ((d[i][4] + d[i][5]) + (d[i][6] + d[i][7]));
    s1[i] = s2[0] + s2[1];
  }
  const f32x2 mu = spread2(add2(s1[0], s1[1]) * (1.f / 64.f));
#pragma unroll
  for (int i = 0; i < 2; ++i) {
    f32x2 q2 = {0.f, 0.f};
#pragma unroll
    for (int j = 0; j < 8; ++j) { if (!(j & 3)) dma_at<3, 8, NFREE3>(c, 2 * i + (j >> 2)); d[i][j] -= mu[i]; q2 += d[i][j] * d[i][j]; }
    s1[i] = q2[0] + q2[1];
  }
  const f32x2 m2 = spread2(add2(s1[0], s1[1]));
  const unsigned sb = W_STATS + (tm * 4 + wave) * 4096;
  if (g == 0) st16(c, sb + tn * 256 + fr * 16, (u32x4){min(__float_as_uint(mu[0]), POISON - 1), min(__float_as_uint(m2[0]), POISON - 1), min(__float_as_uint(mu[1]), POISON - 1), min(__float_as_uint(m2[1]), POISON - 1)});
  GAP();
  dmas<3, 12, (NFREE3 < 16 ? NFREE3 : 16)>(c);
  STAMP();
  u32x4 pv[4];
  pg_hop<1>(wave);
  ld16x4(c, pv, sb + c.vA, 1024);
  retry(c, pv, sb + c.vA, 1024);
  STAMP();
  {
    f32x2 um[4], s2 = {0.f, 0.f}, q2 = {0.f, 0.f};
#pragma unroll
    for (int t = 0; t < 4; ++t) { um[t] = (f32x2){__uint_as_float(pv[t][0]), __uint_as_float(pv[t][2])}; s2 += um[t]; q2 += (f32x2){__uint_as_float(pv[t][1]), __uint_as_float(pv[t][3])}; }
    const float mg = add2(s2[0], s2[1]) * (1.f / 16.f);
    const f32x2 mean = spread2(mg);
    f32x2 e2 = {0.f, 0.f};
#pragma unroll
    for (int t = 0; t < 4; ++t) { const f32x2 dd = um[t] - mean; e2 += dd * dd; }
    q2 += e2 * 64.f;
    const f32x2 rs = spread2(ln_rstd(add2(q2[0], q2[1]) * (1.f / 1024.f))), mb = rbf2(mean);
    const unsigned ob = W_H2 + 2 * tn * KBS + (tm * 8 + 2 * wave) * 1024 + c.vA;
#pragma unroll
    for (int i = 0; i < 2; ++i) {
      const float dm = (mu[i] - mb[i]) * rs[i];
#pragma unroll
      for (int s = 0; s < 2; ++s) {
        u32x4 o;
#pragma unroll
        for (int j = 0; j < 4; ++j) {
          const f32x2 y = (d[i][4 * s + j] * rs[i] + dm) * c.sc1[4 * s + j] + c.sh[4 * s + j];
          o[j] = pk2(y[0], y[1]);
        }
        o[1] = min(o[1], POISON - 1); o[3] = min(o[3], POISON - 1);
        st16(c, ob + s * KBS + i * 1024, o);
      }
    }
  }
  GAP();
  STAMP();
  STAMP();
}

DI void phase3(Ctx& c) {
  using namespace en;
  int tid = opaque_tid(), lane = tid & 63; const int b = opaque_bid(), wave = c.wave;
  int tm, tn, tm4, tn4; map_g3(b, tm, tn); map_g2(b, tm4, tn4);
  static_assert(PL.d1[3] - PL.d0[3] <= NFREE3 && NFREE3 <= 16, "every piece fetched so far is older than the poll of the statistics");
  bar();
  rd0<8 * T3>(c, make_iseq<4 * RD>{});
  setup3(c);
  STAMP(); STAMP();
  start_nf<3>(c, W_H2 + (tm * 8 + 2 * wave) * 1024);
  STAMP();
  stages<32, 40>(c);
  STAMP();
  stages<40, 48>(c);
  STAMP();
  stages<48, 56>(c);
  STAMP();
  stages<56, 64>(c);
  STAMP();
  stages<64, 72>(c);
  STAMP();
  stages<72, 80>(c);
  STAMP();
  stages<80, 88>(c);
  STAMP();
  stages<88, 96>(c);
  STAMP();
  stages<96, 104>(c);
  STAMP();
  stages<104, 110>(c);
  STAMP();
  stages<110, NT - 1>(c);
  tid = opaque_tid(); lane = tid & 63;
  const int g = lane >> 4;
  unsigned oo = ((tm4 * 128 + wave * 32 + (lane & 7)) * D_ + tn4 * 64 + (lane & 8) * 4 + (g & 1) * 16 + (g >> 1) * 8) * 2;
  pin(oo);
  GAP();
  stages<NT - 1, NT>(c);
  mfma_fence();
  STAMP();
  pin(c.tch);
  wait_vm<0>();
  fin_(c, make_iseq<32>{});
  const rsrc_t ro = mk_rsrc(c.out, S_ * D_ * 2);
#pragma unroll
  for (int rb = 0; rb < 2; ++rb) {
    u32x4 o[2];
#pragma unroll
    for (int s = 0; s < 2; ++s)
      o[s] = swz_rows(gated_resid(c.acc[U_P][s][rb][0], c.acc[U_P][s][rb][1], c.eg[s], c.er[rb][s]));
    u32x4 w[2];
#pragma unroll
    for (int e = 0; e < 4; ++e) {
      w[0][e] = __builtin_amdgcn_update_dpp(o[0][e], o[1][e], 0x128, 0xf, 0xc, false);
      w[1][e] = __builtin_amdgcn_update_dpp(o[1][e], o[0][e], 0x128, 0xf, 0x3, false);
    }
#pragma unroll
    for (int s = 0; s < 2; ++s) __builtin_amdgcn_raw_buffer_store_b128(w[s], ro, oo + (rb * 16 + s * 8) * D_ * 2, 0, AUX_SC1);
  }
  STAMP();
}

__global__ __launch_bounds__(256, 1) __attribute__((aligned(4096))) void block_kernel(const u16* x, const u16* wqkv, const u16* wo, const u16* w1, const u16* w3, const u16* w2,
                                                       const u16* cc, const u16* wmod, u16* out, char* ws) {
  extern __shared__ __attribute__((aligned(16))) char smem[];
  asm volatile("" ::: ACLOB);
  Ctx c;
  c.x = x; c.wqkv = wqkv; c.wo = wo; c.w1 = w1; c.w3 = w3; c.w2 = w2; c.cc = cc; c.wmod = wmod; c.out = out; c.ws = ws;
  c.lds0 = (unsigned)(uintptr_t)(LDS_PTR(char))smem;
  asm volatile("s_getpc_b64 %0\n.Lblk_begin:" : "=s"(c.pc0));
  c.wave = __builtin_amdgcn_readfirstlane(threadIdx.x >> 6);
  STAMP0();
  phase1(c);
  phase2(c);
  phase3(c);
  asm volatile(".Lblk_end:");
}

extern "C" void kernel_launch(void* const* d_in, const int* in_sizes, int n_in, void* d_out, int out_size, void* d_ws,
                              size_t ws_size, hipStream_t stream) {
  (void)hipFuncSetAttribute((const void*)block_kernel, hipFuncAttributeMaxDynamicSharedMemorySize, LDS_TOTAL);
  block_kernel<<<256, 256, LDS_TOTAL, stream>>>((const u16*)d_in[0], (const u16*)d_in[1], (const u16*)d_in[2], (const u16*)d_in[4],
                                                (const u16*)d_in[5], (const u16*)d_in[6], (const u16*)d_in[7], (const u16*)d_in[8], (u16*)d_out, (char*)d_ws);
}
```

```cpp
#include <hip/hip_runtime.h>
#include <hip/hip_bf16.h>
#include <stdint.h>
#include <utility>

typedef unsigned short u16;
typedef short bf16x8 __attribute__((ext_vector_type(8)));
typedef short bf16x4 __attribute__((ext_vector_type(4)));
typedef float f32x4 __attribute__((ext_vector_type(4)));
typedef float f32x2 __attribute__((ext_vector_type(2)));
typedef unsigned int u32x4 __attribute__((ext_vector_type(4)));
typedef unsigned int u32x2 __attribute__((ext_vector_type(2)));
typedef __amdgpu_buffer_rsrc_t rsrc_t;
#define LDS_PTR(T) __attribute__((address_space(3))) T*
#define DI __device__ __forceinline__
#define GAP() __builtin_amdgcn_sched_barrier(0)
template <int... Is> using iseq = std::integer_sequence<int, Is...>;
template <int N> using make_iseq = std::make_integer_sequence<int, N>;

constexpr int S_ = 2048, D_ = 1024, F_ = 3584;
constexpr int KBS = (S_ / 16) * 1024;

DI float bf2f(u16 v) { return __uint_as_float(((unsigned)v) << 16); }
typedef __bf16 bf16x2_t __attribute__((ext_vector_type(2)));
DI unsigned pk2(float lo, float hi) { const f32x2 v = {lo, hi}; return __builtin_bit_cast(unsigned, __builtin_convertvector(v, bf16x2_t)); }
DI float rbf(float v) { return __uint_as_float(pk2(0.f, v)); }
DI float lo2f(unsigned u) { return __uint_as_float(u << 16); }
DI float hi2f(unsigned u) { return __uint_as_float(u & 0xffff0000u); }
template <int N> DI void wait_vm() { asm volatile("s_waitcnt vmcnt(%0)" ::"n"(N) : "memory"); }
template <int N> DI void wait_lgkm() { __builtin_amdgcn_s_waitcnt(0xC07F | (N << 8)); asm volatile("" ::: "memory"); }
DI void bar() { asm volatile("" ::: "memory"); __builtin_amdgcn_s_barrier(); asm volatile("" ::: "memory"); }
template <int IMM> DI void m0_add(unsigned base) { asm volatile("s_add_u32 m0, %0, %1" ::"s"(base), "n"(IMM) : "scc", "memory"); }
template <int N> DI void nops() { asm volatile("s_nop %0" ::"n"(N - 1)); }
template <class T> DI void pin(T& v) { asm volatile("" : "+v"(v)); }
template <class T> DI void pins(T& v) { asm volatile("" : "+s"(v)); }
DI unsigned sel(bool c, unsigned a, unsigned b) { pin(a); pin(b); return c ? a : b; }
constexpr unsigned OOB = 0x7fffffff;
DI rsrc_t mk_rsrc(const void* p, unsigned bytes) { return __builtin_amdgcn_make_buffer_rsrc((void*)p, 0, bytes, 0x00020000); }
template <int OFF> DI void bdma(unsigned voff, const rsrc_t& rs, unsigned soff) {
  asm volatile("buffer_load_dwordx4 %0, %1, %2 offen offset:%3 lds" ::"v"(voff), "s"(rs), "s"(soff), "n"(OFF) : "memory");
}
#define ACLOB "a0","a1","a2","a3","a4","a5","a6","a7","a8","a9","a10","a11","a12","a13","a14","a15","a16","a17","a18","a19","a20","a21","a22","a23","a24","a25","a26","a27","a28","a29","a30","a31","a32","a33","a34","a35","a36","a37","a38","a39","a40","a41","a42","a43","a44","a45","a46","a47","a48","a49","a50","a51","a52","a53","a54","a55","a56","a57","a58","a59","a60","a61","a62","a63","a64","a65","a66","a67","a68","a69","a70","a71","a72","a73","a74","a75","a76","a77","a78","a79","a80","a81","a82","a83","a84","a85","a86","a87","a88","a89","a90","a91","a92","a93","a94","a95","a96","a97","a98","a99","a100","a101","a102","a103","a104","a105","a106","a107","a108","a109","a110","a111","a112","a113","a114","a115","a116","a117","a118","a119","a120","a121","a122","a123","a124","a125","a126","a127","a128","a129","a130","a131","a132","a133","a134","a135","a136","a137","a138","a139","a140","a141","a142","a143","a144","a145","a146","a147","a148","a149","a150","a151","a152","a153","a154","a155","a156","a157","a158","a159","a160","a161","a162","a163","a164","a165","a166","a167","a168","a169","a170","a171","a172","a173","a174","a175","a176","a177","a178","a179","a180","a181","a182","a183","a184","a185","a186","a187","a188","a189","a190","a191","a192","a193","a194","a195","a196","a197","a198","a199","a200","a201","a202","a203","a204","a205","a206","a207","a208","a209","a210","a211","a212","a213","a214","a215","a216","a217","a218","a219","a220","a221","a222","a223","a224","a225","a226","a227","a228","a229","a230","a231","a232","a233","a234","a235","a236","a237","a238","a239","a240","a241","a242","a243","a244","a245","a246","a247","a248","a249","a250","a251","a252","a253","a254","a255"
template <int R> DI void ld_act(unsigned voff, const rsrc_t& rs, unsigned soff) {
  asm volatile("buffer_load_dwordx4 a[%c3:%c4], %0, %1, %2 offen offset:%5 sc1" ::"v"(voff), "s"(rs), "s"(soff), "i"(4 * R), "i"(4 * R + 3), "n"((R & 1) * 1024) : "memory");
}
template <int R, bool SW, bool FIRST> DI void mfa(f32x4& c, bf16x8 w) {
  if constexpr (FIRST && SW) asm volatile("v_mfma_f32_16x16x32_bf16 %0, a[%c2:%c3], %1, 0" : "=&v"(c) : "v"(w), "i"(4 * R), "i"(4 * R + 3));
  else if constexpr (FIRST) asm volatile("v_mfma_f32_16x16x32_bf16 %0, %1, a[%c2:%c3], 0" : "=&v"(c) : "v"(w), "i"(4 * R), "i"(4 * R + 3));
  else if constexpr (SW) asm volatile("v_mfma_f32_16x16x32_bf16 %0, a[%c2:%c3], %1, %0" : "+v"(c) : "v"(w), "i"(4 * R), "i"(4 * R + 3));
  else asm volatile("v_mfma_f32_16x16x32_bf16 %0, %1, a[%c2:%c3], %0" : "+v"(c) : "v"(w), "i"(4 * R), "i"(4 * R + 3));
}
DI void mfv(f32x4& d, const u32x4& a, const u32x4& b) { asm volatile("v_mfma_f32_16x16x32_bf16 %0, %1, %2, 0" : "=&v"(d) : "v"(a), "v"(b)); }
DI void mfma_fence() { GAP(); asm volatile("s_nop 7\n\ts_nop 1" ::: "memory"); GAP(); }
DI bf16x4 ldsT(unsigned addr) { return __builtin_amdgcn_ds_read_tr16_b64_v4i16((LDS_PTR(bf16x4))(uintptr_t)addr); }
DI bf16x8 cat(bf16x4 lo, bf16x4 hi) { return __builtin_shufflevector(lo, hi, 0, 1, 2, 3, 4, 5, 6, 7); }
DI u32x4 pack8(const f32x4& a, const f32x4& b) { return (u32x4){pk2(a[0], a[1]), pk2(a[2], a[3]), pk2(b[0], b[1]), pk2(b[2], b[3])}; }
DI void v_mul(float& d, float a, float b) { asm volatile("v_mul_f32 %0, %1, %2" : "=v"(d) : "v"(a), "v"(b)); }
DI void v_mulc(float& d, float a) { asm volatile("v_mul_f32 %0, 0xbfb8aa3b, %1" : "=v"(d) : "v"(a)); }
DI void v_exp(float& d, float a) { asm volatile("v_exp_f32 %0, %1" : "=v"(d) : "v"(a)); }
DI void v_fmac(float& d) { asm volatile("v_fma_f32 %0, %0, %1, %1" : "+v"(d) : "s"(-1.4426950408889634f)); }
DI void v_rcp(float& d) { asm volatile("v_rcp_f32 %0, %0" : "+v"(d)); }
DI unsigned v_cvt(float a, float b) { unsigned d; asm volatile("v_cvt_pk_bf16_f32 %0, %1, %2" : "=v"(d) : "v"(a), "v"(b)); return d; }

DI float fmax1(float a, float b) { return __builtin_amdgcn_fmed3f(a, b, __builtin_inff()); }
DI float xadd16(float v) { auto r = __builtin_amdgcn_permlane16_swap(__float_as_uint(v), __float_as_uint(v), false, false); return __uint_as_float(r[0]) + __uint_as_float(r[1]); }
DI float xadd32(float v) { auto r = __builtin_amdgcn_permlane32_swap(__float_as_uint(v), __float_as_uint(v), false, false); return __uint_as_float(r[0]) + __uint_as_float(r[1]); }
DI float add2(float u, float v) {
  auto r = __builtin_amdgcn_permlane16_swap(__float_as_uint(u), __float_as_uint(v), false, false);
  return xadd32(__uint_as_float(r[0]) + __uint_as_float(r[1]));
}
DI f32x2 spread2(float t) {
  auto r = __builtin_amdgcn_permlane16_swap(__float_as_uint(t), __float_as_uint(t), false, false);
  return (f32x2){__uint_as_float(r[0]), __uint_as_float(r[1])};
}
template <int CTRL> DI float dpp_add(float v) { return v + __uint_as_float(__builtin_amdgcn_update_dpp(0, __float_as_uint(v), CTRL, 0xf, 0xf, true)); }
DI f32x2 red8(const f32x2 (&a)[4]) {
  f32x2 o;
#pragma unroll
  for (int e = 0; e < 2; ++e) {
    auto r0 = __builtin_amdgcn_permlane16_swap(__float_as_uint(a[0][e]), __float_as_uint(a[1][e]), false, false);
    auto r1 = __builtin_amdgcn_permlane16_swap(__float_as_uint(a[2][e]), __float_as_uint(a[3][e]), false, false);
    const float u = __uint_as_float(r0[0]) + __uint_as_float(r0[1]), v = __uint_as_float(r1[0]) + __uint_as_float(r1[1]);
    auto r2 = __builtin_amdgcn_permlane32_swap(__float_as_uint(u), __float_as_uint(v), false, false);
    o[e] = dpp_add<0x128>(__uint_as_float(r2[0]) + __uint_as_float(r2[1]));
  }
  return o;
}
DI float row16_sum(float v) {
  v = dpp_add<0xB1>(v);
  v = dpp_add<0x4E>(v);
  v = dpp_add<0x141>(v);
  v = dpp_add<0x140>(v);
  return v;
}
DI f32x2 unpk(unsigned u) { return (f32x2){lo2f(u), hi2f(u)}; }
DI f32x2 rbf2(f32x2 v) { return (f32x2){rbf(v[0]), rbf(v[1])}; }
DI float ln_rstd(float var) { return __builtin_amdgcn_rcpf(rbf(__builtin_amdgcn_sqrtf(rbf(rbf(var) + 1e-5f)))); }
DI float silu_bf(u16 c) { const float x = bf2f(c); return rbf(x * __builtin_amdgcn_rcpf(1.f + __builtin_amdgcn_exp2f(x * -1.4426950408889634f))); }
DI unsigned umax4(const u32x4& v) { return max(max(v[0], v[1]), max(v[2], v[3])); }
DI unsigned umax16(const u32x4 (&v)[4]) { return max(max(umax4(v[0]), umax4(v[1])), max(umax4(v[2]), umax4(v[3]))); }
template <class T> DI T lds_ld(unsigned a) { return *(const LDS_PTR(T))(uintptr_t)a; }
template <class T> DI void lds_st(unsigned a, const T& v) { *(LDS_PTR(T))(uintptr_t)a = v; }
DI u32x4 swz_rows(const u32x4& o) {
  auto r0 = __builtin_amdgcn_permlane16_swap(o[0], o[2], false, false);
  auto r1 = __builtin_amdgcn_permlane16_swap(o[1], o[3], false, false);
  return (u32x4){r0[0], r1[0], r0[1], r1[1]};
}

constexpr unsigned W_H1 = 1u << 16, W_O = W_H1 + (4u << 20), W_X1 = W_O + (4u << 20), W_H2 = W_X1 + (4u << 20), W_HH = W_H2 + (4u << 20);
constexpr unsigned W_PART = 32u << 20, W_LATE = W_PART + (128u << 10), W_LN1 = W_LATE + (128u << 10), W_FLAGS = W_LN1 + (32u << 10), W_STATS = W_FLAGS + (128u << 10), W_HALO = W_STATS + (256u << 10);
constexpr unsigned W_XCH = 34u << 20, W_END = 42u << 20;
enum { PH_LN1 = 0, PH_G1 = 2, PH_G2, PH_G3, PH_G4 };
constexpr unsigned POISON = 0xffffffffu;
constexpr int AUX_SC1 = 16;
constexpr unsigned SPIN_CAP = 1u << 17;
constexpr int SLOT = 16384, L_KV = 8 * SLOT, L_WV = L_KV + 20480, L_RED = L_WV + 4096, LDS_TOTAL = 163840;

#define STAMP()
#define STAMP0()
#define FST()
DI int opaque_tid() { int t = threadIdx.x; asm volatile("" : "+v"(t)); return t; }
DI int opaque_bid() { int b = blockIdx.x; asm volatile("" : "+s"(b)); return b; }

DI int panel(int b) { return ((b >> 2) & 1) * 8 + (b >> 5); }
DI void map_g1(int b, int& tm, int& tn) { tm = panel(b); tn = (b & 3) * 4 + (((b >> 3) + 3) & 3); }
DI void map_g2(int b, int& tm, int& tn) { tm = panel(b); tn = (b & 2) * 4 + ((b >> 3) & 3) * 2 + (b & 1); }
DI void map_g3(int b, int& tm, int& tn) { tm = panel(b); tn = (b & 3) * 4 + ((b >> 3) & 3); }

struct Ctx {
  const u16 *x, *wqkv, *wo, *w1, *w3, *w2, *cc, *wmod; u16* out; char* ws;
  rsrc_t rs, rC, rQ, rO, r1, r3, r2, rM;
  f32x4 acc[13][2][2][2];
  bf16x4 wl[8][2], wh[8][2];
  float t[8]; u32x4 o;
  unsigned bW[2][2], bM[2][2], vA, vK[4], v1[4], v2[4], v3[4], vM[4];
  unsigned lds0, sBB[2], sAA[2], sA0, mB, so[2], sB4[2][2], sA4[2], fl3, fl4, xs4;
  int wave; unsigned long long pc0;
  u32x4 kf[2][2], vf[2][2], hl[4]; unsigned kvw, hso, hst;
  u32x4 xr[2][2], lw[8]; float ls[8]; f32x2 ga[4];
  u32x4 mp[2][4]; u32x2 mv[12]; unsigned mo, tag[2]; f32x2 eg[2][4], er[2][2][4], sc1[8], sh[8];
  unsigned ck, cw[2], zo, pollv, pollo, flv, tch, vT1, vT2, vT3, vT4, vT5, vC, vG0; u32x4 xl[8];
};
DI void st16(const Ctx& c, unsigned off, const u32x4& v) { __builtin_amdgcn_raw_buffer_store_b128(v, c.rs, off, 0, AUX_SC1); }
DI void ld16x4(const Ctx& c, u32x4 (&v)[4], unsigned off, unsigned step) {
  asm volatile("buffer_load_dwordx4 %0, %4, %5, 0 offen sc1\n\tbuffer_load_dwordx4 %1, %4, %5, %6 offen sc1\n\tbuffer_load_dwordx4 %2, %4, %5, %7 offen sc1\n\tbuffer_load_dwordx4 %3, %4, %5, %8 offen sc1\n\ts_waitcnt vmcnt(0)"
               : "=&v"(v[0]), "=&v"(v[1]), "=&v"(v[2]), "=&v"(v[3]) : "v"(off), "s"(c.rs), "s"(step), "s"(2 * step), "s"(3 * step) : "memory");
}
DI void retry(const Ctx& c, u32x4 (&v)[4], unsigned off, unsigned step) {
  if (__builtin_expect(__any(umax16(v) == POISON), 0)) {
#pragma unroll 1
    for (unsigned spins = 0; spins < SPIN_CAP; ++spins) { ld16x4(c, v, off, step); if (!__any(umax16(v) == POISON)) break; __builtin_amdgcn_s_sleep(1); }
  }
}
DI u32x4 granule(const Ctx& c, float a, float b) { return (u32x4){__float_as_uint(a), c.tag[0], __float_as_uint(b), c.tag[1]}; }
DI bool tagged(const Ctx& c, const u32x4 (&v)[4]) {
  const unsigned d = ((v[0][1] ^ c.tag[0]) | (v[1][1] ^ c.tag[0]) | (v[2][1] ^ c.tag[0]) | (v[3][1] ^ c.tag[0])) | ((v[0][3] ^ c.tag[1]) | (v[1][3] ^ c.tag[1]) | (v[2][3] ^ c.tag[1]) | (v[3][3] ^ c.tag[1]));
  return __all(d == 0);
}
DI unsigned flag_off(int tm, int wave, int col, int ph) { return W_FLAGS + ((tm * 4 + wave) * 16 + col) * 128 + ph * 4; }
DI void set_flag(const Ctx& c, unsigned off, unsigned val) {
  asm volatile("s_mov_b64 exec, 1\n\tbuffer_store_dword %0, off, %1, %2 sc1\n\ts_mov_b64 exec, -1" ::"v"(val), "s"(c.rs), "s"(off) : "memory");
}
DI void spin(const Ctx& c, unsigned voff, unsigned need) {
#pragma unroll 1
  for (unsigned spins = 0; spins < SPIN_CAP; ++spins) {
    if (__all(__builtin_amdgcn_raw_buffer_load_b32(c.rs, voff, 0, AUX_SC1) >= need)) break;
    __builtin_amdgcn_s_sleep(1);
    asm volatile("" ::: "memory");
  }
  asm volatile("" ::: "memory");
}
template <int PH> DI void await(const Ctx& c, unsigned line) {
#pragma unroll 1
  for (unsigned spins = 0; spins < SPIN_CAP; ++spins) {
    const u32x4 v = __builtin_amdgcn_raw_buffer_load_b128(c.rs, line, 0, AUX_SC1);
    if (__all(v[0] == c.tag[0] && v[1] == c.tag[1] && (PH == PH_LN1 || v[PH] != 0))) break;
    __builtin_amdgcn_s_sleep(1);
    asm volatile("" ::: "memory");
  }
  asm volatile("" ::: "memory");
}
#define L4 7
#define NP3 7
#define PRE3 4
#define QW 16
#define HP4 11
#define LG 5
#define DENSE 20
#define PAIR 16
#define UP0 73
#define UP1 93
#define NFD 12
#define NF2 4
#define XD1 12
#define XD2 8
#define XD3 10
namespace en {
constexpr int RD = 4, NT = 116, NH = 8 * NT, T3 = 32, T4 = 88, NB4 = 14 - L4;
constexpr int seg_t0(int s) { return s == 1 ? 0 : s == 2 ? 24 : s == 3 ? T3 : NT; }
constexpr int seg_of(int H) { return H < 192 ? 1 : H < 256 ? 2 : 3; }
constexpr int seg_end(int H) { return 8 * seg_t0(seg_of(H) + 1); }
constexpr int seg_np(int s) { return s == 1 ? 8 : s == 2 ? 8 : NP3; }
constexpr int seg_pre(int s) { return s == 3 ? PRE3 : 8; }
enum { U_K, U_V, U_Q, U_O, U_G0, U_U0, U_G1, U_U1, U_M, U_G2, U_U2, U_S, U_P };
constexpr int unit(int T) {
  if (T >= T4) { const int s = T - T4; return U_S + (s < 2 * NB4 ? s & 1 : s >= 2 * NB4 + L4); }
  return T < 32 ? T >> 3 : T < 64 ? U_G0 + ((T - 32) >> 4) * 2 + (T & 1) : T < 72 ? U_M : T - 72 < LG || (T - 72 < 16 - LG && ((T - 72 - LG) & 1)) ? U_G2 : U_U2;
}
constexpr int grp(int T) {
  if (T >= T4) { const int s = T - T4; return s < 2 * NB4 ? s / 2 : s < 2 * NB4 + L4 ? s - NB4 : s - NB4 - L4; }
  return T < 32 ? T & 7 : T < 64 ? (T >> 1) & 7 : T < 72 ? T & 7 : T - 72 < LG ? T - 72 : T - 72 >= 16 - LG ? T - 80 : (T - 72 - LG) / 2 + (unit(T) == U_G2 ? LG : 0);
}
constexpr int u2stage(int j) { int T = 72; while (unit(T) != U_U2 || grp(T) != j) ++T; return T; }
constexpr int g4first(int j) { return T4 + (j < NB4 ? 2 * j : j + NB4); }
constexpr int g4last(int j) { return T4 + (j < NB4 ? 2 * j + 1 : j + NB4 + L4); }
constexpr int g4kb(int j) { return j < 8 ? 32 * (j / 4) + 4 * (j % 4) : j < 10 ? 96 + 4 * (j - 8) : 64 + 4 * (j - 10); }
constexpr bool g4mix(int j) { return j == 8 || j == 9; }
constexpr bool barst(int T) { return T < NT - 1 && ((T & 1) || (T >= 6 && T < 24) || (T >= 46 && T < 76) || (T > 103 && T < 110)); }
constexpr bool isbar(int H) { return (H & 7) == 8 - RD && barst(H >> 3); }
constexpr int cert(int T) { int b = T - 1; while (!barst(b)) --b; return 8 * b + 8 - RD; }
enum { K_A = 1, K_D, K_A4, K_P, K_XS, K_XL, K_T, K_C, K_G, K_Z };
enum { A_CHK1 = 1, A_CHK2, A_CHK3, A_CHK4, A_FLAG, A_FLAG4, A_CHKX, A_CK = 16 };
enum { P_K, P_GA, P_V, P_GB, P_GC, P_GD, P_GR, P_PREV, P_GO, P_UA, P_UB, PR_HH };
constexpr int P_FA = PR_HH + 28, NPR = P_FA + 1, HH_ST = 13;
constexpr bool pr_up(int i) { return i - PR_HH < 20 ? (i - PR_HH) & 1 : i - PR_HH >= 24; }
constexpr int pr_blk(int i) { return i - PR_HH < 20 ? (i - PR_HH) / 2 : 10 + (i - PR_HH) % 4; }
constexpr int pr_avail(int i) {
  const int a[PR_HH] = {64, 106, 128, 0, 146, 0, 0, 165, 179, 192, 0}, n = i - PR_HH;
  if (i == P_FA) return NH - 56;
  return n < 0 ? a[i] : 8 * T3 + (n < 8 ? 128 : n < 16 ? 256 : n < 20 ? 320 : n < 24 ? 448 - 8 * LG : 448) - (n < 16 && !pr_up(i) ? 8 : 0);
}
constexpr int pr_len(int i) { return i == P_FA ? 56 : i == P_K || i == P_V ? 32 : i == P_PREV || i == P_GR || i == P_GO ? 1 : i == P_UA ? 64 : i == P_UB ? 48 : i < PR_HH ? 32 : pr_up(i) ? HH_ST + 1 : 40; }
constexpr bool noflag(int s) { return s == 3; }
constexpr int E_CK = 8192;
enum { S_M0 = 1, S_SB, S_SA, S_SA4 };
struct Plan { short sc[4 * NH], nr[4 * NH], op[NH + 1], e[4 * NH], act[NH + 1], vw[NH + 1], d0[5], d1[5], slack, halo; };
constexpr Plan mk() {
  Plan p{};
  int atA[64] = {}, atD[4 * NT] = {}, atA4[112] = {}, req[NH + 1] = {}, nvm[NH + 1] = {};
  bool quiet[NH + 64] = {};
  for (int H = 0; H <= NH; ++H) req[H] = -1000;
  auto need = [&](int H, int x) { if (x > req[H]) req[H] = x; };
  auto window = [&](int H, int o0, int o1 = 0, int o2 = 0, int o3 = 0, int o4 = 0, int o5 = 0, int o6 = 0) {
    const int o[7] = {o0, o1, o2, o3, o4, o5, o6};
    for (int i = 0; i < 7 && o[i]; ++i) p.op[H++] = o[i];
    for (int i = 0; i < QW && seg_of(H + i) == seg_of(H - 1); ++i) quiet[H + i] = true;
  };
  auto poll = [&](int H, int a) { const int l = a == A_CHK4 ? 16 : 24; p.op[H - l] = K_P * 1024; p.act[H] = a; need(H, H - l); };
  constexpr int T = K_T * 1024, G = K_G * 1024, C = K_C * 1024, H3 = 8 * T3, H4 = 8 * T4;
  window(84, G, G + 1, G + 2, G + 3, T + 1); window(120, G + 4, G + 5, G + 6, G + 7, T + 2); window(155, C, T, T + 3, T + 4); p.op[190] = C + 1;
  need(108, 87); need(148, 123);
  window(H3 + 146, C, T + 7, T + 8, T + 9); window(H3 + 267, T + 5, T + 6); window(H3 + 308, C, T + 10, T + 11, T + 12);
  p.op[58] = T + 23; p.op[210] = T + 17; p.op[211] = T + 18; p.op[H3 + 67] = T + 19; p.op[H3 + 75] = T + 20;
  p.op[H3 + 366] = T + 13; p.op[H3 + 430] = T + 14; p.op[H3 + 496] = T + 16; p.op[H3 + 460] = C; p.op[H3 + 506] = T + 15; p.op[H3 + 546] = T + 16; p.op[H3 + 548] = T + 15;
  for (int i = 0; i < 4; ++i) p.op[98 + i] = K_Z * 1024 + i;
  const int chk[4] = {H3 + 360, H3 + 438, H4 + 42, H4 + 8 * HP4 + 16}, hx0 = 8 * g4first(13) + 10;
  for (int i = 0; i < 4; ++i) poll(chk[i], A_CHK1 + i);
  int nd = 0, n4 = 0, nxs = 0, nxl = 0, hchk = 100000;
  p.slack = 1000;
  for (int s = 1; s <= 3; ++s) {
    const int T0 = seg_t0(s), T1 = seg_t0(s + 1), dbl = s == 3;
    int na = 2 * seg_pre(s), lim = T0 + 7;
    p.d0[s] = nd; if (nd < 4 * (T0 + seg_np(s))) nd = 4 * (T0 + seg_np(s)); p.d1[s] = nd;
    for (int n = 0; n < na; ++n) atA[n] = 8 * T0 - na + n;
    auto useA = [&](int n) { return 8 * T0 + (n >> 3) * (8 << dbl) + (n & 6) - (!noflag(s) ? 0 : n < 8 ? 2 : n < 2 * 8 ? 8 : 16 - (n & 6)); };
    for (int H = 8 * T0; H < 8 * T1; ++H) {
      if (isbar(H)) lim = (H >> 3) + 7;
      if (p.op[H] || quiet[H]) continue;
      if (H >= hx0 && nxs < 8) {
        p.op[H] = K_XS * 1024 + nxs++;
        if (nxs == 8) {
          const int hf = (H + XD1 + 1) & ~1; hchk = (hf + XD2 + XD3 + 1) & ~1;
          p.act[hf] = A_FLAG4; need(hf, H); p.op[hf + XD2] = K_P * 1024 + 1; p.act[hchk] = A_CHKX; need(hchk, hf + XD2);
        }
        continue;
      }
      if (H >= hchk && nxl < 8) { p.op[H] = K_XL * 1024 + nxl++; continue; }
      if (s == 3 && (H & 7) == 3 && H - 8 * T0 > 8 && H < H4 - 8 * LG - 40) continue;
      const int Td = nd >> 2, j = n4 >> 3;
      const bool dok = Td < NT && Td <= lim && Td < T1 + 8;
      const int dd = !dok ? 100000 : Td < T1 ? cert(Td) : 8 * T1 + 2 * (Td - T1);
      int da = 100000;
      if (na < 64) da = useA(na);
      else if (n4 < 112 && H >= 8 * (j < 8 ? u2stage(j) : g4last(j - 8)) + (n4 & 6) + 2 && H >= chk[j < 8 ? j >> 2 : j < 10 ? 2 : 3]) da = cert(g4first(j));
      if (da <= dd && da < 100000) { if (na < 64) { atA[na] = H; p.op[H] = K_A * 1024 + na++; } else { atA4[n4] = H; p.op[H] = K_A4 * 1024 + n4++; } }
      else if (dok && (H >= 8 * (Td - (barst(Td - 7) && barst(Td - 8) ? 7 : 6)) + 4 + 2 * (nd & 3) || da < 100000)) { atD[nd] = H; p.op[H] = K_D * 1024 + nd++; }
    }
    if (nd < 4 * T1 || na < 64) p.slack = -5;
    for (int n = noflag(s) ? 3 : 1; n < 64; n += 2) need(useA(n), atA[n]);
    if (noflag(s)) { for (int g = 1; g < 8; ++g) p.act[8 * T0 + 16 * g] = A_CK + g; for (int k = 1; k < 4; ++k) p.act[8 * T0 + 2 * k] = A_CK; }
    for (int t = p.d1[s] >> 2; t < T1; ++t) need(cert(t), atD[4 * t + 3]);
  }
  for (int j = 0; j < 14; ++j) need(cert(g4first(j)), atA4[8 * j + 7]);
  if (n4 < 112 || nxl < 8 || nd < 4 * NT) p.slack = -1;
  int used[4 * NH] = {};
  for (int q = 0; q < 4 * NH; ++q) { p.nr[q] = (q >> 2) + RD < seg_end(q >> 2); used[q] = p.nr[q]; }
  for (int H = 0; H < NH; ++H) {
    if (p.op[H]) { nvm[H]++; used[4 * H] = 3; if ((H < 8 * UP0 || H >= 8 * UP1) && seg_of(H) != 2 && !(H >= 136 && H < 160)) { p.nr[4 * H + 1] += p.nr[4 * H]; p.nr[4 * H] = 0; used[4 * H + 1] = p.nr[4 * H + 1]; } }
    if ((H & 1) && H + 1 < seg_end(H)) { used[4 * H + 3]++; }
  }
  for (int H = 0; H < NH; ++H) {
    const int k = p.op[H] / 1024, i = p.op[H] % 1024, h0 = 8 * seg_t0(seg_of(H));
    auto put = [&](int code, int back) {
      for (int q = 4 * H - 1; q >= 4 * (H - back) + 1 && q >= 4 * h0; --q) if (used[q] < 2 && !p.sc[q]) { p.sc[q] = code; used[q]++; return; }
      for (int q = 4 * H - 1; q >= 4 * (H - back) + 1 && q >= 4 * h0; --q) if (!p.sc[q]) { p.sc[q] = code; used[q]++; return; }
      p.slack = -8;
    };
    if (k == K_D && !(i & 3)) { put(S_M0 * 1024 + (i >> 2), 1); put(S_SB * 1024 + (i >> 2), 3); }
    if (k == K_A && !(i & 7) && H > h0) put(S_SA * 1024 + (i >> 3), 3);
    if (k == K_A4 && !(i & 7)) put(S_SA4 * 1024 + (i >> 3), 3);
  }
  int pr = 0, ix = 0;
  for (int q = 0; q < 4 * NH; ++q) p.e[q] = -1;
  for (int s = 1; s <= 3; ++s) if (noflag(s)) for (int ks = 1; ks < 32; ++ks) {
    const int w = ks < 8 ? 2 : 1, nq = ks < 8 ? 7 : 15, no = 6 / w;
    const int q0 = 4 * (8 * seg_t0(s) + (ks >> 2) * 16 + 2 * (ks & 3) - (ks < 4 ? 2 : ks < 8 ? 8 : 16 - 2 * (ks & 3)));
    int fr = 0, st = 0;
    for (int q = q0; q < q0 + nq; ++q) fr += used[q] < 2;
    for (int q = q0; q < q0 + nq && st < no; ++q) if (used[q] < 2 || (used[q] == 2 && fr < no - st)) { fr -= used[q] < 2; p.e[q] = E_CK + 8 * ks + w * st++; used[q] = 3; }
    st = st * w / 2;
    if (st < 3) p.slack = -9;
  }
  for (int q = 0; q < 4 * NH; ++q) {
    const int H = q >> 2;
    if (pr == NPR || q < 4 * pr_avail(pr) + 8) continue;
    const int n = pr >= P_FA ? -1 : pr - PR_HH;
    const bool st = n >= 0 && pr_up(pr) && ix == HH_ST, big = st || (n >= 0 && !pr_up(pr) && ((ix >> 3) & 1)), dense = n >= DENSE;
    if (st && (q & 7) == 7) continue;
    if (big && (n < PAIR || st) && p.nr[q]) {
      if (used[q] == 1 && (q & 3) < 3 && used[q + 1] == 1 && p.nr[q + 1] == 1) { p.nr[q] = 0; p.nr[q + 1] = 2; used[q + 1] = 2; }
      else if (used[q] >= 2) continue;
    } else if (used[q] >= 2 && !(dense && used[q] == 2 && p.nr[q] == 1 && !((q & 3) == 3 && isbar(H + 1)))) continue;
    p.e[q] = pr * 64 + ix;
    if (pr == P_PREV) p.halo = H;
    if ((pr == P_GR && H > 175) || (pr == P_PREV && H > 182) || (pr == P_GO && H > 191) || (pr == P_V && H > 149) || (pr == P_UB && H > 255)) p.slack = -6;
    if (st) {
      nvm[H]++;
      if (n == 7 || n == 15 || n == 19 || n == 27) {
        int hb = H + (n == 27 ? 22 : 30); while (!isbar(hb) || p.act[hb]) ++hb;
        p.act[hb] = A_FLAG; need(hb, H);
        if (hb + 16 > chk[n == 7 ? 0 : n == 15 ? 1 : n == 19 ? 2 : 3] - (n == 27 ? 16 : 24)) p.slack = -3;
      }
    }
    if (++ix == pr_len(pr)) { ix = 0; ++pr; }
  }
  if (pr < NPR) p.slack = -2;
  for (int H = 0; H < NH; ++H) {
    int n = -1;
    if (req[H] != -1000) {
      const int h0 = 8 * seg_t0(seg_of(H));
      n = req[H] < h0 ? h0 - 1 - req[H] : 0;
      for (int h = req[H] < h0 ? h0 : req[H] + 1; h < H; ++h) n += nvm[h];
      if (req[H] >= h0 && H - req[H] < p.slack) p.slack = H - req[H];
    }
    p.vw[H] = n > 63 ? 63 : n;
  }
  { int n = 0; for (int h = p.halo + 1; h < 192; ++h) n += nvm[h]; p.halo = n; }
  return p;
}
constexpr Plan PL = mk();
static_assert(PL.slack >= 7, "every load is issued well before it is needed");

#define PGTAB 53, 267, 470, 659, 890, 1125, 1311, 1499, 1696, 1894, 2092, 2286, 2475, 2665, 2838, 3025, 3239, 3452, 3657
constexpr int PGS[] = {PGTAB}, NPG = sizeof(PGS) / sizeof(int);
constexpr int pg_of(int q) { for (int i = 0; i < NPG; ++i) if (PGS[i] == q) return i; return -1; }
constexpr int pg_grp(int i) { return i < NPG && PGS[i] >= 0 ? PGS[i] >= 32 * T3 : -1; }
constexpr int pg_first(int g) { int i = 0; while (i < NPG && pg_grp(i) != g) ++i; return i; }
constexpr int pg_lab(int i, int g) { return pg_grp(i) == g ? i : 1000 + g; }
template <int I> DI void pg_mark() { asm volatile(".Lpg%c0:\n\ts_cbranch_execz .Lpg%c1" ::"i"(I), "i"(pg_lab(I + 4, pg_grp(I)))); }
template <int G> DI void pg_hop(int wave) {
  constexpr int f = pg_first(G);
  asm volatile("s_mov_b64 exec, 0\n\ts_cmp_eq_u32 %0, 0\n\ts_cbranch_scc1 .Lpg%c1\n\ts_cmp_eq_u32 %0, 1\n\ts_cbranch_scc1 .Lpg%c2\n\ts_cmp_eq_u32 %0, 2\n\ts_cbranch_scc1 .Lpg%c3\n\ts_branch .Lpg%c4\n"
               ".Lpg%c5:\n\ts_mov_b64 exec, -1" ::"s"(wave), "i"(pg_lab(f, G)), "i"(pg_lab(f + 1, G)), "i"(pg_lab(f + 2, G)), "i"(pg_lab(f + 3, G)), "i"(1000 + G) : "scc", "memory");
}
template <int H, int R> DI void rd(Ctx& c) {
  constexpr bool mx = unit(H >> 3) == U_M;
  constexpr int cb = R >> 1, hi = R & 1, off = ((H >> 3) & 7) * SLOT + ((H >> 1) & 3) * 4096 + (H & 1) * (mx ? 1024 : 256) + hi * 2048;
  const unsigned ad = (mx ? c.bM[cb][off >> 16] : c.bW[cb][off >> 16]) + (off & 65535);
  if constexpr (hi) c.wh[H & 7][cb] = ldsT(ad); else c.wl[H & 7][cb] = ldsT(ad);
}
template <int T> DI void dma_off(Ctx& c) {
  constexpr int u = unit(T), j = grp(T);
  unsigned& sB = c.sBB[T & 1];
  if constexpr (u <= U_Q) sB = j * 128 * 6144 + (u + 1) % 3 * 2048;
  else if constexpr (u == U_O) sB = c.sB4[0][0] + j * 128 * 2048;
  else if constexpr (u < U_S) sB = j * 128 * F_ * 2 + (u == U_M ? 0 : u >= U_G2 ? 4096 : u >= U_G1 ? 2048 : 0);
  else sB = c.sB4[g4mix(j)][u - U_S] + g4kb(j) * 32 * 2048;
  pins(sB);
}
template <int D> DI void dma(Ctx& c) {
  constexpr int u = unit(D >> 2), i = D & 3;
  if constexpr (u <= U_Q) bdma<i * 1024>(c.v1[i], c.rQ, c.sBB[(D >> 2) & 1]);
  else if constexpr (u == U_O) bdma<i * 1024>(c.v2[i], c.rO, c.sBB[(D >> 2) & 1]);
  else if constexpr (u == U_M) bdma<i * 1024>(c.vM[i], (i & 1) ? c.r3 : c.r1, c.sBB[(D >> 2) & 1]);
  else if constexpr (u < U_S) bdma<i * 1024>(c.v3[i], u == U_U0 || u == U_U1 || u == U_U2 ? c.r3 : c.r1, c.sBB[(D >> 2) & 1]);
  else bdma<i * 1024>(c.v2[i], c.r2, c.sBB[(D >> 2) & 1]);
}
template <int D> DI void dma1(Ctx& c) {
  if constexpr (!(D & 3)) { dma_off<(D >> 2)>(c); m0_add<((D >> 2) & 7) * SLOT>(c.mB); nops<1>(); }
  dma<D>(c);
}
template <int B, int... Ds> DI void dmas_(Ctx& c, iseq<Ds...>) { (dma1<B + Ds>(c), ...); }
template <int S, int A, int B> DI void dmas(Ctx& c) {
  constexpr int n = PL.d1[S] - PL.d0[S], a = A < n ? A : n, b = B < n ? B : n;
  GAP(); dmas_<PL.d0[S] + a>(c, make_iseq<b - a>{}); GAP();
}
template <int S, int B, int E, int... Is> DI void dma_at_(Ctx& c, int n, iseq<Is...>) { ((n == Is && B + Is < E ? dmas<S, B + Is, B + Is + 1>(c) : void()), ...); }
template <int S, int B, int E> DI void dma_at(Ctx& c, int n) { dma_at_<S, B, E>(c, n, make_iseq<16>{}); }
constexpr int NFREE3 = 4 * 39 - PL.d0[3];

template <int N, bool UP, int I> DI void e_hh(Ctx& c) {
  constexpr bool mx = N == 8 || N == 9;
  constexpr int n = N < 8 ? N : mx ? N - 8 : N - 2, S = (n >> 1) & 1, B = n & 1, ug = mx ? U_M : N < 8 ? U_G0 + (N >> 2) * 2 : U_G2, e = I & 7, ph = I >> 3;
  f32x4& g = c.acc[ug][S][B][e >> 2];
  float r;
  if constexpr (UP) {
    if constexpr (I < 8) v_mul(c.t[e], g[e & 3], c.acc[mx ? U_M : ug + 1][mx ? 1 : S][B][e >> 2][e & 3]);
    else if constexpr (I < 12) c.o[e] = v_cvt(c.t[2 * e], c.t[2 * e + 1]);
    else if constexpr (I == HH_ST) asm volatile("buffer_store_dwordx4 %0, %1, %2, %3 offen offset:%4 sc1" ::"v"(c.o), "v"(c.vA), "s"(c.rs), "s"(c.so[mx] + (mx ? 96 : (n >> 2) * 32 + S) * KBS), "n"(B * 1024) : "memory");
  } else if constexpr (ph == 0) { v_mulc(r, g[e & 3]); g[e & 3] = r; }
  else if constexpr (ph == 1) v_exp(c.t[e], g[e & 3]);
  else if constexpr (ph == 2) v_fmac(c.t[e]);
  else if constexpr (ph == 3) v_rcp(c.t[e]);
  else { v_mul(r, g[e & 3], c.t[e]); g[e & 3] = r; }
}
template <int U, int F> DI void e_kv(Ctx& c) {
  u32x4 (&d)[2][2] = U ? c.vf : c.kf;
  if constexpr (F < 8 || F >= 24) { constexpr int B = F < 8; const f32x4& a = c.acc[U][(F >> 2) & 1][B][(F >> 1) & 1]; d[(F >> 2) & 1][B][F & 3] = v_cvt(a[2 * (F & 1)], a[2 * (F & 1) + 1]); }
  else if constexpr (F < 16) { unsigned r = d[(F >> 2) & 1][1][F & 3]; asm volatile("v_min_u32 %0, -2, %0" : "+v"(r)); d[(F >> 2) & 1][1][F & 3] = r; }
  else if constexpr (F == 17 || F == 19) asm volatile("buffer_store_dwordx4 %0, %1, %2, %3 offen offset:%4 sc1" ::"v"(d[F == 19][1]), "v"(c.vA), "s"(c.rs), "s"(c.hst), "n"(U * 2048 + (F == 19) * 1024) : "memory");
  else if constexpr (F == 20 || F == 23) lds_st(c.kvw + 4096 + U * 2048 + (F == 23) * 1024, d[F == 23][1]);
}
DI void e_prev(Ctx& c) {
  asm volatile("s_cmp_eq_u32 %4, 0\n\ts_cbranch_scc1 1f\n\tds_read_b128 %0, %5\n\tds_read_b128 %1, %5 offset:1024\n\tds_read_b128 %2, %5 offset:2048\n\tds_read_b128 %3, %5 offset:3072\n\ts_branch 2f\n"
               "1:\n\tbuffer_load_dwordx4 %0, %6, %7, %8 offen sc1\n\tbuffer_load_dwordx4 %1, %6, %7, %8 offen offset:1024 sc1\n\tbuffer_load_dwordx4 %2, %6, %7, %8 offen offset:2048 sc1\n\tbuffer_load_dwordx4 %3, %6, %7, %8 offen offset:3072 sc1\n2:"
               : "=&v"(c.hl[0]), "=&v"(c.hl[1]), "=&v"(c.hl[2]), "=&v"(c.hl[3]) : "s"(c.wave), "v"(c.kvw), "v"(c.vA), "s"(c.rs), "s"(c.hso < W_HALO ? W_END : c.hso) : "scc", "memory");
}
DI void gemv_out(const Ctx& c, unsigned off, int lane) {
  const unsigned rb = c.lds0 + L_RED + (lane & 31) * 8;
  const f32x2 sum = (lds_ld<f32x2>(rb) + lds_ld<f32x2>(rb + 256)) + (lds_ld<f32x2>(rb + 512) + lds_ld<f32x2>(rb + 768));
  if (lane < 32) st16(c, off + lane * 16, granule(c, sum[0], sum[1]));
}
template <int P, int I> DI void e_gv(Ctx& c) {
  if constexpr (P == P_GR) lds_st(c.lds0 + L_RED + c.wave * 256 + ((c.vA >> 4) & 7) * 32 + (c.vA >> 8) * 8, red8(c.ga));
  else if constexpr (P == P_GO) { if (c.wave == 3) gemv_out(c, W_LATE + blockIdx.x * 512, c.vA >> 4); }
  else {
    constexpr int r = 2 * (P == P_GA ? 0 : P == P_GB ? 1 : P == P_GC ? 2 : 3) + (I >> 4), e = I & 7;
    if constexpr (!(I & 8) && (e & 1)) asm volatile("v_and_b32 %0, 0xffff0000, %1" : "=v"(c.t[e]) : "v"(c.lw[r][e >> 1]));
    else if constexpr (!(I & 8)) asm volatile("v_lshlrev_b32 %0, 16, %1" : "=v"(c.t[e]) : "v"(c.lw[r][e >> 1]));
    else {
      float a = r ? c.ga[e >> 1][e & 1] : 0.f;
      if constexpr (r == 0) v_mul(a, c.t[e], c.ls[r]); else asm volatile("v_fmac_f32 %0, %1, %2" : "+v"(a) : "v"(c.t[e]), "v"(c.ls[r]));
      c.ga[e >> 1][e & 1] = a;
    }
  }
}
template <int I> DI void e_un(Ctx& c) {
  auto un = [](unsigned u) { float r; if constexpr (I & 1) asm volatile("v_and_b32 %0, 0xffff0000, %1" : "=v"(r) : "v"(u)); else asm volatile("v_lshlrev_b32 %0, 16, %1" : "=v"(r) : "v"(u)); return r; };
  constexpr int j = (I & 15) >> 1;
  if constexpr (I < 32) c.er[I >> 4][(I >> 3) & 1][(I >> 1) & 3][I & 1] = un(c.xr[I >> 4][(I >> 3) & 1][(I >> 1) & 3]);
  else if constexpr (I < 48) c.eg[j >> 2][j & 3][I & 1] = un(c.mv[j >> 1][j & 1]);
  else if constexpr (I < 64) c.sh[j][I & 1] = un(c.mv[4 + (j >> 1)][j & 1]);
  else if constexpr (I < 80) c.sc1[j][I & 1] = un(c.mv[8 + (j >> 1)][j & 1]);
  else {
    float r = c.sc1[j][I & 1];
    if constexpr (I < 96) asm volatile("v_add_f32 %0, 1.0, %0" : "+v"(r)); else asm volatile("v_cvt_pk_bf16_f32 %0, 0, %0" : "+v"(r));
    c.sc1[j][I & 1] = r;
  }
}
template <int P, int I> DI void e_fin(Ctx& c) {
  if constexpr (P == 0 && I < 4) c.mv[I] = lds_ld<u32x2>(c.lds0 + L_WV + c.wave * 1024 + 384 + I * 32 + (c.vA >> 8) * 8);
  else if constexpr (P == 0 && I >= 8) e_un<I - 8>(c);
  else if constexpr (P == 1) {
    constexpr int rb = I >> 4, s = (I >> 3) & 1, j = (I >> 1) & 3;
    float r = c.er[rb][s][j][I & 1];
    asm volatile("v_fmac_f32 %0, %1, %2" : "+v"(r) : "v"(c.eg[s][j][I & 1]), "v"(c.xl[4 * s + 2 * rb + (j >> 1)][I & 3]));
    c.er[rb][s][j][I & 1] = r;
  }
}
template <int... Is> DI void fin_(Ctx& c, iseq<Is...>) { (e_fin<1, Is>(c), ...); }
DI void touch(Ctx& c, unsigned v, const rsrc_t& r, unsigned o) { asm volatile("buffer_load_dword %0, %1, %2, %3 offen" : "+v"(c.tch) : "v"(v), "s"(r), "s"(o) : "memory"); }
template <int W> DI void warm(Ctx& c) {
  if constexpr (W == 0) touch(c, c.vT2, c.rO, 0);
  else if constexpr (W < 5) touch(c, c.vT3 | (c.vA >= 256 ? 0x40000000u : 0u), (W & 1) ? c.r1 : c.r3, (W - 1) / 2 * 2048);
  else if constexpr (W < 7) touch(c, c.vT3, (W & 1) ? c.r1 : c.r3, (W - 1) / 2 * 2048);
  else if constexpr (W < 9) touch(c, c.vT4, (W & 1) ? c.r1 : c.r3, 0);
  else if constexpr (W < 13) touch(c, c.vT5, c.r2, (W - 9) * (2 << 20));
  else if constexpr (W < 17) touch(c, c.vT2, c.r2, (W - 13) * (2 << 20));
  else if constexpr (W == 23) touch(c, c.vT1, c.rQ, 0);
  else touch(c, c.vT3 | (c.vA < 256 ? 0x40000000u : 0u), (W & 1) ? c.r1 : c.r3, (W - 17) / 2 * 2048);
}
DI unsigned slow_col(const void* base, int first, int n) {
  const unsigned k = (3 - (unsigned)((uintptr_t)base >> 7) - first) & 7;
  return k < n ? (first + k) * 128 : 0x40000000u;
}
DI void warm_code(Ctx& c) {
  unsigned o;
  asm volatile("s_mov_b32 %0, .-.Lblk_begin" : "=s"(o));
  touch(c, c.vC, c.rC, o);
}
template <int KS, int ST> DI void e_ck(Ctx& c) {
  if constexpr (ST % 3 < 2) asm volatile("v_accvgpr_read_b32 %0, a%c1" : "=v"(c.cw[ST % 3]) : "i"(8 * KS + 4 * (ST / 3) + 2 * (ST % 3) + 1));
  else if constexpr ((KS & 3) || ST == 5) asm volatile("v_max3_u32 %0, %0, %1, %2" : "+v"(c.ck) : "v"(c.cw[0]), "v"(c.cw[1]));
  else asm volatile("v_max_u32 %0, %1, %2" : "=v"(c.ck) : "v"(c.cw[0]), "v"(c.cw[1]));
}
template <int G, int... Is> DI void ck_group(Ctx& c, iseq<Is...>) { (e_ck<4 * G + Is / 6, Is % 6>(c), ...); }
template <int G, int... Is> DI void ld_group(Ctx& c, iseq<Is...>) { (ld_act<8 * G + Is>(c.vK[Is >> 1], c.rs, c.sA0 + G * 4 * KBS), ...); }
template <int G> DI void ck_retry(Ctx& c) {
  if (__builtin_expect(__any(c.ck == POISON), 0)) {
#pragma unroll 1
    for (unsigned spins = 0; spins < SPIN_CAP; ++spins) {
      ld_group<G>(c, make_iseq<8>{});
      wait_vm<0>();
      ck_group<G>(c, make_iseq<24>{});
      if (!__any(c.ck == POISON)) break;
    }
  }
}
template <int A> DI void action(Ctx& c) {
  if constexpr (A >= A_CK) ck_retry<A - A_CK>(c);
  else
  if constexpr (A == A_FLAG) { c.flv += 1; set_flag(c, c.fl3, c.flv); }
  else if constexpr (A == A_FLAG4) set_flag(c, c.fl4, c.flv);
  else {
    constexpr unsigned need = A == A_CHKX ? 1 : A;
    pin(c.pollv);
    if (__builtin_expect(!__all(c.pollv >= need), 0)) spin(c, c.pollo + (A == A_CHKX ? (c.fl4 ^ 128) : 0), need);
    if constexpr (A == A_CHK4) { c.pollo = 0; pin(c.pollo); }
  }
}
constexpr int rd_before(int q) { int n = 0; for (int i = q & ~3; i < q; ++i) n += PL.nr[i]; return n; }
template <int H, int YOUNG> DI void wait_for(Ctx& c) {
  constexpr int v = PL.vw[H] < 0 ? 63 : PL.vw[H];
  __builtin_amdgcn_s_waitcnt((v & 15) | ((v >> 4) << 14) | 0x70 | (YOUNG << 8));
  asm volatile("" ::: "memory");
}
template <int H> DI void waits(Ctx& c) {
  static_assert(!(H & 1) || (PL.vw[H] < 0 && !PL.act[H]), "waits sit in front of even half-steps");
  if constexpr (isbar(H)) bar();
  GAP();
  if constexpr (PL.act[H] != 0) { action<PL.act[H]>(c); GAP(); }
}
template <int H, int M> DI void slot(Ctx& c) {
  constexpr int cb = M >> 1, rb = M & 1, u = unit(H >> 3), j = grp(H >> 3), k = PL.op[H] / 1024, i = PL.op[H] % 1024, e = PL.e[4 * H + M];
  mfa<(j & 7) * 8 + (H & 6) + rb, u == U_V, j == 0 && (H & 6) == 0>(c.acc[u][H & 1][rb][cb], cat(c.wl[H & 7][cb], c.wh[H & 7][cb]));
  if constexpr (pg_of(4 * H + M) >= 0) pg_mark<pg_of(4 * H + M)>();
  GAP();
  constexpr int q = 4 * H + M, sk = PL.sc[q] / 1024, si = PL.sc[q] % 1024;
  if constexpr (sk == S_M0) m0_add<(si & 7) * SLOT>(c.mB);
  if constexpr (sk == S_SB) dma_off<si>(c);
  if constexpr (sk == S_SA) { c.sAA[si & 1] = c.sA0 + si * 4 * KBS; pins(c.sAA[si & 1]); }
  if constexpr (sk == S_SA4) { c.sAA[si & 1] = c.sA4[g4mix(si)] + g4kb(si) * KBS; pins(c.sAA[si & 1]); }
  if constexpr ((H & 1) && H + 1 < seg_end(H) && M == 3) wait_for<H + 1, (H + 5 <= seg_end(H) ? 4 + rd_before(q) : 0)>(c);
  if constexpr (M == 3 && H + 1 < seg_end(H)) waits<H + 1>(c);
  GAP();
  if constexpr (PL.nr[q] >= 1) rd<H + RD, rd_before(q)>(c);
  if constexpr (PL.nr[q] == 2) { GAP(); rd<H + RD, rd_before(q) + 1>(c); }
  GAP();
  if constexpr (M == 0) {
    if constexpr (k == K_D) dma<i>(c);
    if constexpr (k == K_T) warm<i>(c);
    if constexpr (k == K_C && i == 0) warm_code(c);
    if constexpr (k == K_C && i == 1) touch(c, c.vA << 20, c.rC, (((blockIdx.x >> 3) & 7) * 4 + c.wave) << 12);
    if constexpr (k == K_G && i < 8) asm volatile("buffer_load_dwordx4 %0, %1, %2, %3 offen nt" : "=v"(c.lw[i & 7]) : "v"(c.vG0), "s"(c.rM), "s"(i * 6144 * 2) : "memory");
    if constexpr (k == K_A || k == K_A4) ld_act<i & 63>(c.vK[(i >> 1) & 3], c.rs, c.sAA[(i >> 3) & 1]);
    if constexpr (k == K_P) asm volatile("buffer_load_dword %0, %1, %2, %3 offen sc1" : "+v"(c.pollv) : "v"(c.pollo), "s"(c.rs), "s"(i ? c.fl4 ^ 128 : 0) : "memory");
    if constexpr (k == K_Z) asm volatile("buffer_store_dwordx4 %0, %1, %2, %3 offen offset:%4 sc1\n\ts_nop 1" ::"v"((u32x4){POISON, POISON, POISON, POISON}), "v"(c.vK[i >> 1]), "s"(c.rs), "s"(c.zo), "n"((i & 1) * 1024) : "memory");
    if constexpr (k == K_XS) asm volatile("buffer_store_dwordx4 %0, %1, %2, %3 offen offset:%4 sc1" ::"v"(c.acc[U_S][i >> 2][(i >> 1) & 1][i & 1]), "v"(c.vA), "s"(c.rs), "s"(c.xs4 + (i >> 2) * 4096), "n"((i & 3) * 1024) : "memory");
    if constexpr (k == K_XL) asm volatile("buffer_load_dwordx4 %0, %1, %2, %3 offen offset:%4 sc1" : "=v"(c.xl[i]) : "v"(c.vA), "s"(c.rs), "s"((c.xs4 ^ 32768) + (i >> 2) * 4096), "n"((i & 3) * 1024) : "memory");
  }
  GAP();
  if constexpr (e >= E_CK) { e_ck<(e - E_CK) / 8, (e - E_CK) % 8>(c); if constexpr (e - E_CK < 8 * 8) e_ck<(e - E_CK) / 8, (e - E_CK) % 8 + 1>(c); }
  else if constexpr (e >= 64 * P_FA) e_fin<e / 64 - P_FA, e % 64>(c);
  else if constexpr (e >= 64 * PR_HH) e_hh<pr_blk(e / 64), pr_up(e / 64), e % 64>(c);
  else if constexpr (e == 64 * P_PREV) e_prev(c);
  else if constexpr (e >= 0 && (e / 64 == P_K || e / 64 == P_V)) e_kv<e / 64 == P_V, e % 64>(c);
  else if constexpr (e >= 64 * P_UA) e_un<e - 64 * P_UA>(c);
  else if constexpr (e >= 0) e_gv<e / 64, e % 64>(c);
  GAP();
}
template <int H0, int... Hs> DI void hsteps_(Ctx& c, iseq<Hs...>) { ((slot<H0 + Hs, 0>(c), slot<H0 + Hs, 1>(c), slot<H0 + Hs, 2>(c), slot<H0 + Hs, 3>(c)), ...); }
template <int Ta, int Tb> DI void stages(Ctx& c) { hsteps_<8 * Ta>(c, make_iseq<8 * (Tb - Ta)>{}); }
template <int H0, int... Is> DI void rd0(Ctx& c, iseq<Is...>) { (rd<H0 + Is / 4, Is % 4>(c), ...); }
template <int... Is> DI void ld0(Ctx& c, iseq<Is...>) { (ld_act<Is>(c.vK[(Is >> 1) & 3], c.rs, c.sA0 + (Is >> 3) * 4 * KBS), ...); }
template <int S> DI void start(Ctx& c, unsigned base) {
  c.sA0 = base; pins(c.sA0);
  ld0(c, make_iseq<2 * seg_pre(S)>{});
  static_assert(seg_pre(S) % 4 == 0 && PL.op[8 * seg_t0(S)] / 1024 == K_A, "the stream opens with the next group");
  c.sAA[(seg_pre(S) / 4) & 1] = base + seg_pre(S) * KBS; pins(c.sAA[(seg_pre(S) / 4) & 1]);
  bar();
  rd0<8 * seg_t0(S)>(c, make_iseq<4 * RD>{});
  GAP();
  wait_for<8 * seg_t0(S), 8>(c);
  GAP();
}
template <int S> DI void start_nf(Ctx& c, unsigned base) {
  c.sA0 = base; pins(c.sA0);
  ld0(c, make_iseq<2 * seg_pre(S)>{});
  c.sAA[(seg_pre(S) / 4) & 1] = base + seg_pre(S) * KBS; pins(c.sAA[(seg_pre(S) / 4) & 1]);
  wait_vm<2 * seg_pre(S) - 2>();
  ck_group<0>(c, make_iseq<6>{});
  ck_retry<0>(c);
  GAP();
  wait_for<8 * seg_t0(S), 8>(c);
  GAP();
}
DI void lane_off(unsigned (&v)[4], int lane, int wave, int pitch, int col) {
  const int g = lane >> 4, q = (lane >> 2) & 3, p = lane & 3;
#pragma unroll
  for (int i = 0; i < 4; ++i) { v[i] = (32 * wave + 8 * i + 4 * (g >> 1) + q) * pitch + col + (g & 1) * 64 + (p ^ (g & 2)) * 16 - 1024 * i; pin(v[i]); }
}
DI void mod_load(Ctx& c) {
  asm volatile("buffer_load_dwordx4 %0, %8, %9, 0 offen sc1\n\tbuffer_load_dwordx4 %1, %8, %9, 0 offen offset:512 sc1\n\tbuffer_load_dwordx4 %2, %8, %9, 0 offen offset:1024 sc1\n\tbuffer_load_dwordx4 %3, %8, %9, 0 offen offset:1536 sc1\n\t"
               "buffer_load_dwordx4 %4, %8, %9, %10 offen sc1\n\tbuffer_load_dwordx4 %5, %8, %9, %10 offen offset:512 sc1\n\tbuffer_load_dwordx4 %6, %8, %9, %10 offen offset:1024 sc1\n\tbuffer_load_dwordx4 %7, %8, %9, %10 offen offset:1536 sc1"
               : "=&v"(c.mp[0][0]), "=&v"(c.mp[0][1]), "=&v"(c.mp[0][2]), "=&v"(c.mp[0][3]), "=&v"(c.mp[1][0]), "=&v"(c.mp[1][1]), "=&v"(c.mp[1][2]), "=&v"(c.mp[1][3]) : "v"(c.mo), "s"(c.rs), "s"(65536) : "memory");
}
template <int N> DI void mod_wait(Ctx& c) {
  asm volatile("s_waitcnt vmcnt(%8)" : "+v"(c.mp[0][0]), "+v"(c.mp[0][1]), "+v"(c.mp[0][2]), "+v"(c.mp[0][3]), "+v"(c.mp[1][0]), "+v"(c.mp[1][1]), "+v"(c.mp[1][2]), "+v"(c.mp[1][3]) : "n"(N) : "memory");
}
DI void mod_vectors(Ctx& c, int lane) {
  const int g = lane >> 4;
  if (__builtin_expect(!(tagged(c, c.mp[0]) && tagged(c, c.mp[1])), 0)) {
#pragma unroll 1
    for (unsigned spins = 0; spins < SPIN_CAP; ++spins) { mod_load(c); mod_wait<0>(c); if (tagged(c, c.mp[0]) && tagged(c, c.mp[1])) break; }
  }
  dmas<2, 2, 4>(c);
  f32x4 a;
#pragma unroll
  for (int e = 0; e < 4; ++e) { const u32x4 (&m)[4] = c.mp[e >> 1]; a[e] = (__uint_as_float(m[0][2 * (e & 1)]) + __uint_as_float(m[1][2 * (e & 1)])) + (__uint_as_float(m[2][2 * (e & 1)]) + __uint_as_float(m[3][2 * (e & 1)])); }
  const unsigned pb = c.lds0 + L_WV + c.wave * 1024;
  lds_st(pb + lane * 4, pk2(a[0], a[1])); lds_st(pb + 256 + lane * 4, pk2(a[2], a[3]));
#pragma unroll
  for (int i = 0; i < 12; ++i) c.mv[i] = lds_ld<u32x2>(pb + i * 32 + g * 8);
}
}

DI u32x4 gated_resid(const f32x4& a0, const f32x4& a1, const f32x2 (&eg)[4], const f32x2 (&er)[4]) {
  u32x4 o;
#pragma unroll
  for (int j = 0; j < 4; ++j) {
    const f32x4& v = j < 2 ? a0 : a1;
    const f32x2 r = (f32x2){v[2 * (j & 1)], v[2 * (j & 1) + 1]} * eg[j] + er[j];
    o[j] = pk2(r[0], r[1]);
  }
  return o;
}

DI void phase1(Ctx& c) {
  using namespace en;
  const int tid = opaque_tid(), lane = tid & 63, b = opaque_bid(), wave = c.wave;
  const int fr = lane & 15, g = lane >> 4, q = (lane >> 2) & 3, p = lane & 3;
  c.tch = 0;
  asm volatile("global_load_dword %0, %1, %2" : "+v"(c.tch) : "v"((b >> 3) * 256 + (tid & 1) * 128), "s"(c.pc0) : "memory");
  GAP();
  int tm, tn; map_g1(b, tm, tn);
  int tm2, tn2; map_g2(b, tm2, tn2);
  u32x4 gw[4], xs[4], lc; u32x2 gc, ep;
  c.rM = mk_rsrc(c.wmod, D_ * 6 * D_ * 2);
  {
    const int k0 = (b & 7) * 128 + (tid >> 3) * 4;
    asm volatile("global_load_dwordx4 %0, %1, %2" : "=v"(lc) : "v"((tid >> 3) * 16), "s"(c.cc + (b & 3) * 256) : "memory");
    asm volatile("global_load_dwordx2 %4, %10, %11\n\tbuffer_load_dwordx4 %0, %5, %6, 0 offen nt\n\tbuffer_load_dwordx4 %1, %5, %6, %7 offen nt\n\tbuffer_load_dwordx4 %2, %5, %6, %8 offen nt\n\tbuffer_load_dwordx4 %3, %5, %6, %9 offen nt"
                 : "=&v"(gw[0]), "=&v"(gw[1]), "=&v"(gw[2]), "=&v"(gw[3]), "=&v"(gc)
                 : "v"((k0 * 6144 + (b >> 3) * 64 + (tid & 7) * 8) * 2), "s"(c.rM), "s"(12288), "s"(24576), "s"(36864), "v"(k0 * 2), "s"(c.cc) : "memory");
    asm volatile("global_load_dwordx2 %0, %1, %2 sc1" : "=v"(ep) : "v"(0), "s"(c.ws) : "memory");
    asm volatile("global_load_dwordx4 %0, %4, %5\n\tglobal_load_dwordx4 %1, %4, %5 offset:512\n\tglobal_load_dwordx4 %2, %4, %5 offset:1024\n\tglobal_load_dwordx4 %3, %4, %5 offset:1536"
                 : "=&v"(xs[0]), "=&v"(xs[1]), "=&v"(xs[2]), "=&v"(xs[3]) : "v"((tid >> 5) * 2048 + (tid & 31) * 16), "s"(c.x + (tm2 * 128 + tn2 * 8) * D_) : "memory");
  }
  GAP();
  c.rs = mk_rsrc(c.ws, W_END); c.rQ = mk_rsrc(c.wqkv, D_ * 3 * D_ * 2); c.rO = mk_rsrc(c.wo, D_ * D_ * 2);
  c.r1 = mk_rsrc(c.w1, D_ * F_ * 2); c.r3 = mk_rsrc(c.w3, D_ * F_ * 2); c.r2 = mk_rsrc(c.w2, D_ * F_ * 2);
  { unsigned n; asm volatile("s_mov_b32 %0, .Lblk_end-.Lblk_begin" : "=s"(n)); c.rC = mk_rsrc((const void*)c.pc0, n); }
  c.mB = c.lds0 + wave * 4096;
  pins(c.rs); pins(c.rQ); pins(c.rO); pins(c.rM); pins(c.r1); pins(c.r3); pins(c.r2); pins(c.rC); pins(c.mB);
  const int j8 = ((b >> 3) * 4 + wave) * 8;
  const unsigned slow1 = (j8 + (lane & 7)) * 6144 + slow_col(c.wqkv, (b & 3) * 4, 4), row1 = (b * 4 + wave) * 6144 + fr * 128;
  c.vT1 = sel(lane < 8, slow1, OOB); pin(c.vT1);
  c.vC = sel(lane == 2, (b * 4 + wave) * 128, j8 * 32 + (lane & 1) * 128);
  FST();
  warm_code(c);
  unsigned tk = sel(lane < 16, row1, sel(lane < 24, slow1, OOB)), tvq = sel(lane < 32, row1 + (lane >> 4) * 4096, sel(lane < 40, slow1 + 4096, OOB));
  unsigned i0 = W_HALO + (((tm2 - 1) & 15) * 16 + tn2) * 4096 + wave * 1024 + lane * 16, iz = lane < 16 ? POISON : 0u;
  unsigned i1 = sel(lane < 16, W_STATS + (tm2 * 4 + wave) * 4096 + tn2 * 256 + lane * 16, sel(lane == 16, flag_off(tm2, wave, tn2, PH_G1), OOB));
  unsigned xo = (fr * D_ + (g & 1) * 16 + (g >> 1) * 8) * 2, xo1 = xo + 16 * D_ * 2;
  unsigned o0 = W_PART + ((wave >> 1) * 16 + tn2) * 4096 + (wave & 1) * 2048 + lane * 16, o1 = W_LN1 + (tm2 * 128 + wave * 32 + fr) * 16;
  unsigned fl1 = sel(lane < 16, flag_off(tm, wave, fr, 0), flag_off(min(tm + 1, 15), wave, tn, 0));
  pin(tk); pin(tvq); pin(i0); pin(i1); pin(iz); pin(xo); pin(xo1); pin(o0); pin(o1); pin(fl1);
  lane_off(c.v1, lane, wave, 6144, tn * 128);
  c.vA = lane * 16; pin(c.vA); c.vK[0] = c.vA;
#pragma unroll
  for (int i = 1; i < 4; ++i) { c.vK[i] = c.vA + i * KBS; pin(c.vK[i]); }
  c.vT2 = sel(lane < 16, (b * 4 + wave) * 2048 + lane * 128, sel(lane < 24, (j8 + lane - 16) * 2048 + slow_col(c.wo, (b & 2) * 4, 8), OOB));
  c.vT3 = sel(lane < 16, (b * 4 + wave) * (F_ * 2) + lane * 128, sel(lane < 24, (j8 + lane - 16) * (F_ * 2) + slow_col(c.w1, (b & 3) * 4, 4), OOB));
  c.vG0 = (((b & 3) * 256 + (tid >> 3) * 8) * 6144 + 2048 + (b >> 2) * 64 + (tid & 7) * 8) * 2;
  pin(c.vG0);
  pin(c.vT2); pin(c.vT3);
#pragma unroll
  for (int w = 0; w < 2; ++w) {
    const unsigned bw = c.lds0 + w * 65536 + g * 512 + q * 64 + p * 8;
    c.bW[0][w] = bw + (g & 1) * 32; c.bW[1][w] = bw + ((g & 1) ^ 1) * 32;
    pin(c.bW[0][w]); pin(c.bW[1][w]);
  }
  lane_off(c.v2, lane, wave, 2048, 0);
  { int tm3, tn3; map_g3(b, tm3, tn3); lane_off(c.v3, lane, wave, F_ * 2, tn3 * 128); }
  c.sB4[0][0] = tn2 * 128; c.zo = W_H2 + 2 * tn2 * KBS + (tm2 * 8 + 2 * wave) * 1024; pins(c.zo);
  c.mo = W_LATE + ((lane >> 5) * 16 + tn2) * 2048 + (lane & 31) * 16; pin(c.mo);
  c.kvw = c.lds0 + L_KV + wave * 4096 + lane * 16; pin(c.kvw);
  c.hso = W_HALO + ((tm - 1) * 16 + tn) * 4096;
  c.hst = wave == 3 ? c.hso + 65536 : W_END; pins(c.hst);
  unsigned e_sel[4]; int e_base; f32x2 e_bias;
  {
    e_base = (fr & ~1) - 4 * g;
    const bool first = tm == 0 && wave == 0;
    e_bias = (f32x2){first && e_base < 0 ? -3.0e38f : 0.f, first && e_base < 2 ? -3.0e38f : 0.f};
    const unsigned s0 = (fr & 1) ? 0x01000c0cu : 0x0c0c0100u, s1 = (fr & 1) ? 0x03020c0cu : 0x0c0c0302u, z = 0x0c0c0c0cu;
    e_sel[0] = e_base >= 0 ? z : s0; e_sel[1] = e_base >= 2 ? z : s1; e_sel[2] = e_base >= 0 ? s0 : z; e_sel[3] = e_base >= 2 ? s1 : z;
    pin(e_base); pin(e_bias); pin(e_sel[0]); pin(e_sel[1]); pin(e_sel[2]); pin(e_sel[3]);
  }
  GAP();
  float sv[4];
  asm volatile("s_waitcnt vmcnt(10)" : "+v"(gc), "+v"(lc)::"memory");
#pragma unroll
  for (int i = 0; i < 4; ++i) { sv[i] = silu_bf((u16)(gc[i >> 1] >> (16 * (i & 1)))); pin(sv[i]); }
#pragma unroll
  for (int i = 0; i < 8; ++i) { c.ls[i] = silu_bf((u16)(lc[i >> 1] >> (16 * (i & 1)))); pin(c.ls[i]); }
  GAP();
  FST();
  asm volatile("s_waitcnt vmcnt(%5)" : "+v"(gw[0]), "+v"(gw[1]), "+v"(gw[2]), "+v"(gw[3]), "+v"(ep) : "n"(5) : "memory");
  c.tag[0] = __builtin_amdgcn_readfirstlane(ep[0]) + 1; c.tag[1] = __builtin_amdgcn_readfirstlane(ep[1]) + 1;
  touch(c, tk, c.rQ, 2048);
  GAP();
  FST();
  {
    f32x2 acc[4];
#pragma unroll
    for (int j = 0; j < 4; ++j) acc[j] = (f32x2){0.f, 0.f};
#pragma unroll
    for (int i = 0; i < 4; ++i) {
#pragma unroll
      for (int j = 0; j < 4; ++j) acc[j] += unpk(gw[i][j]) * sv[i];
    }
    lds_st(c.lds0 + L_RED + wave * 256 + (lane & 7) * 32 + g * 8, red8(acc));
    wait_lgkm<0>(); bar();
    if (wave == 3) gemv_out(c, W_PART + b * 512, lane);
    GAP();
    asm volatile("global_load_dwordx4 %0, %4, %6\n\tglobal_load_dwordx4 %1, %4, %6 offset:64\n\tglobal_load_dwordx4 %2, %5, %6\n\tglobal_load_dwordx4 %3, %5, %6 offset:64"
                 : "=&v"(c.xr[0][0]), "=&v"(c.xr[0][1]), "=&v"(c.xr[1][0]), "=&v"(c.xr[1][1]) : "v"(xo), "v"(xo1), "s"(c.x + (tm2 * 128 + wave * 32) * D_ + tn2 * 64) : "memory");
    asm volatile("s_waitcnt vmcnt(%4)" : "+v"(xs[0]), "+v"(xs[1]), "+v"(xs[2]), "+v"(xs[3]) : "n"(6) : "memory");
    f32x2 f[16], s2 = {0.f, 0.f};
#pragma unroll
    for (int i = 0; i < 16; ++i) { f[i] = unpk(xs[i >> 2][i & 3]); s2 += f[i]; }
    const float mean = xadd16(row16_sum(s2[0] + s2[1])) * (1.f / 1024.f);
    f32x2 q2 = {0.f, 0.f};
#pragma unroll
    for (int i = 0; i < 16; ++i) { const f32x2 d = f[i] - mean; q2 += d * d; }
    const float rstd = ln_rstd(xadd16(row16_sum(q2[0] + q2[1])) * (1.f / 1024.f));
    if (!(lane & 31)) st16(c, W_LN1 + (tm2 * 128 + tn2 * 8 + (tid >> 5)) * 16, granule(c, rbf(mean), rstd));
  }
  st16(c, i0, (u32x4){POISON, POISON, POISON, POISON}); st16(c, i1, (u32x4){iz, iz, iz, iz});
  GAP();
  FST();
  bar();
  dmas<1, 0, NFD>(c);
  FST();
  f32x4 ln;
  {
    u32x4 v[4];
#pragma unroll 1
    for (unsigned spins = 0; spins < SPIN_CAP; ++spins) {
      asm volatile("buffer_load_dwordx4 %0, %4, %6, 0 offen sc1\n\tbuffer_load_dwordx4 %1, %4, %6, 0 offen offset:1024 sc1\n\tbuffer_load_dwordx4 %2, %5, %6, 0 offen sc1\n\tbuffer_load_dwordx4 %3, %5, %6, 0 offen offset:256 sc1\n\ts_waitcnt vmcnt(0)"
                   : "=&v"(v[0]), "=&v"(v[1]), "=&v"(v[2]), "=&v"(v[3]) : "v"(o0), "v"(o1), "s"(c.rs) : "memory");
      if (tagged(c, v)) break;
      __builtin_amdgcn_s_sleep(1);
    }
    pin(c.xr[0][0]); pin(c.xr[0][1]); pin(c.xr[1][0]); pin(c.xr[1][1]);
    FST();
    ln = (f32x4){__uint_as_float(v[2][0]), __uint_as_float(v[2][2]), __uint_as_float(v[3][0]), __uint_as_float(v[3][2])};
    lds_st(c.lds0 + L_RED + wave * 256 + (lane & 31) * 8, (f32x2){xadd32(__uint_as_float(v[0][0]) + __uint_as_float(v[1][0])), xadd32(__uint_as_float(v[0][2]) + __uint_as_float(v[1][2]))});
    wait_lgkm<0>(); bar();
    touch(c, tvq, c.rQ, 0);
  }
  FST();
  {
    const f32x4 r = lds_ld<f32x4>(c.lds0 + L_RED + (g & 1) * 512 + fr * 16) + lds_ld<f32x4>(c.lds0 + L_RED + (g & 1) * 512 + 256 + fr * 16);
    const f32x2 one = (g & 1) ? (f32x2){1.f, 1.f} : (f32x2){0.f, 0.f};
    const f32x2 m0 = rbf2(rbf2((f32x2){r[0], r[1]}) + one), m1 = rbf2(rbf2((f32x2){r[2], r[3]}) + one);
    const unsigned Mt = c.lds0 + L_WV + wave * 1024, off = W_H1 + 2 * tn2 * KBS + (tm2 * 8 + 2 * wave) * 1024 + lane * 16;
    lds_st(Mt + (lane & 31) * 16, (f32x4){m0[0], m0[1], m1[0], m1[1]});
#pragma unroll
    for (int s = 0; s < 2; ++s) {
      const unsigned a = Mt + s * 128 + g * 16;
      const f32x4 sh0 = lds_ld<f32x4>(a), sh1 = lds_ld<f32x4>(a + 64), sc0 = lds_ld<f32x4>(a + 256), sc1 = lds_ld<f32x4>(a + 320);
      const f32x2 sc[4] = {{sc0[0], sc0[1]}, {sc0[2], sc0[3]}, {sc1[0], sc1[1]}, {sc1[2], sc1[3]}}, sh[4] = {{sh0[0], sh0[1]}, {sh0[2], sh0[3]}, {sh1[0], sh1[1]}, {sh1[2], sh1[3]}};
#pragma unroll
      for (int i = 0; i < 2; ++i) {
        c.xr[i][s] = swz_rows(c.xr[i][s]);
        u32x4 o;
#pragma unroll
        for (int j = 0; j < 4; ++j) {
          const f32x2 y = rbf2(rbf2(rbf2(unpk(c.xr[i][s][j]) - ln[2 * i]) * ln[2 * i + 1]) * sc[j]) + sh[j];
          o[j] = pk2(y[0], y[1]);
        }
        st16(c, off + s * KBS + i * 1024, o);
      }
    }
  }
  GAP();
  STAMP();
  dmas<1, NFD, NFD + NF2>(c);
  wait_vm<NF2>();
  asm volatile("s_mov_b64 exec, 1\n\tbuffer_store_dwordx2 %0, off, %1, %2 sc1\n\ts_mov_b64 exec, -1" ::"v"((u32x2){c.tag[0], c.tag[1]}), "s"(c.rs), "s"(flag_off(tm2, wave, tn2, 0)) : "memory");
  STAMP();
  bar();
  FST();
  dmas<1, NFD + NF2, 64>(c);
  FST();
  pg_hop<0>(wave);
  await<PH_LN1>(c, fl1);
  if (b + wave == 0) asm volatile("s_mov_b64 exec, 1\n\tbuffer_store_dwordx2 %0, off, %1, 0 sc1\n\ts_mov_b64 exec, -1" ::"v"((u32x2){c.tag[0], c.tag[1]}), "s"(c.rs) : "memory");
  STAMP();
  start<1>(c, W_H1 + (tm * 8 + 2 * wave) * 1024);
  STAMP();
  stages<0, 8>(c);
  STAMP();
  stages<8, 16>(c);
  STAMP();
  stages<16, 24>(c);
  mfma_fence();
  STAMP();
  {
    u32x4 qf[2][2];
#pragma unroll
    for (int i = 0; i < 4; ++i) qf[i >> 1][i & 1] = pack8(c.acc[U_Q][i >> 1][i & 1][0], c.acc[U_Q][i >> 1][i & 1][1]);
    asm volatile("s_waitcnt vmcnt(%4) lgkmcnt(0)" : "+v"(c.hl[0]), "+v"(c.hl[1]), "+v"(c.hl[2]), "+v"(c.hl[3]) : "n"(PL.halo) : "memory");
    if (wave == 0) retry(c, c.hl, c.vA + c.hso, 1024);
    u32x4 vo[2][2][2];
#pragma unroll
    for (int h = 0; h < 2; ++h)
#pragma unroll
      for (int j = 0; j < 2; ++j) {
        vo[h][0][j] = (u32x4){c.hl[2 + h][2 * j], c.hl[2 + h][2 * j + 1], c.vf[h][0][2 * j], c.vf[h][0][2 * j + 1]};
        vo[h][1][j] = (u32x4){c.vf[h][0][2 * j], c.vf[h][0][2 * j + 1], c.vf[h][1][2 * j], c.vf[h][1][2 * j + 1]};
      }
#pragma unroll
    for (int i = 0; i < 4; ++i) { pin(qf[i >> 1][i & 1]); pin(c.hl[i]); }
    GAP(); nops<2>();
    f32x4 sp[2][2], sc[2][2];
#pragma unroll
    for (int h = 0; h < 2; ++h) { mfv(sp[h][0], c.hl[h], qf[h][0]); mfv(sc[h][0], c.kf[h][0], qf[h][0]); mfv(sp[h][1], c.kf[h][0], qf[h][1]); mfv(sc[h][1], c.kf[h][1], qf[h][1]); }
    GAP();
#pragma unroll
    for (int i = 0; i < 8; ++i) pin(vo[i >> 2][(i >> 1) & 1][i & 1]);
    mfma_fence();
    const bool par = fr & 1, cur0 = e_base >= 0, cur1 = e_base >= 2;
    auto red2 = [](float& u, float& v, auto op) {
      auto r = __builtin_amdgcn_permlane16_swap(__float_as_uint(u), __float_as_uint(v), false, false);
      const float m = op(__uint_as_float(r[0]), __uint_as_float(r[1]));
      auto r2 = __builtin_amdgcn_permlane32_swap(__float_as_uint(m), __float_as_uint(m), false, false);
      const float t = op(__uint_as_float(r2[0]), __uint_as_float(r2[1]));
      auto r3 = __builtin_amdgcn_permlane16_swap(__float_as_uint(t), __float_as_uint(t), false, false);
      u = __uint_as_float(r3[0]); v = __uint_as_float(r3[1]);
    };
    f32x2 l[2][2]; float mx[2][2], sm[2][2]; u32x4 pw[2][2];
#pragma unroll
    for (int h = 0; h < 2; ++h)
#pragma unroll
      for (int i = 0; i < 2; ++i) {
        const float a0 = sp[h][i][0], a1 = sp[h][i][1], a2 = sp[h][i][2], a3 = sp[h][i][3], b0 = sc[h][i][0], b1 = sc[h][i][1], b2 = sc[h][i][2], b3 = sc[h][i][3];
        const float x0 = cur0 ? b0 : a0, x1 = cur0 ? b1 : a1, x2 = cur1 ? b2 : a2, x3 = cur1 ? b3 : a3;
        const float y0 = par ? x1 : x0, y1 = par ? x3 : x2;
        l[h][i] = rbf2(rbf2((f32x2){y0, y1}) * 0.17677669529663687f);
        if (i == 0) l[h][i] += e_bias;
        mx[h][i] = fmax1(l[h][i][0], l[h][i][1]);
      }
#pragma unroll
    for (int h = 0; h < 2; ++h) red2(mx[h][0], mx[h][1], [](float x, float y) { return fmax1(x, y); });
#pragma unroll
    for (int h = 0; h < 2; ++h)
#pragma unroll
      for (int i = 0; i < 2; ++i) {
        const f32x2 t = (l[h][i] - mx[h][i]) * 1.4426950408889634f;
        l[h][i] = (f32x2){__builtin_amdgcn_exp2f(t[0]), __builtin_amdgcn_exp2f(t[1])};
        sm[h][i] = l[h][i][0] + l[h][i][1];
      }
#pragma unroll
    for (int h = 0; h < 2; ++h) red2(sm[h][0], sm[h][1], [](float x, float y) { return x + y; });
#pragma unroll
    for (int h = 0; h < 2; ++h)
#pragma unroll
      for (int i = 0; i < 2; ++i) {
        const f32x2 pr = l[h][i] * __builtin_amdgcn_rcpf(sm[h][i]);
        const unsigned pp = pk2(pr[0], pr[1]);
        pw[h][i] = (u32x4){__builtin_amdgcn_perm(pp, pp, e_sel[0]), __builtin_amdgcn_perm(pp, pp, e_sel[1]), __builtin_amdgcn_perm(pp, pp, e_sel[2]), __builtin_amdgcn_perm(pp, pp, e_sel[3])};
        pin(pw[h][i]);
      }
    GAP(); nops<2>();
    f32x4 o[2][2][2];
#pragma unroll
    for (int i = 0; i < 8; ++i) mfv(o[i >> 2][(i >> 1) & 1][i & 1], vo[i >> 2][(i >> 1) & 1][i & 1], pw[i >> 2][(i >> 1) & 1]);
    mfma_fence();
    const unsigned oo = W_O + 2 * tn * KBS + (tm * 8 + 2 * wave) * 1024 + c.vA;
#pragma unroll
    for (int i = 0; i < 4; ++i) st16(c, oo + (i >> 1) * KBS + (i & 1) * 1024, pack8(o[i >> 1][i & 1][0], o[i >> 1][i & 1][1]));
  }
  GAP();
  mod_load(c);
  STAMP();
  wait_vm<8>();
  set_flag(c, flag_off(tm, wave, tn, PH_G1), 1);
  STAMP();
}

DI void setup3(Ctx& c) {
  using namespace en;
  const int lane = opaque_tid() & 63, b = opaque_bid(), wave = c.wave;
  int tm, tn, tm4, tn4; map_g3(b, tm, tn); map_g2(b, tm4, tn4);
  {
    const int g = lane >> 4, q = (lane >> 2) & 3, p = lane & 3;
#pragma unroll
    for (int i = 0; i < 4; ++i) {
      c.bM[i >> 1][i & 1] = c.bW[i >> 1][i & 1] - g * 256; pin(c.bM[i >> 1][i & 1]);
      c.vM[i] = (32 * wave + 16 * (i >> 1) + 4 * g + q) * (F_ * 2) + (96 + tn) * 64 + (p ^ (2 * (g & 1))) * 16 - 1024 * i; pin(c.vM[i]);
    }
    const int j8 = ((b >> 3) * 4 + wave) * 8;
    c.vT4 = sel(lane < 16, (j8 + (lane >> 1)) * (F_ * 2) + 6144 + (b & 3) * 256 + (lane & 1) * 128, OOB);
    c.vT5 = sel(lane < 16, c.vT2, OOB);
    c.vT2 = sel((lane >> 3) == 2, (j8 + lane - 16) * 2048 + slow_col(c.w2, (b & 2) * 4, 8), OOB);
    pin(c.vT2); pin(c.vT4); pin(c.vT5);
    const int kh = tn4 & 1;
#pragma unroll
    for (int i = 0; i < 4; ++i) { c.sB4[i >> 1][i & 1] = (tn4 ^ 1 ^ (i & 1)) * 128 + kh * (16 >> (i >> 1)) * 32 * 2048; pins(c.sB4[i >> 1][i & 1]); }
#pragma unroll
    for (int i = 0; i < 2; ++i) { c.sA4[i] = W_HH + kh * (16 >> i) * KBS + (tm4 * 8 + 2 * wave) * 1024; pins(c.sA4[i]); }
    c.so[1] = W_HH + tn * KBS + (tm * 8 + 2 * wave) * 1024; c.so[0] = c.so[1] + tn * KBS;
    c.fl3 = flag_off(tm, wave, tn, PH_G3); c.fl4 = flag_off(tm4, wave, tn4, PH_G4);
    c.xs4 = W_XCH + (tm4 * 16 + tn4) * 32768 + wave * 8192;
    c.pollo = flag_off(tm4, wave, kh * 8 + (lane & 7), PH_G3); c.flv = 0; c.pollv = 0;
    pin(c.pollo); pin(c.flv); pins(c.so[0]); pins(c.so[1]); pins(c.fl3); pins(c.fl4); pins(c.xs4);
  }
}
DI void phase2(Ctx& c) {
  using namespace en;
  const int tid = opaque_tid(), lane = tid & 63, b = opaque_bid(), wave = c.wave;
  const int fr = lane & 15, g = lane >> 4;
  int tm, tn; map_g2(b, tm, tn);
  bar();
  dmas<2, 0, 2>(c);
  mod_wait<3>(c);
  mod_vectors(c, lane);
  dmas<2, 4, 64>(c);
  STAMP();
  await<PH_G1>(c, flag_off(tm, wave, fr, 0));
  STAMP();
  start<2>(c, W_O + (tm * 8 + 2 * wave) * 1024);
  STAMP();
  stages<24, 32>(c);
  mfma_fence();
  STAMP();
  f32x2 d[2][8]; float s1[2];

#pragma unroll
  for (int i = 0; i < 2; ++i) {
#pragma unroll
    for (int s = 0; s < 2; ++s) {
      dma_at<3, 0, NFREE3>(c, 4 * i + 2 * s);
      c.xr[i][s] = gated_resid(c.acc[U_O][s][i][0], c.acc[U_O][s][i][1], c.eg[s], c.er[i][s]);
      dma_at<3, 0, NFREE3>(c, 4 * i + 2 * s + 1);
#pragma unroll
      for (int j = 0; j < 4; ++j) d[i][4 * s + j] = unpk(c.xr[i][s][j]);
    }
    const f32x2 s2 = ((d[i][0] + d[i][1]) + (d[i][2] + d[i][3])) + ((d[i][4] + d[i][5]) + (d[i][6] + d[i][7]));
    s1[i] = s2[0] + s2[1];
  }
  const f32x2 mu = spread2(add2(s1[0], s1[1]) * (1.f / 64.f));
#pragma unroll
  for (int i = 0; i < 2; ++i) {
    f32x2 q2 = {0.f, 0.f};
#pragma unroll
    for (int j = 0; j < 8; ++j) { if (!(j & 3)) dma_at<3, 8, NFREE3>(c, 2 * i + (j >> 2)); d[i][j] -= mu[i]; q2 += d[i][j] * d[i][j]; }
    s1[i] = q2[0] + q2[1];
  }
  const f32x2 m2 = spread2(add2(s1[0], s1[1]));
  const unsigned sb = W_STATS + (tm * 4 + wave) * 4096;
  if (g == 0) st16(c, sb + tn * 256 + fr * 16, (u32x4){min(__float_as_uint(mu[0]), POISON - 1), min(__float_as_uint(m2[0]), POISON - 1), min(__float_as_uint(mu[1]), POISON - 1), min(__float_as_uint(m2[1]), POISON - 1)});
  GAP();
  dmas<3, 12, (NFREE3 < 16 ? NFREE3 : 16)>(c);
  STAMP();
  u32x4 pv[4];
  pg_hop<1>(wave);
  ld16x4(c, pv, sb + c.vA, 1024);
  retry(c, pv, sb + c.vA, 1024);
  STAMP();
  {
    f32x2 um[4], s2 = {0.f, 0.f}, q2 = {0.f, 0.f};
#pragma unroll
    for (int t = 0; t < 4; ++t) { um[t] = (f32x2){__uint_as_float(pv[t][0]), __uint_as_float(pv[t][2])}; s2 += um[t]; q2 += (f32x2){__uint_as_float(pv[t][1]), __uint_as_float(pv[t][3])}; }
    const float mg = add2(s2[0], s2[1]) * (1.f / 16.f);
    const f32x2 mean = spread2(mg);
    f32x2 e2 = {0.f, 0.f};
#pragma unroll
    for (int t = 0; t < 4; ++t) { const f32x2 dd = um[t] - mean; e2 += dd * dd; }
    q2 += e2 * 64.f;
    const f32x2 rs = spread2(ln_rstd(add2(q2[0], q2[1]) * (1.f / 1024.f))), mb = rbf2(mean);
    const unsigned ob = W_H2 + 2 * tn * KBS + (tm * 8 + 2 * wave) * 1024 + c.vA;
#pragma unroll
    for (int i = 0; i < 2; ++i) {
      const float dm = (mu[i] - mb[i]) * rs[i];
#pragma unroll
      for (int s = 0; s < 2; ++s) {
        u32x4 o;
#pragma unroll
        for (int j = 0; j < 4; ++j) {
          const f32x2 y = (d[i][4 * s + j] * rs[i] + dm) * c.sc1[4 * s + j] + c.sh[4 * s + j];
          o[j] = pk2(y[0], y[1]);
        }
        o[1] = min(o[1], POISON - 1); o[3] = min(o[3], POISON - 1);
        st16(c, ob + s * KBS + i * 1024, o);
      }
    }
  }
  GAP();
  STAMP();
  STAMP();
}

DI void phase3(Ctx& c) {
  using namespace en;
  int tid = opaque_tid(), lane = tid & 63; const int b = opaque_bid(), wave = c.wave;
  int tm, tn, tm4, tn4; map_g3(b, tm, tn); map_g2(b, tm4, tn4);
  static_assert(PL.d1[3] - PL.d0[3] <= NFREE3 && NFREE3 <= 16, "every piece fetched so far is older than the poll of the statistics");
  bar();
  rd0<8 * T3>(c, make_iseq<4 * RD>{});
  setup3(c);
  STAMP(); STAMP();
  start_nf<3>(c, W_H2 + (tm * 8 + 2 * wave) * 1024);
  STAMP();
  stages<32, 40>(c);
  STAMP();
  stages<40, 48>(c);
  STAMP();
  stages<48, 56>(c);
  STAMP();
  stages<56, 64>(c);
  STAMP();
  stages<64, 72>(c);
  STAMP();
  stages<72, 80>(c);
  STAMP();
  stages<80, 88>(c);
  STAMP();
  stages<88, 96>(c);
  STAMP();
  stages<96, 104>(c);
  STAMP();
  stages<104, 110>(c);
  STAMP();
  stages<110, NT - 1>(c);
  tid = opaque_tid(); lane = tid & 63;
  const int g = lane >> 4;
  unsigned oo = ((tm4 * 128 + wave * 32 + (lane & 7)) * D_ + tn4 * 64 + (lane & 8) * 4 + (g & 1) * 16 + (g >> 1) * 8) * 2;
  pin(oo);
  GAP();
  stages<NT - 1, NT>(c);
  mfma_fence();
  STAMP();
  pin(c.tch);
  wait_vm<0>();
  fin_(c, make_iseq<32>{});
  const rsrc_t ro = mk_rsrc(c.out, S_ * D_ * 2);
#pragma unroll
  for (int rb = 0; rb < 2; ++rb) {
    u32x4 o[2];
#pragma unroll
    for (int s = 0; s < 2; ++s)
      o[s] = swz_rows(gated_resid(c.acc[U_P][s][rb][0], c.acc[U_P][s][rb][1], c.eg[s], c.er[rb][s]));
    u32x4 w[2];
#pragma unroll
    for (int e = 0; e < 4; ++e) {
      w[0][e] = __builtin_amdgcn_update_dpp(o[0][e], o[1][e], 0x128, 0xf, 0xc, false);
      w[1][e] = __builtin_amdgcn_update_dpp(o[1][e], o[0][e], 0x128, 0xf, 0x3, false);
    }
#pragma unroll
    for (int s = 0; s < 2; ++s) __builtin_amdgcn_raw_buffer_store_b128(w[s], ro, oo + (rb * 16 + s * 8) * D_ * 2, 0, AUX_SC1);
  }
  STAMP();
}

__global__ __launch_bounds__(256, 1) __attribute__((aligned(4096))) void block_kernel(const u16* x, const u16* wqkv, const u16* wo, const u16* w1, const u16* w3, const u16* w2,
                                                       const u16* cc, const u16* wmod, u16* out, char* ws) {
  extern __shared__ __attribute__((aligned(16))) char smem[];
  asm volatile("" ::: ACLOB);
  Ctx c;
  c.x = x; c.wqkv = wqkv; c.wo = wo; c.w1 = w1; c.w3 = w3; c.w2 = w2; c.cc = cc; c.wmod = wmod; c.out = out; c.ws = ws;
  c.lds0 = (unsigned)(uintptr_t)(LDS_PTR(char))smem;
  asm volatile("s_getpc_b64 %0\n.Lblk_begin:" : "=s"(c.pc0));
  c.wave = __builtin_amdgcn_readfirstlane(threadIdx.x >> 6);
  STAMP0();
  phase1(c);
  phase2(c);
  phase3(c);
  asm volatile(".Lblk_end:");
}

extern "C" void kernel_launch(void* const* d_in, const int* in_sizes, int n_in, void* d_out, int out_size, void* d_ws,
                              size_t ws_size, hipStream_t stream) {
  (void)hipFuncSetAttribute((const void*)block_kernel, hipFuncAttributeMaxDynamicSharedMemorySize, LDS_TOTAL);
  block_kernel<<<256, 256, LDS_TOTAL, stream>>>((const u16*)d_in[0], (const u16*)d_in[1], (const u16*)d_in[2], (const u16*)d_in[4],
                                                (const u16*)d_in[5], (const u16*)d_in[6], (const u16*)d_in[7], (const u16*)d_in[8], (u16*)d_out, (char*)d_ws);
}
```
